```python
import torch

def _softmax(x, axis=-1):
    return torch.softmax(x, dim=axis)
def _gelu(x):
    return torch.nn.functional.gelu(x, approximate="tanh")
def _silu(x):
    return torch.nn.functional.silu(x)
def _sigmoid(x):
    return torch.sigmoid(x)
def _layernorm(x, axis=-1, eps=1e-5):
    m = x.mean(dim=axis, keepdim=True)
    v = x.var(dim=axis, keepdim=True, unbiased=False)
    return (x - m) / torch.sqrt(v + eps)
def _scatter_add(tgt, idx, src, axis=0):
    return tgt.clone().index_add_(axis, idx, src)
def _segment_sum(src, seg, n):
    out = torch.zeros((n,) + src.shape[1:], dtype=src.dtype)
    return out.index_add_(0, seg, src)
def _pad(x, pw):
    flat = tuple(v for pair in reversed(pw) for v in pair)
    return torch.nn.functional.pad(x, flat)

kernel_name = 'gated_mix__ssm_conv_s4d__dense_activation_squared_relu__prompt'

def setup_inputs():

    _w = (lambda a: (lambda t: t.to(torch.bfloat16) if t.is_floating_point() else t)(torch.as_tensor(a)))
    def _A_setup():
        Bsz, L, D, N = 1, 2048, 640, 128
        return {
            "u": _w((lambda *s: torch.randn(*s, dtype=torch.bfloat16) * 0.05)(Bsz, L, D)),
            "log_A_real": _w((lambda *s: torch.randn(*s, dtype=torch.bfloat16) * 6.4)(D, N) * 0.5),
            "A_imag": _w((lambda *s: torch.randn(*s, dtype=torch.bfloat16) * 0.1)(D, N) * 3.14),
            "C_real": _w((lambda *s: torch.randn(*s, dtype=torch.bfloat16) * 0.4)(D, N) / N ** 0.5),
            "C_imag": _w((lambda *s: torch.randn(*s, dtype=torch.bfloat16) * 0.4)(D, N) / N ** 0.5),
            "log_dt": _w((lambda *s: torch.randn(*s, dtype=torch.bfloat16) * 0.1)(D) * 0.5 - 4.0),
            "D_skip": _w((lambda *s: torch.randn(*s, dtype=torch.bfloat16) * 0.1)(D)),
        }
    _A_in = _A_setup()

    def _B_setup():
        B, S, D, F = 1, 2048, 640, 1728
        return {
            "x": _w((lambda *s: torch.randn(*s, dtype=torch.bfloat16) * 0.05)(B, S, D)),
            "w1": _w((lambda *s: torch.randn(*s, dtype=torch.bfloat16) * 0.05)(D, F)),
            "b1": _w((lambda *s: torch.randn(*s, dtype=torch.bfloat16) * 0.05)(F)),
            "w2": _w((lambda *s: torch.randn(*s, dtype=torch.bfloat16) * 0.05)(F, D)),
            "b2": _w((lambda *s: torch.randn(*s, dtype=torch.bfloat16) * 0.05)(D)),
        }
    _B_in = _B_setup()

    return {
        "A_u": _A_in["u"],
        "A_log_A_real": _A_in["log_A_real"],
        "A_A_imag": _A_in["A_imag"],
        "A_C_real": _A_in["C_real"],
        "A_C_imag": _A_in["C_imag"],
        "A_log_dt": _A_in["log_dt"],
        "A_D_skip": _A_in["D_skip"],
        "B_x": _B_in["x"],
        "B_w1": _B_in["w1"],
        "B_b1": _B_in["b1"],
        "B_w2": _B_in["w2"],
        "B_b2": _B_in["b2"],
        "gate_w": _w((lambda *s: torch.randn(*s, dtype=torch.bfloat16) * 0.05)(640) * 16.0),
    }

def reference(A_u, A_log_A_real, A_A_imag, A_C_real, A_C_imag, A_log_dt, A_D_skip, B_x, B_w1, B_b1, B_w2, B_b2, gate_w):
    def _A_ref(u, log_A_real, A_imag, C_real, C_imag, log_dt, D_skip):
        L = u.shape[1]
        dt = torch.exp(log_dt)
        A = -torch.exp(log_A_real) + 1j * A_imag
        C = (C_real + 1j * C_imag) * dt[:, None]
        pos = (lambda x, dt: x.to(dt))(torch.arange(L), log_dt.dtype)
        P = torch.exp((A * dt[:, None])[:, :, None] * pos[None, None, :])
        K = 2.0 * torch.einsum("dn,dnl->dl", C, P).real
        L2 = 2 * L
        u_t = (lambda x, *d: x.permute(*d))(u, 0, 2, 1)
        Kf = (lambda x, *a, **k: torch.fft.rfft(x.float(), *a, **k))(K, L2)
        Uf = (lambda x, *a, **k: torch.fft.rfft(x.float(), *a, **k))(u_t, L2)
        y = (lambda x, *a, **k: torch.fft.irfft(x, *a, **k).to(torch.bfloat16))(Uf * Kf[None, :, :], L2)[:, :, :L]
        y = (lambda x, *d: x.permute(*d))(y, 0, 2, 1)
        return y + u * D_skip[None, None, :]

    def _B_ref(x, w1, b1, w2, b2):
        h = torch.einsum("bsd,df->bsf", x, w1) + b1
        zero = torch.zeros_like(h)
        r = torch.where(h > zero, h, zero)
        h = r * r
        out = torch.einsum("bsf,fd->bsd", h, w2) + b2
        return out

    x = A_u
    g = _sigmoid((x * gate_w).sum(-1, keepdims=True))
    a = _A_ref(x, A_log_A_real, A_A_imag, A_C_real, A_C_imag, A_log_dt, A_D_skip)
    b = _B_ref(x, B_w1, B_b1, B_w2, B_b2)
    return g * a + (1 - g) * b
```

```cpp
#include <hip/hip_runtime.h>
#include <hip/hip_bf16.h>
typedef unsigned short u16;
constexpr int L = 2048, D = 640, N = 128, F = 1728;
__device__ __forceinline__ float bf2f(u16 x) { return __uint_as_float(((unsigned)x) << 16); }
__device__ __forceinline__ u16 f2bf(float f) { unsigned u = __float_as_uint(f); u += 0x7fffu + ((u >> 16) & 1u); return (u16)(u >> 16); }
__device__ __forceinline__ float rbf(float f) { return bf2f(f2bf(f)); }

__global__ void kgen(const u16* lar, const u16* aim, const u16* cr, const u16* ci, const u16* ldt, float* K) {
  int d = blockIdx.x;
  __shared__ float zr[N], zi[N], Cr[N], Ci[N];
  float dt = rbf(expf(bf2f(ldt[d])));
  if (threadIdx.x < N) {
    int n = threadIdx.x;
    float ar = -rbf(expf(bf2f(lar[d * N + n])));
    zr[n] = ar * dt; zi[n] = bf2f(aim[d * N + n]) * dt;
    Cr[n] = bf2f(cr[d * N + n]) * dt; Ci[n] = bf2f(ci[d * N + n]) * dt;
  }
  __syncthreads();
  for (int l = threadIdx.x; l < L; l += blockDim.x) {
    float pos = rbf((float)l);
    float s = 0.f;
    for (int n = 0; n < N; ++n) {
      float e = expf(zr[n] * pos); float sn, cs; sincosf(zi[n] * pos, &sn, &cs);
      s += e * (Cr[n] * cs - Ci[n] * sn);
    }
    K[d * L + l] = 2.f * s;
  }
}
__global__ void conv(const u16* u, const float* K, const u16* dskip, u16* a) {
  int d = blockIdx.x;
  __shared__ float Ks[L], us[L];
  for (int l = threadIdx.x; l < L; l += blockDim.x) { Ks[l] = K[d * L + l]; us[l] = bf2f(u[l * D + d]); }
  __syncthreads();
  float ds = bf2f(dskip[d]);
  for (int l = threadIdx.x; l < L; l += blockDim.x) {
    float s = 0.f;
    for (int j = 0; j <= l; ++j) s += Ks[l - j] * us[j];
    a[l * D + d] = f2bf(rbf(s) + rbf(us[l] * ds));
  }
}
__global__ void gemm1(const u16* x, const u16* w1, const u16* b1, u16* h) {
  int f = blockIdx.x * 64 + (threadIdx.x & 63), l = blockIdx.y * 4 + (threadIdx.x >> 6);
  float s = 0.f;
  for (int k = 0; k < D; ++k) s += bf2f(x[l * D + k]) * bf2f(w1[k * F + f]);
  float v = rbf(rbf(s) + bf2f(b1[f]));
  v = v > 0.f ? v : 0.f;
  h[l * F + f] = f2bf(v * v);
}
__global__ void gemm2(const u16* x, const u16* h, const u16* w2, const u16* b2, const u16* gw, const u16* a, u16* out) {
  int d = blockIdx.x * 64 + (threadIdx.x & 63), l = blockIdx.y * 4 + (threadIdx.x >> 6);
  float s = 0.f;
  for (int k = 0; k < F; ++k) s += bf2f(h[l * F + k]) * bf2f(w2[k * D + d]);
  float b = rbf(rbf(s) + bf2f(b2[d]));
  float gs = 0.f;
  for (int k = 0; k < D; ++k) gs += rbf(bf2f(x[l * D + k]) * bf2f(gw[k]));
  gs = rbf(gs);
  float g = rbf(1.f / (1.f + expf(-gs)));
  float av = bf2f(a[l * D + d]);
  out[l * D + d] = f2bf(rbf(g * av) + rbf(rbf(1.f - g) * b));
}
extern "C" void kernel_launch(void* const* d_in, const int* in_sizes, int n_in, void* d_out, int out_size, void* d_ws, size_t ws_size, hipStream_t stream) {
  const u16* u = (const u16*)d_in[0];
  float* K = (float*)d_ws;
  u16* a = (u16*)((char*)d_ws + (size_t)D * L * 4);
  u16* h = a + L * D;
  kgen<<<D, 256, 0, stream>>>((const u16*)d_in[1], (const u16*)d_in[2], (const u16*)d_in[3], (const u16*)d_in[4], (const u16*)d_in[5], K);
  conv<<<D, 256, 0, stream>>>(u, K, (const u16*)d_in[6], a);
  gemm1<<<dim3(F / 64, L / 4), 256, 0, stream>>>(u, (const u16*)d_in[8], (const u16*)d_in[9], h);
  gemm2<<<dim3(D / 64, L / 4), 256, 0, stream>>>(u, h, (const u16*)d_in[10], (const u16*)d_in[11], (const u16*)d_in[12], a, (u16*)d_out);
}
```

```cpp
#include <hip/hip_runtime.h>
#include <stdint.h>

typedef unsigned short u16;
typedef unsigned long long u64;
typedef short s16x4 __attribute__((ext_vector_type(4)));
typedef short s16x8 __attribute__((ext_vector_type(8)));
typedef float f32x2 __attribute__((ext_vector_type(2)));
typedef float f32x4 __attribute__((ext_vector_type(4)));
typedef unsigned u32x2 __attribute__((ext_vector_type(2)));
typedef unsigned u32x4 __attribute__((ext_vector_type(4)));
typedef __bf16 bf16x2 __attribute__((ext_vector_type(2)));
#define LAS __attribute__((address_space(3)))
#define GAS __attribute__((address_space(1)))
#define DI __device__ __forceinline__

constexpr int SL = 2048, SD = 640, SN = 128, SF = 1728;

constexpr unsigned WS_FT = 0, WS_FH = 32768, WS_FA = 65536, WS_EP = 98304;
constexpr unsigned WS_GP = 131072;
constexpr unsigned WS_UT = 262144, UT_ROW = 4224;
constexpr unsigned WS_W2T = 3u << 20;
constexpr unsigned WS_AT = 6u << 20;
constexpr unsigned WS_H = 9u << 20;
constexpr unsigned WS_END = 17u << 20;
constexpr unsigned WS_DBG = 32u << 20;

constexpr unsigned L_RING = 0;
constexpr unsigned L_TS = 131072;
constexpr unsigned L_OP = 0;
constexpr unsigned L_UC = 65536, UC_STRIDE = 5184;
constexpr unsigned L_KR = 107008, KR_STRIDE = 4608;
constexpr unsigned L_XC = 116224;
constexpr unsigned L_CX = 0;
constexpr unsigned L_RED = 0;
constexpr unsigned L_AT = 141312;
constexpr unsigned L_TOTAL = 151552;

struct Params {
  const u16 *u, *lar, *aim, *cr, *ci, *ldt, *dsk, *w1, *b1, *w2, *b2, *gw;
  u16* out;
  char* ws;
};

DI float bf2f(unsigned x) { return __uint_as_float(x << 16); }
DI unsigned pk(float lo, float hi) { f32x2 v = {lo, hi}; return __builtin_bit_cast(unsigned, __builtin_convertvector(v, bf16x2)); }
DI float rbf(float f) { return bf2f(pk(f, f) & 0xffffu); }
DI __amdgpu_buffer_rsrc_t mkrsrc(const void* p, unsigned bytes) { return __builtin_amdgcn_make_buffer_rsrc((void*)p, 0, bytes, 0x00020000); }
DI float relu(float x) { float r; asm("v_max_f32 %0, 0, %1" : "=v"(r) : "v"(x)); return r; }
DI int rfl(int x) { return __builtin_amdgcn_readfirstlane(x); }

#define DMA(ldsaddr, voff, rsrc, soff) \
  asm volatile("s_mov_b32 m0, %0\n\ts_nop 0\n\tbuffer_load_dwordx4 %1, %2, %3 offen lds" ::"s"(ldsaddr), "v"(voff), "s"(rsrc), "s"(soff) : "memory")
#define DMA_SC1(ldsaddr, voff, rsrc, soff) \
  asm volatile("s_mov_b32 m0, %0\n\ts_nop 0\n\tbuffer_load_dwordx4 %1, %2, %3 offen sc1 lds" ::"s"(ldsaddr), "v"(voff), "s"(rsrc), "s"(soff) : "memory")
#define WAITV(n) asm volatile("s_waitcnt vmcnt(" #n ")" ::: "memory")
#define WAITL0() asm volatile("s_waitcnt lgkmcnt(0)" ::: "memory")
#define BAR() do { asm volatile("s_waitcnt lgkmcnt(0)\n\ts_barrier" ::: "memory"); } while (0)
#define SCHED() __builtin_amdgcn_sched_barrier(0)

DI s16x4 trd(unsigned a) { return __builtin_amdgcn_ds_read_tr16_b64_v4i16((LAS s16x4*)a); }
DI s16x8 cat(s16x4 a, s16x4 b) { return __builtin_shufflevector(a, b, 0, 1, 2, 3, 4, 5, 6, 7); }
DI s16x8 ld128(unsigned a) { return *(LAS s16x8*)a; }
DI s16x4 ld64(unsigned a) { return *(LAS s16x4*)a; }
DI void st128(unsigned a, u32x4 v) { *(LAS u32x4*)a = v; }
DI void st64(unsigned a, u32x2 v) { *(LAS u32x2*)a = v; }
DI void st32(unsigned a, unsigned v) { *(LAS unsigned*)a = v; }
DI void st16(unsigned a, unsigned v) { *(LAS u16*)a = (u16)v; }
DI f32x4 mfma(s16x8 a, s16x8 b, f32x4 c) { return __builtin_amdgcn_mfma_f32_16x16x32_bf16(__builtin_bit_cast(__attribute__((ext_vector_type(8))) __bf16, a), __builtin_bit_cast(__attribute__((ext_vector_type(8))) __bf16, b), c, 0, 0, 0); }
DI void st_sc1(__amdgpu_buffer_rsrc_t r, unsigned voff, unsigned soff, u32x4 v) {
  asm volatile("buffer_store_dwordx4 %0, %1, %2, %3 offen sc1\n\ts_nop 1" ::"v"(v), "v"(voff), "s"(r), "s"(soff) : "memory");
}
#define DMA4(ldsaddr, v0, v1, v2, v3, rsrc, s0, s1, s2, s3) \
  asm volatile("s_mov_b32 m0, %0\n\ts_nop 0\n\tbuffer_load_dwordx4 %1, %5, %6 offen lds\n\tbuffer_load_dwordx4 %2, %5, %7 offen offset:1024 lds\n\t" \
               "buffer_load_dwordx4 %3, %5, %8 offen offset:2048 lds\n\tbuffer_load_dwordx4 %4, %5, %9 offen offset:3072 lds" \
               ::"s"(ldsaddr), "v"(v0), "v"(v1), "v"(v2), "v"(v3), "s"(rsrc), "s"(s0), "s"(s1), "s"(s2), "s"(s3) : "memory")
#define DMA4_SC1(ldsaddr, v0, rsrc, s0) \
  asm volatile("s_mov_b32 m0, %0\n\ts_nop 0\n\tbuffer_load_dwordx4 %1, %2, %3 offen sc1 lds\n\tbuffer_load_dwordx4 %1, %2, %3 offen offset:1024 sc1 lds\n\t" \
               "buffer_load_dwordx4 %1, %2, %3 offen offset:2048 sc1 lds\n\tbuffer_load_dwordx4 %1, %2, %3 offen offset:3072 sc1 lds" \
               ::"s"(ldsaddr), "v"(v0), "s"(rsrc), "s"(s0) : "memory")
DI u32x2 ald64(const void* p) { u32x2 r; asm volatile("global_load_dwordx2 %0, %1, off" : "=v"(r) : "v"(p) : "memory"); return r; }
DI unsigned ald16(const void* p) { unsigned r; asm volatile("global_load_ushort %0, %1, off" : "=v"(r) : "v"(p) : "memory"); return r; }
DI u64 apoll(const char* ws, unsigned off) { u64 r; asm volatile("global_load_dwordx2 %0, %1, %2 sc1" : "=v"(r) : "v"(off), "s"(ws) : "memory"); return r; }
#define RDY(x) asm volatile("; ready %0" : "+v"(x))
template <int I> DI void wload(u32x4 (&W)[70], unsigned voff, __amdgpu_buffer_rsrc_t r, unsigned so) {
  const unsigned s2 = so + (I >> 2) * 4096;
  if (I < 44) asm volatile("buffer_load_dwordx4 %0, %1, %2, %3 offen offset:%4 sc1" : "=a"(W[I]) : "v"(voff), "s"(r), "s"(s2), "i"((I & 3) * 1024));
  else if (I < 70) asm volatile("buffer_load_dwordx4 %0, %1, %2, %3 offen offset:%4 sc1" : "=v"(W[I < 70 ? I : 0]) : "v"(voff), "s"(r), "s"(s2), "i"((I & 3) * 1024));
}
template <int I0, int N> DI void wloads(u32x4 (&W)[70], unsigned voff, __amdgpu_buffer_rsrc_t r, unsigned so) {
  if constexpr (N > 0) { wload<I0>(W, voff, r, so); wloads<I0 + 1, N - 1>(W, voff, r, so); }
}
struct Prm { unsigned lar, aim, cr, ci, ldt, dsk; };

#define STAMP(i)

DI void wait_flags(const char* ws, unsigned off, u64 tag) {
  const GAS u64* f = (const GAS u64*)(ws + off);
#pragma unroll 1
  for (unsigned spins = 0; spins < (1u << 18); ++spins) {
    u64 v = __hip_atomic_load(f, __ATOMIC_RELAXED, __HIP_MEMORY_SCOPE_AGENT);
    if (__all(v == tag)) break;
    __builtin_amdgcn_s_sleep(1);
  }
  asm volatile("" ::: "memory");
}
DI void set_flag(char* ws, unsigned off, u64 tag) { __hip_atomic_store((GAS u64*)(ws + off), tag, __ATOMIC_RELAXED, __HIP_MEMORY_SCOPE_AGENT); }

DI f32x2 cmul(f32x2 a, f32x2 b) {
  f32x2 t, r;
  asm("v_pk_mul_f32 %0, %1, %2 op_sel:[0,0] op_sel_hi:[0,1]" : "=v"(t) : "v"(a), "v"(b));
  asm("v_pk_fma_f32 %0, %1, %2, %3 op_sel:[1,1,0] op_sel_hi:[1,0,1] neg_lo:[1,0,0]" : "=v"(r) : "v"(a), "v"(b), "v"(t));
  return r;
}
DI f32x2 cexp2(float e, float t) {
  float m = __builtin_amdgcn_exp2f(e), f = __builtin_amdgcn_fractf(t);
  f32x2 r = {m * __builtin_amdgcn_cosf(f), m * __builtin_amdgcn_sinf(f)};
  return r;
}

struct Ctx {
  int lane, wave, q, l15;
  int b, rg, cg, s, p64, ct;
  u64 tag;
  __amdgpu_buffer_rsrc_t rws;
};

DI void load_prm(const Params& p, const Ctx& c, int d, Prm& m) {
  const int n = d * SN + 64 * (c.wave & 1) + c.lane;
  m.lar = ald16(p.lar + n); m.aim = ald16(p.aim + n); m.cr = ald16(p.cr + n); m.ci = ald16(p.ci + n);
  m.ldt = ald16(p.ldt + d); m.dsk = ald16(p.dsk + d);
}
DI void rdy_prm(Prm& m) { RDY(m.lar); RDY(m.aim); RDY(m.cr); RDY(m.ci); RDY(m.ldt); RDY(m.dsk); }
DI void load_u(const Ctx& c, int d) {
  const unsigned voff = c.lane * 16;
  const int pair = c.wave >> 1, hw = c.wave & 1;
#pragma unroll
  for (int cc = 0; cc < 2; ++cc) {
    const int cpy = 2 * hw + cc;
    const unsigned dst = L_UC + (pair * 4 + cpy) * UC_STRIDE, so = WS_UT + d * UT_ROW + 96 + 2 * cpy;
    DMA4_SC1(dst, voff, c.rws, so);
    DMA_SC1(dst + 4096, voff, c.rws, so + 4096);
  }
}

template <bool LAST>
DI void ssm_round(const Params& p, const Ctx& c, int d, Prm& m, bool pub_h, bool hst, u32x4 (&W)[70], unsigned wso, u64& pollv, unsigned polloff) {
  const int lane = c.lane, q = c.q, l15 = c.l15, pair = c.wave >> 1, hw = c.wave & 1;
  const unsigned wvo = lane * 16;
  const float lar = bf2f(m.lar), aim = bf2f(m.aim), cr = bf2f(m.cr), ci = bf2f(m.ci), ldt = bf2f(m.ldt), dsk = bf2f(m.dsk);
  if (!LAST) load_prm(p, c, d + 2, m);
  const float dt = rbf(expf(ldt));
  const float zr = -rbf(expf(lar)) * dt, zi = aim * dt;
  const float er = zr * 1.4426950408889634f, th = zi * 0.15915494309189535f;
  const f32x2 W1 = cexp2(er, th), W8 = cexp2(8.f * er, 8.f * th), W128 = cexp2(128.f * er, 128.f * th);
  const f32x2 W2 = cmul(W1, W1), W4 = cmul(W2, W2);
  const unsigned opw = L_OP + c.wave * 16384;
  const unsigned sw = ((lane & 1) << 1) | (((lane >> 2) & 1) << 2) | ((lane >> 1) & 1);
  const unsigned rowre = opw + lane * 256, rowim = rowre + 128;
  auto put = [&](int chunk, const f32x2 (&v)[8], bool neg) {
    u32x4 re = {pk(v[0].x, v[1].x), pk(v[2].x, v[3].x), pk(v[4].x, v[5].x), pk(v[6].x, v[7].x)};
    u32x4 im;
    if (neg) im = (u32x4){pk(-v[0].y, -v[1].y), pk(-v[2].y, -v[3].y), pk(-v[4].y, -v[5].y), pk(-v[6].y, -v[7].y)};
    else im = (u32x4){pk(v[0].y, v[1].y), pk(v[2].y, v[3].y), pk(v[4].y, v[5].y), pk(v[6].y, v[7].y)};
    unsigned o = ((unsigned)chunk ^ sw) << 4;
    st128(rowre + o, re); st128(rowim + o, im);
  };
  {
    f32x2 v[8];
    f32x2 T = {2.f * cr * dt, 2.f * ci * dt};
    v[0] = T; for (int r = 1; r < 8; ++r) v[r] = cmul(v[r - 1], W1);
    put(0, v, false);
    if (LAST) wloads<0, 4>(W, wvo, c.rws, wso);
    T = cmul(T, W128);
    v[0] = T; for (int r = 1; r < 8; ++r) v[r] = cmul(v[r - 1], W1);
    put(1, v, false);
    if (LAST) wloads<4, 4>(W, wvo, c.rws, wso);
    T = cmul(T, W128); v[0] = T; for (int r = 1; r < 4; ++r) v[r] = cmul(v[r - 1], W2);
    T = cmul(T, W128); v[4] = T; for (int r = 5; r < 8; ++r) v[r] = cmul(v[r - 1], W2);
    put(2, v, false);
    if (LAST) wloads<8, 4>(W, wvo, c.rws, wso);
    for (int i = 0; i < 4; ++i) { T = cmul(T, W128); v[2 * i] = T; v[2 * i + 1] = cmul(T, W4); }
    put(3, v, false);
    if (LAST) wloads<12, 4>(W, wvo, c.rws, wso);
    for (int i = 0; i < 8; ++i) { T = cmul(T, W128); v[i] = T; }
    put(4, v, false);
    if (LAST) wloads<16, 4>(W, wvo, c.rws, wso);
    T = cmul(T, W128); v[0] = T; for (int r = 1; r < 8; ++r) v[r] = (f32x2){0.f, 0.f};
    put(5, v, false);
    if (LAST) wloads<20, 4>(W, wvo, c.rws, wso);
    v[0] = (f32x2){1.f, 0.f}; v[1] = W8; for (int r = 2; r < 8; ++r) v[r] = cmul(v[r - 1], W8);
    put(6, v, true);
    if (LAST) wloads<24, 4>(W, wvo, c.rws, wso);
    v[0] = cmul(v[7], W8); for (int r = 1; r < 8; ++r) v[r] = cmul(v[r - 1], W8);
    put(7, v, true);
    if (LAST) wloads<28, 4>(W, wvo, c.rws, wso);
  }
  f32x4 D0 = {0, 0, 0, 0}, D1 = D0, D2 = D0;
  {
    const int r = l15 >> 2, pp = lane & 3;
    unsigned ad[4][2];
#pragma unroll
    for (int tile = 0; tile < 4; ++tile)
#pragma unroll
      for (int t = 0; t < 2; ++t) {
        unsigned swz = ((r >> 1) << 1) | ((q & 1) << 2) | t;
        ad[tile][t] = opw + (8 * q + 4 * t + r) * 128 + (((2 * tile + (pp >> 1)) ^ swz) << 4) + (pp & 1) * 8;
      }
#pragma unroll
    for (int ks = 0; ks < 4; ++ks) {
      s16x8 fb = cat(trd(ad[3][0] + ks * 4096), trd(ad[3][1] + ks * 4096));
      s16x8 f0 = cat(trd(ad[0][0] + ks * 4096), trd(ad[0][1] + ks * 4096));
      s16x8 f1 = cat(trd(ad[1][0] + ks * 4096), trd(ad[1][1] + ks * 4096));
      s16x8 f2 = cat(trd(ad[2][0] + ks * 4096), trd(ad[2][1] + ks * 4096));
      D0 = mfma(f0, fb, D0); D1 = mfma(f1, fb, D1); D2 = mfma(f2, fb, D2);
    }
  }
  const unsigned xme = L_XC + c.wave * 2048 + lane * 16, xot = L_XC + (c.wave ^ 1) * 2048 + lane * 16;
  const unsigned kr = L_KR + pair * KR_STRIDE;
  if (hw == 0) {
    st128(xme, __builtin_bit_cast(u32x4, D0)); st128(xme + 1024, __builtin_bit_cast(u32x4, D1));
    if (lane < 30) st128(kr + 4096 + lane * 16, (u32x4){0u, 0u, 0u, 0u});
  } else {
    st128(xme, __builtin_bit_cast(u32x4, D2));
  }
  BAR();
  if (hw == 0) {
    f32x4 o = __builtin_bit_cast(f32x4, ld128(xot)); D2 += o;
#pragma unroll
    for (int r = 0; r < 4; ++r) {
      const int i = 4 * q + r, a = 1024 + 128 * i + 8 * l15;
      const unsigned e = pk(D2[r], D2[r]);
      if (a < 2048) st64(kr + 2 * (2044 - a), (u32x2){e, e});
      if (a <= 2048 && a != 1024) st64(kr + 2 * (2048 - a), (u32x2){e, e});
      if (a == 1024) st32(kr + 2 * 1024, e);
      if (a < 2048 && !(l15 & 1)) st16(kr + 2 * (2043 - a), e);
    }
  } else {
    f32x4 o0 = __builtin_bit_cast(f32x4, ld128(xot)), o1 = __builtin_bit_cast(f32x4, ld128(xot + 1024));
    D0 += o0; D1 += o1;
    if (q >= 2) {
#pragma unroll
      for (int i2 = 0; i2 < 2; ++i2) {
        const int a = 512 + 128 * (2 * (q - 2) + i2) + 8 * l15;
        const unsigned e00 = pk(D1[2 * i2], D1[2 * i2]), e10 = pk(D1[2 * i2 + 1], D1[2 * i2]), e11 = pk(D1[2 * i2 + 1], D1[2 * i2 + 1]);
        st32(kr + 2 * (2048 - a), e00);
        st64(kr + 2 * (2044 - a), (u32x2){e10, e00});
        st32(kr + 2 * (2042 - a), e11);
      }
    }
    asm volatile("" ::: "memory");
    if (q < 2) {
      const int a = 256 + 128 * q + 8 * l15;
      const unsigned e00 = pk(D1[0], D1[0]), e21 = pk(D1[2], D1[1]), e22 = pk(D1[2], D1[2]), e33 = pk(D1[3], D1[3]);
      st16(kr + 2 * (2048 - a), e00);
      st64(kr + 2 * (2044 - a), (u32x2){e21, e00});
      st32(kr + 2 * (2042 - a), e22);
      st16(kr + 2 * (2041 - a), e33);
    }
    asm volatile("" ::: "memory");
    {
      const int a = 128 * (q >> 1) + 8 * l15 + 4 * (q & 1);
      st64(kr + 2 * (2044 - a), (u32x2){pk(D0[3], D0[2]), pk(D0[1], D0[0])});
    }
  }
  if (LAST) { if (hst) WAITV(40); else WAITV(32); } else { WAITV(0); rdy_prm(m); }
  BAR();
  f32x4 acc[8];
#pragma unroll
  for (int i = 0; i < 8; ++i) acc[i] = (f32x4){0, 0, 0, 0};
  {
    const unsigned cpy = (l15 + 1) & 3;
    const unsigned abase = L_UC + (pair * 4 + cpy) * UC_STRIDE + 2 * (4 * ((l15 + 1) >> 2) + 8 * q) + 64 * (4 * hw);
    const unsigned bbase = kr + 2 * (2032 - 16 * l15 + 8 * q) + 64 * (4 * hw) - 7 * 512;
#pragma unroll
    for (int s0 = 0; s0 < 4; ++s0) {
      s16x8 A[8], B[8];
#pragma unroll
      for (int i = 0; i < 8; ++i) {
        A[i] = cat(ld64(abase + 64 * s0 + 512 * i), ld64(abase + 64 * s0 + 512 * i + 8));
        B[i] = ld128(bbase + 64 * s0 + 512 * (7 - i));
      }
#pragma unroll
      for (int s1 = 0; s1 < 8; ++s1)
#pragma unroll
        for (int cp = 0; cp + s1 < 8; ++cp) acc[s1 + cp] = mfma(A[s1], B[cp], acc[s1 + cp]);
      if (LAST) {
        if (s0 == 0) wloads<32, 10>(W, wvo, c.rws, wso);
        if (s0 == 1) {
          WAITV(42);
          BAR();
          if (pub_h && threadIdx.x == 0) set_flag(p.ws, WS_FH + c.b * 128, c.tag);
          wloads<42, 14>(W, wvo, c.rws, wso);
        }
        if (s0 == 2) wloads<56, 14>(W, wvo, c.rws, wso);
        if (s0 == 3) pollv = apoll(p.ws, polloff);
      }
    }
  }
  {
    const unsigned cme = L_CX + c.wave * 4096 + lane * 16, cot = L_CX + (c.wave ^ 1) * 4096 + lane * 16;
    if (hw == 0) { for (int i = 0; i < 4; ++i) st128(cme + i * 1024, __builtin_bit_cast(u32x4, acc[4 + i])); }
    else { for (int i = 0; i < 4; ++i) { st128(cme + i * 1024, __builtin_bit_cast(u32x4, acc[i])); acc[i] = acc[4 + i]; } }
    BAR();
    unsigned o[4][2];
#pragma unroll
    for (int i = 0; i < 4; ++i) {
      f32x4 y = acc[i] + __builtin_bit_cast(f32x4, ld128(cot + i * 1024));
      const int l = 256 * (4 * hw + i) + 16 * l15 + 4 * q;
      u32x2 uu = __builtin_bit_cast(u32x2, ld64(L_UC + pair * 4 * UC_STRIDE + 2 * (l + 16)));
      y[0] += dsk * bf2f(uu.x & 0xffffu); y[1] += dsk * __uint_as_float(uu.x & 0xffff0000u);
      y[2] += dsk * bf2f(uu.y & 0xffffu); y[3] += dsk * __uint_as_float(uu.y & 0xffff0000u);
      o[i][0] = pk(y[0], y[1]); o[i][1] = pk(y[2], y[3]);
    }
#pragma unroll
    for (int i = 0; i < 4; i += 2) {
      auto r0 = __builtin_amdgcn_permlane16_swap(o[i][0], o[i + 1][0], false, false);
      auto r1 = __builtin_amdgcn_permlane16_swap(o[i][1], o[i + 1][1], false, false);
      u32x4 v = {r0[0], r1[0], r0[1], r1[1]};
      const unsigned voff = 2 * (256 * (q & 1) + 16 * l15 + 8 * (q >> 1));
      st_sc1(c.rws, voff, WS_AT + d * 4096 + 512 * (4 * hw + i), v);
    }
  }
  BAR();
  if (!LAST) load_u(c, d + 2);
}

__global__ void __launch_bounds__(256, 1) fused(Params p) {
  extern __shared__ __attribute__((aligned(16))) char smem[];
  Ctx c;
  const int tid = threadIdx.x;
  c.lane = tid & 63; c.wave = rfl(tid >> 6); c.q = c.lane >> 4; c.l15 = c.lane & 15;
  const int lane = c.lane, w = c.wave, q = c.q, l15 = c.l15;
  c.b = blockIdx.x; const int g = c.b & 7; c.s = c.b >> 3; c.rg = g >> 1; c.cg = g & 1;
  c.p64 = c.rg * 8 + (c.s >> 2); c.ct = c.cg * 4 + (c.s & 3);
  unsigned pf;
  {
    u64 pc; asm volatile("s_getpc_b64 %0" : "=s"(pc));
    const char* a = (const char*)(pc & ~127ull) + (tid < 256 ? tid : 0) * 128;
    asm volatile("global_load_dword %0, %1, off" : "=v"(pf) : "v"(a) : "memory");
  }
  STAMP(0);
  u64 ep;
  asm volatile("s_load_dwordx2 %0, %1, 0x0" : "=s"(ep) : "s"(p.ws + WS_EP) : "memory");
  c.rws = mkrsrc(p.ws, WS_END);
  const __amdgpu_buffer_rsrc_t rx = mkrsrc(p.u, SL * SD * 2), rw1 = mkrsrc(p.w1, SD * SF * 2), rw2 = mkrsrc(p.w2, SF * SD * 2);

  const bool two = c.p64 < 22;
#pragma unroll
  for (int i = 0; i < 5; ++i) {
    const int idx = w + 4 * i;
    const int ii = idx < 10 ? idx : (idx < 15 ? idx - 10 : idx - 15);
    const unsigned gi = 64 * ii + lane, row = (gi * 6554) >> 16, gr = gi - row * 10;
    const unsigned voff = row * 1280 + gr * 16;
    if (idx < 10) { DMA(L_TS + idx * 1024, voff, rx, (unsigned)(c.p64 * 64 * 1280 + c.ct * 160)); }
    else if (idx < 15 || two) { const int ks = c.p64 + (idx < 15 ? 0 : 32); DMA(L_TS + idx * 1024, voff, rw2, (unsigned)(ks * 32 * 1280 + c.ct * 160)); }
  }
  const bool g1 = c.s < 28;
  const int pi = c.s / 7, ti = c.s - pi * 7, P = c.rg * 4 + pi;
  const int nun = ((c.rg & 1) ^ c.cg) ? 13 : 14;
  const int u0 = (c.cg ? ((c.rg & 1) ? 13 : 14) : 0) + 2 * ti;
  const bool half = (ti == 6) && (nun == 13);
  const int wf = w & 1, wm = w >> 1;
  const unsigned vx0 = (lane >> 3) * 1280 + (((lane & 7) ^ (lane >> 4)) << 4), vx1 = (lane >> 3) * 1280 + (((lane & 7) ^ (4 + (lane >> 4))) << 4);
  const unsigned vw0 = q * 3456 + ((l15 ^ (q << 1)) << 4), vw1 = q * 3456 + ((l15 ^ ((q | 4) << 1)) << 4);
  auto stage = [&](int kt) {
    const unsigned slot = L_RING + (kt & 3) * 32768 + w * 4096;
    const unsigned sx = (P * 128 + 32 * w) * 1280 + kt * 128, sw = (kt * 64 + 16 * w) * 3456 + u0 * 128;
    DMA4(slot, vx0, vx1, vx0, vx1, rx, sx, sx + (10240 - 1024), sx + 2 * (10240 - 1024), sx + 3 * (10240 - 1024));
    DMA4(slot + 16384, vw0, vw0, vw1, vw1, rw1, sw, sw + (13824 - 1024), sw + 2 * (13824 - 1024), sw + 3 * (13824 - 1024));
  };
  u32x2 gwv[5], b1v[4];
#pragma unroll
  for (int i = 0; i < 5; ++i) gwv[i] = ald64(p.gw + c.ct * 80 + (tid & 3) * 20 + 4 * i);
#pragma unroll
  for (int ft = 0; ft < 4; ++ft) { const int f = u0 * 64 + 64 * wf + 16 * ft + 4 * q; b1v[ft] = ald64(p.b1 + (f < SF ? f : 0)); }
  int d = c.ct * 80 + 2 * c.p64 + 4 * (c.p64 >> 3) + (w >> 1);
  Prm prm;
  load_prm(p, c, d, prm);
  if (g1) { stage(0); stage(1); stage(2); stage(3); WAITV(32); } else { WAITV(0); }
#pragma unroll
  for (int i = 0; i < 5; ++i) RDY(gwv[i]);
#pragma unroll
  for (int i = 0; i < 4; ++i) RDY(b1v[i]);
  rdy_prm(prm); RDY(pf);
  asm volatile("s_waitcnt lgkmcnt(0)" : "+s"(ep)::"memory");
  c.tag = ep + 1;
  BAR();
  STAMP(1);
  {
    const int r = l15 >> 2, pp = lane & 3;
#pragma unroll
    for (int i = 0; i < 5; ++i) {
      const int idx = w + 4 * i;
      if (idx < 10) {
        const unsigned a = L_TS + (8 * q + r + (pp >> 1) * 32) * 160 + (8 * idx + 4 * (pp & 1)) * 2;
        s16x8 v = cat(trd(a), trd(a + 4 * 160));
        const unsigned voff = (l15 & 7) * UT_ROW + (8 * q + (l15 >> 3) * 32) * 2;
        st_sc1(c.rws, voff, WS_UT + (c.ct * 80 + 8 * idx) * UT_ROW + 128 + c.p64 * 128, __builtin_bit_cast(u32x4, v));
      } else if (idx < 15 || two) {
        const int un = idx < 15 ? 0 : 1, nt = idx - 10 - 5 * un, ks = c.p64 + 32 * un;
        const unsigned a = L_TS + 10240 + un * 5120 + (4 * q + r) * 160 + (16 * nt + 4 * pp) * 2;
        s16x8 v = cat(trd(a), trd(a + 16 * 160));
        st_sc1(c.rws, lane * 16, WS_W2T + ((c.ct * 54 + ks) * 5 + nt) * 1024, __builtin_bit_cast(u32x4, v));
      }
    }
    float gs = 0.f;
    const unsigned ga = L_TS + (tid >> 2) * 160 + (tid & 3) * 40;
#pragma unroll
    for (int i = 0; i < 5; ++i) {
      u32x2 uu = __builtin_bit_cast(u32x2, ld64(ga + i * 8));
      const unsigned ux = uu.x, uy = uu.y, gx = gwv[i].x, gy = gwv[i].y;
      gs = __builtin_amdgcn_fdot2_f32_bf16(__builtin_bit_cast(bf16x2, ux), __builtin_bit_cast(bf16x2, gx), gs, false);
      gs = __builtin_amdgcn_fdot2_f32_bf16(__builtin_bit_cast(bf16x2, uy), __builtin_bit_cast(bf16x2, gy), gs, false);
    }
    gs += __shfl_xor(gs, 1); gs += __shfl_xor(gs, 2);
    if ((tid & 3) == 0) asm volatile("buffer_store_dword %0, %1, %2, %3 offen sc1" ::"v"(gs), "v"((tid >> 2) * 4), "s"(c.rws), "s"(WS_GP + (c.p64 * 8 + c.ct) * 256) : "memory");
    if (c.p64 == 0 && tid < 160) st_sc1(c.rws, (tid >> 1) * UT_ROW + 96 + (tid & 1) * 16, WS_UT + c.ct * 80 * UT_ROW, (u32x4){0u, 0u, 0u, 0u});
  }
  STAMP(2);
  u32x4 W[70];
  const int ks0 = w < 2 ? 14 * w : 28 + 13 * (w - 2);
  const unsigned ftoff = WS_FT + (8 * (4 * (lane & 7) + (c.ct & 3)) + 2 * ((lane >> 3) & 3) + c.cg) * 128;
  u64 pollv = 0;
  if (g1) {
    f32x4 acc[4][4];
#pragma unroll
    for (int ft = 0; ft < 4; ++ft) {
      f32x4 bv = {bf2f(b1v[ft].x & 0xffffu), __uint_as_float(b1v[ft].x & 0xffff0000u), bf2f(b1v[ft].y & 0xffffu), __uint_as_float(b1v[ft].y & 0xffff0000u)};
#pragma unroll
      for (int mt = 0; mt < 4; ++mt) acc[ft][mt] = bv;
    }
    const int r = l15 >> 2, pp = lane & 3;
    const unsigned xb0 = (64 * wm + l15) * 128 + ((q ^ (l15 >> 1)) << 4), xb1 = xb0 ^ 64;
    unsigned wb[4];
#pragma unroll
    for (int ft = 0; ft < 4; ++ft) wb[ft] = 16384 + (8 * q + r) * 256 + (((8 * wf + 2 * ft + (pp >> 1)) ^ ((r | ((q & 1) << 2)) << 1)) << 4) + (pp & 1) * 8;
    s16x8 A0[4], B0[4], A1[4], B1[4];
    auto rd = [&](s16x8 (&A)[4], s16x8 (&B)[4], unsigned slot, int ks) {
#pragma unroll
      for (int i = 0; i < 4; ++i) {
        B[i] = ld128(slot + (ks ? xb1 : xb0) + i * 2048);
        A[i] = cat(trd(slot + wb[i] + ks * 8192), trd(slot + wb[i] + ks * 8192 + 1024));
      }
    };
    auto mm = [&](s16x8 (&A)[4], s16x8 (&B)[4]) {
#pragma unroll
      for (int ft = 0; ft < 4; ++ft)
#pragma unroll
        for (int mt = 0; mt < 4; ++mt) acc[ft][mt] = mfma(A[ft], B[mt], acc[ft][mt]);
    };
    WAITV(28); BAR();
    rd(A0, B0, L_RING, 0);
#pragma unroll
    for (int kt = 0; kt < 10; ++kt) {
      const unsigned slot = L_RING + (kt & 3) * 32768;
      rd(A1, B1, slot, 1);
      mm(A0, B0);
      if (kt < 3) WAITV(20); else if (kt < 7) WAITV(16); else if (kt == 7) WAITV(8); else WAITV(0);
      if (kt == 9) { RDY(pollv); if (!__all(pollv == c.tag)) wait_flags(p.ws, ftoff, c.tag); }
      BAR();
      if (kt == 3 && tid == 0) set_flag(p.ws, WS_FT + c.b * 128, c.tag);
      if (kt < 6) stage(kt + 4);
      if (kt == 8) pollv = apoll(p.ws, ftoff);
      if (kt == 9) load_u(c, d);
      if (kt < 9) rd(A0, B0, L_RING + ((kt + 1) & 3) * 32768, 0);
      mm(A1, B1);
    }
    STAMP(3);
    if (!(half && wf)) {
#pragma unroll
      for (int mt = 0; mt < 4; ++mt)
#pragma unroll
        for (int a = 0; a < 2; ++a) {
          f32x4 x0 = acc[2 * a][mt], x1 = acc[2 * a + 1][mt];
#pragma unroll
          for (int i = 0; i < 4; ++i) { x0[i] = relu(x0[i]); x1[i] = relu(x1[i]); }
          x0 *= x0; x1 *= x1;
          u32x4 v = {pk(x0[0], x0[1]), pk(x0[2], x0[3]), pk(x1[0], x1[1]), pk(x1[2], x1[3])};
          st_sc1(c.rws, lane * 16, WS_H + ((P * 8 + 4 * wm + mt) * 54 + 2 * (u0 + wf) + a) * 1024, v);
        }
    }
  } else {
    WAITV(0); BAR();
    if (tid == 0) set_flag(p.ws, WS_FT + c.b * 128, c.tag);
    wait_flags(p.ws, ftoff, c.tag);
    load_u(c, d);
  }
  STAMP(4);
  unsigned fhoff;
  {
    const int t = lane < 14 ? lane : 0, cgp = t >= 7, tip = t - 7 * cgp;
    fhoff = WS_FH + (8 * (7 * ((c.p64 >> 1) & 3) + tip) + 2 * c.rg + cgp) * 128;
  }
  const unsigned wso = WS_W2T + (c.ct * 54 + ks0) * 5120;
  if (!g1) {
#pragma unroll 1
    for (int r = 0; r < 2; ++r) { ssm_round<false>(p, c, d, prm, false, false, W, wso, pollv, fhoff); d += 2; }
  }
  STAMP(6);
  ssm_round<true>(p, c, d, prm, g1, g1 && !(half && wf), W, wso, pollv, fhoff);
  STAMP(7);
  WAITV(2); RDY(pollv);
  if (!__all(pollv == c.tag)) wait_flags(p.ws, fhoff, c.tag);
  STAMP(8);
  f32x4 acc[5][4];
#pragma unroll
  for (int nt = 0; nt < 5; ++nt)
#pragma unroll
    for (int mt = 0; mt < 4; ++mt) acc[nt][mt] = (f32x4){0, 0, 0, 0};
  u32x2 b2v[5];
#pragma unroll
  for (int nt = 0; nt < 5; ++nt) b2v[nt] = *(const u32x2*)(p.b2 + c.ct * 80 + 16 * nt + 4 * q);
  {
    constexpr int PD = 5;
    u32x4 H[14][4];
    const unsigned hb = WS_H + (c.p64 * 4 * 54 + ks0) * 1024;
    auto ldh = [&](int k) {
#pragma unroll
      for (int mt = 0; mt < 4; ++mt) H[k][mt] = __builtin_amdgcn_raw_buffer_load_b128(c.rws, lane * 16, hb + (mt * 54 + k) * 1024, 16);
    };
#pragma unroll
    for (int k = 0; k < PD; ++k) ldh(k);
    SCHED();
#pragma unroll
    for (int k = 0; k < 14; ++k) {
      if (k + PD < 14) ldh(k + PD);
      if (k == 6) {
        asm volatile("" :: "v"(H[6][3]));
        BAR();
        if (tid == 0) set_flag(p.ws, WS_FA + c.b * 128, c.tag);
      }
      if (k < 13 || w < 2) {
#pragma unroll
        for (int nt = 0; nt < 5; ++nt)
#pragma unroll
          for (int mt = 0; mt < 4; ++mt)
            acc[nt][mt] = mfma(__builtin_bit_cast(s16x8, W[k * 5 + nt]), __builtin_bit_cast(s16x8, H[k][mt]), acc[nt][mt]);
      }
      SCHED();
    }
  }
  STAMP(9);
#pragma unroll
  for (int mt = 0; mt < 4; ++mt)
    if (mt != w) {
#pragma unroll
      for (int nt = 0; nt < 5; ++nt) st128(L_RED + ((mt * 4 + w) * 5 + nt) * 1024 + lane * 16, __builtin_bit_cast(u32x4, acc[nt][mt]));
    }
  f32x4 own[5];
#pragma unroll
  for (int nt = 0; nt < 5; ++nt) own[nt] = w == 0 ? acc[nt][0] : (w == 1 ? acc[nt][1] : (w == 2 ? acc[nt][2] : acc[nt][3]));
  {
    const int j = lane & 31, c2 = lane & 7;
    const unsigned fa = WS_FA + (8 * (4 * (j & 7) + (c.ct & 3)) + 2 * (j >> 3) + c.cg) * 128;
    const unsigned ft = WS_FT + (8 * (4 * (c.p64 & 7) + (c2 & 3)) + 2 * c.rg + (c2 >> 2)) * 128;
    wait_flags(p.ws, lane < 32 ? fa : ft, c.tag);
  }
  STAMP(10);
#pragma unroll
  for (int i = 0; i < 3; ++i) {
    const int idx = w + 4 * i;
    if (idx < 10) DMA_SC1(L_AT + idx * 1024, (unsigned)((lane >> 3) * 4096 + (lane & 7) * 16), c.rws, (unsigned)(WS_AT + (c.ct * 80 + 8 * idx) * 4096 + c.p64 * 128));
  }
  float gs;
  {
    const unsigned go = WS_GP + c.p64 * 2048;
    const float g0 = __uint_as_float(__builtin_amdgcn_raw_buffer_load_b32(c.rws, (2 * q) * 256 + (16 * w + l15) * 4, go, 16));
    const float g1v = __uint_as_float(__builtin_amdgcn_raw_buffer_load_b32(c.rws, (2 * q + 1) * 256 + (16 * w + l15) * 4, go, 16));
    gs = g0 + g1v; gs += __shfl_xor(gs, 16); gs += __shfl_xor(gs, 32);
  }
  gs = rbf(gs);
  const float gate = rbf(1.f / (1.f + __expf(-gs))), gate1 = rbf(1.f - gate);
  WAITV(0);
  BAR();
  {
    unsigned o[5][2];
#pragma unroll
    for (int nt = 0; nt < 5; ++nt) {
      f32x4 v = own[nt];
#pragma unroll
      for (int sw = 1; sw < 4; ++sw) v += __builtin_bit_cast(f32x4, ld128(L_RED + ((w * 4 + ((w + sw) & 3)) * 5 + nt) * 1024 + lane * 16));
      u32x2 av = __builtin_bit_cast(u32x2, trd(L_AT + (16 * nt + 4 * q + (l15 >> 2)) * 128 + (16 * w + 4 * (lane & 3)) * 2));
      const float a0 = bf2f(av.x & 0xffffu), a1 = __uint_as_float(av.x & 0xffff0000u), a2 = bf2f(av.y & 0xffffu), a3 = __uint_as_float(av.y & 0xffff0000u);
      const float c0 = bf2f(b2v[nt].x & 0xffffu), c1 = __uint_as_float(b2v[nt].x & 0xffff0000u), c2 = bf2f(b2v[nt].y & 0xffffu), c3 = __uint_as_float(b2v[nt].y & 0xffff0000u);
      o[nt][0] = pk(gate * a0 + gate1 * (v[0] + c0), gate * a1 + gate1 * (v[1] + c1));
      o[nt][1] = pk(gate * a2 + gate1 * (v[2] + c2), gate * a3 + gate1 * (v[3] + c3));
    }
    char* orow = (char*)p.out + (size_t)(c.p64 * 64 + 16 * w + l15) * 1280 + c.ct * 160;
#pragma unroll
    for (int nt = 0; nt < 4; nt += 2) {
      auto r0 = __builtin_amdgcn_permlane16_swap(o[nt][0], o[nt + 1][0], false, false);
      auto r1 = __builtin_amdgcn_permlane16_swap(o[nt][1], o[nt + 1][1], false, false);
      *(u32x4*)(orow + 32 * (nt + (q & 1)) + 16 * (q >> 1)) = (u32x4){r0[0], r1[0], r0[1], r1[1]};
    }
    *(u32x2*)(orow + 128 + 8 * q) = (u32x2){o[4][0], o[4][1]};
  }
  STAMP(11);
  if (c.b == 0 && w == 0) {
#pragma unroll
    for (int i = 0; i < 4; ++i) wait_flags(p.ws, WS_FA + (64 * i + lane) * 128, c.tag);
    if (lane == 0) set_flag(p.ws, WS_EP, c.tag);
  }
}

extern "C" void kernel_launch(void* const* d_in, const int* in_sizes, int n_in, void* d_out, int out_size, void* d_ws, size_t ws_size, hipStream_t stream) {
  Params p;
  p.u = (const u16*)d_in[0]; p.lar = (const u16*)d_in[1]; p.aim = (const u16*)d_in[2]; p.cr = (const u16*)d_in[3]; p.ci = (const u16*)d_in[4];
  p.ldt = (const u16*)d_in[5]; p.dsk = (const u16*)d_in[6]; p.w1 = (const u16*)d_in[8]; p.b1 = (const u16*)d_in[9];
  p.w2 = (const u16*)d_in[10]; p.b2 = (const u16*)d_in[11]; p.gw = (const u16*)d_in[12];
  p.out = (u16*)d_out; p.ws = (char*)d_ws;
  hipFuncSetAttribute((const void*)fused, hipFuncAttributeMaxDynamicSharedMemorySize, L_TOTAL);
  fused<<<256, 256, L_TOTAL, stream>>>(p);
}
```

```cpp
#include <hip/hip_runtime.h>
#include <stdint.h>

struct Srd { const void* p; unsigned bytes; unsigned fmt; };
struct Params {
  Srd x, w1, w2, ws;
  const void *lar, *aim, *cr, *ci, *ldt, *dsk, *b1, *b2, *gw;
  void* out;
};

__global__ void __launch_bounds__(256, 1) fused(Params p) {
  asm volatile(
      "s_load_dwordx16 s[4:19], %0, 0x0\n"
      "s_load_dwordx16 s[20:35], %0, 0x40\n"
      "s_load_dwordx4 s[36:39], %0, 0x80\n"
      "s_getpc_b64 s[74:75]\n"
      ".Lpc:\n"
      "s_add_u32 s42, %1, 224\n"
      "s_and_b32 s42, s42, 255\n"
      "v_mov_b32 v0, %2\n"
      "s_lshr_b32 s45, s42, 3\n"
      "v_lshlrev_b32 v40, 12, v0\n"
      "s_lshl_b32 s68, s45, 7\n"
      "v_add_u32 v40, s68, v40\n"
      "v_cmp_gt_u32 vcc, .Lcode_end-.Lpc, v40\n"
      "s_nop 0\n"
      "s_and_saveexec_b64 s[76:77], vcc\n"
      "s_cbranch_execz .Lpf_1\n"
      "global_load_dword v226, v40, s[74:75]\n"
      ".Lpf_1:\n"
      "s_mov_b64 exec, -1\n"
      "v_lshrrev_b32 v41, 6, v0\n"
      "v_and_b32 v1, 63, v0\n"
      "v_readfirstlane_b32 s43, v41\n"
      "s_and_b32 s44, s42, 7\n"
      "v_lshrrev_b32 v5, 4, v1\n"
      "v_and_b32 v4, 15, v1\n"
      "v_lshlrev_b32 v2, 4, v1\n"
      "v_lshrrev_b32 v40, 3, v1\n"
      "v_and_b32 v42, 7, v1\n"
      "v_mul_u32_u24 v40, 1280, v40\n"
      "v_lshl_add_u32 v56, v42, 4, v40\n"
      "s_and_b32 s50, s43, 1\n"
      "s_lshr_b32 s51, s43, 1\n"
      "s_cmp_ge_u32 s45, 28\n"
      "s_cbranch_scc1 .Lrole_s\n"
      "v_xor_b32 v43, v42, v5\n"
      "v_lshl_add_u32 v48, v43, 4, v40\n"
      "s_mul_i32 s68, s45, 37\n"
      "s_lshr_b32 s68, s68, 8\n"
      "s_mul_i32 s69, s68, 7\n"
      "s_sub_u32 s69, s45, s69\n"
      "s_and_b32 s70, s44, 6\n"
      "s_lshl_b32 s48, s70, 1\n"
      "s_add_u32 s48, s48, s68\n"
      "v_xor_b32 v49, 64, v48\n"
      "v_add_u32 v49, 9216, v49\n"
      "s_and_b32 s71, s44, 1\n"
      "s_mul_i32 s49, s71, 7\n"
      "s_add_u32 s49, s49, s69\n"
      "v_add_u32 v50, 18432, v48\n"
      "v_add_u32 v51, 18432, v49\n"
      "s_mul_i32 s68, s68, 2\n"
      "s_add_u32 s52, s49, s68\n"
      "s_sub_u32 s68, s52, 10\n"
      "s_cmp_ge_u32 s52, 10\n"
      "s_cselect_b32 s52, s68, s52\n"
      "s_sub_u32 s68, s52, 10\n"
      "s_cmp_ge_u32 s52, 10\n"
      "s_cselect_b32 s52, s68, s52\n"
      "s_mov_b32 s59, s52\n"
      "s_cmp_ge_u32 s49, 10\n"
      "s_cselect_b32 s101, 9, 0\n"
      "v_lshlrev_b32 v43, 2, v5\n"
      "v_xor_b32 v43, v43, v4\n"
      "v_mul_u32_u24 v44, 3456, v5\n"
      "v_lshl_add_u32 v52, v43, 4, v44\n"
      "s_lshl_b32 s68, s48, 2\n"
      "s_add_u32 s68, s68, s43\n"
      "s_mul_i32 s55, s68, 40960\n"
      "s_bfe_u32 s70, s44, 0x10001\n"
      "s_and_b32 s70, s70, s71\n"
      "s_lshl_b32 s58, s49, 1\n"
      "s_sub_u32 s58, s58, s70\n"
      "v_add_u32 v53, 12800, v52\n"
      "v_add_u32 v54, 25600, v52\n"
      "v_add_u32 v55, 38400, v52\n"
      "s_mul_i32 s56, s43, 55296\n"
      "s_lshl_b32 s68, s58, 7\n"
      "s_add_u32 s56, s56, s68\n"
      "s_lshl_b32 s57, s43, 12\n"
      "s_waitcnt lgkmcnt(0)\n"
      "s_lshr_b32 s68, s45, 2\n"
      "s_and_b32 s46, s44, 6\n"
      "s_lshl_b32 s46, s46, 2\n"
      "s_add_u32 s46, s46, s68\n"
      "s_and_b32 s47, s44, 1\n"
      "s_lshl_b32 s47, s47, 2\n"
      "s_and_b32 s68, s45, 3\n"
      "s_add_u32 s47, s47, s68\n"
      "s_mul_i32 s63, s44, 80\n"
      "s_add_u32 s63, s63, s51\n"
      "v_add_u32 v3, 4096, v2\n"
      "s_lshl_b32 s69, s45, 1\n"
      "s_add_u32 s63, s63, s69\n"
      "s_lshl_b32 s98, s63, 8\n"
      "s_lshl_b32 s99, s50, 7\n"
      "s_add_u32 s98, s98, s99\n"
      "v_lshl_add_u32 v46, v1, 1, s98\n"
      "s_lshl_b32 s99, s63, 1\n"
      "v_mov_b32 v47, s99\n"
      "global_load_ushort v6, v46, s[20:21] nt\n"
      "global_load_ushort v7, v46, s[22:23] nt\n"
      "global_load_ushort v8, v46, s[24:25] nt\n"
      "global_load_ushort v9, v46, s[26:27] nt\n"
      "global_load_ushort v10, v47, s[28:29]\n"
      "global_load_ushort v11, v47, s[30:31]\n"
      "s_lshl_b32 s98, s52, 7\n"
      "s_add_u32 s53, s55, s98\n"
      "s_add_u32 m0, s57, 0\n"
      "s_nop 0\n"
      "buffer_load_dwordx4 v48, s[4:7], s53 offen lds\n"
      "buffer_load_dwordx4 v49, s[4:7], s53 offen offset:1024 lds\n"
      "buffer_load_dwordx4 v50, s[4:7], s53 offen offset:2048 lds\n"
      "buffer_load_dwordx4 v51, s[4:7], s53 offen offset:3072 lds\n"
      "s_lshl_b32 s68, s48, 2\n"
      "s_add_u32 s68, s68, s49\n"
      "s_sub_u32 s68, s68, 10\n"
      "s_lshl_b32 s68, s68, 1\n"
      "s_mul_i32 s69, s48, 10\n"
      "s_add_u32 s69, s69, s49\n"
      "s_add_u32 s69, s69, 128\n"
      "s_cmp_eq_u32 s101, 9\n"
      "s_cselect_b32 s68, s68, s69\n"
      "s_mul_i32 s69, s68, 2428\n"
      "s_lshr_b32 s69, s69, 16\n"
      "s_mul_i32 s70, s69, 27\n"
      "s_sub_u32 s70, s68, s70\n"
      "s_lshl_b32 s72, s70, 2\n"
      "s_add_u32 s72, s72, s43\n"
      "s_mul_i32 s72, s72, 20480\n"
      "s_lshl_b32 s71, s69, 7\n"
      "s_add_u32 s93, s72, s71\n"
      "s_mul_i32 s70, s70, 80\n"
      "s_lshl_b32 s71, s69, 2\n"
      "s_add_u32 s70, s70, s71\n"
      "s_add_u32 s70, s70, s43\n"
      "s_lshl_b32 s70, s70, 10\n"
      "s_add_u32 s92, s70, 3145728\n"
      "s_cmp_ge_u32 s69, 10\n"
      "s_cselect_b32 s92, 2146435072, s92\n"
      "s_cselect_b32 s93, 2146435072, s93\n"
      "s_lshl_b32 s98, s43, 11\n"
      "s_add_u32 m0, s98, 144384\n"
      "s_nop 0\n"
      "buffer_load_dwordx4 v56, s[12:15], s93 offen nt lds\n"
      "s_add_u32 s98, s93, 9216\n"
      "buffer_load_dwordx4 v56, s[12:15], s98 offen offset:1024 nt lds\n"
      "s_cmp_lg_u32 s101, 9\n"
      "s_cbranch_scc1 .Lnw_2\n"
      "s_add_u32 s68, s68, 1\n"
      "s_mul_i32 s69, s68, 2428\n"
      "s_lshr_b32 s69, s69, 16\n"
      "s_mul_i32 s70, s69, 27\n"
      "s_sub_u32 s70, s68, s70\n"
      "s_lshl_b32 s72, s70, 2\n"
      "s_add_u32 s72, s72, s43\n"
      "s_mul_i32 s72, s72, 20480\n"
      "s_lshl_b32 s71, s69, 7\n"
      "s_add_u32 s89, s72, s71\n"
      "s_mul_i32 s70, s70, 80\n"
      "s_lshl_b32 s71, s69, 2\n"
      "s_add_u32 s70, s70, s71\n"
      "s_add_u32 s70, s70, s43\n"
      "s_lshl_b32 s70, s70, 10\n"
      "s_add_u32 s88, s70, 3145728\n"
      "s_cmp_ge_u32 s69, 10\n"
      "s_cselect_b32 s88, 2146435072, s88\n"
      "s_cselect_b32 s89, 2146435072, s89\n"
      "s_lshl_b32 s98, s43, 11\n"
      "s_add_u32 m0, s98, 152576\n"
      "s_nop 0\n"
      "buffer_load_dwordx4 v56, s[12:15], s89 offen nt lds\n"
      "s_add_u32 s98, s89, 9216\n"
      "buffer_load_dwordx4 v56, s[12:15], s98 offen offset:1024 nt lds\n"
      ".Lnw_2:\n"
      "s_lshl_b32 s68, s58, 7\n"
      "s_lshl_b32 s69, s59, 7\n"
      "s_cmp_eq_u32 s50, 0\n"
      "s_cselect_b32 s68, s68, s69\n"
      "s_cselect_b32 s69, 2176, 0\n"
      "s_add_u32 s70, s69, 1264\n"
      "s_cmp_eq_u32 s50, 0\n"
      "s_cselect_b64 s[76:77], s[32:33], s[36:37]\n"
      "v_add_u32 v40, s68, v2\n"
      "v_min_u32 v40, s70, v40\n"
      "s_lshl_b32 s68, s50, 10\n"
      "s_add_u32 m0, s68, 160768\n"
      "s_mov_b64 exec, 0xffff\n"
      "global_load_lds_dwordx4 v40, s[76:77]\n"
      "s_mov_b64 exec, -1\n"
      "s_load_dwordx2 s[40:41], s[16:17], 98304\n"
      "v_lshrrev_b32 v40, 5, v1\n"
      "v_lshrrev_b32 v41, 2, v4\n"
      "v_lshl_add_u32 v42, v40, 3, v41\n"
      "v_lshlrev_b32 v42, 8, v42\n"
      "v_and_b32 v43, 1, v1\n"
      "v_lshl_add_u32 v42, v43, 3, v42\n"
      "v_and_b32 v43, 1, v5\n"
      "v_bfe_u32 v44, v1, 1, 1\n"
      "v_lshl_or_b32 v43, v43, 1, v44\n"
      "s_lshl_b32 s68, s50, 3\n"
      "v_or_b32 v43, s68, v43\n"
      "v_lshlrev_b32 v41, 2, v41\n"
      "v_xor_b32 v43, v43, v41\n"
      "v_lshl_add_u32 v42, v43, 4, v42\n"
      "v_add_u32 v208, 16384, v42\n"
      "v_xor_b32 v209, 64, v208\n"
      "v_add_u32 v210, 0x10000, v208\n"
      "v_add_u32 v211, 0x10000, v209\n"
      "v_and_b32 v41, 31, v1\n"
      "v_bfe_u32 v42, v1, 1, 3\n"
      "v_xor_b32 v42, v42, v40\n"
      "s_lshl_b32 s68, s51, 13\n"
      "v_lshl_add_u32 v41, v41, 7, s68\n"
      "v_lshl_add_u32 v212, v42, 4, v41\n"
      "v_xor_b32 v213, 32, v212\n"
      "v_xor_b32 v214, 64, v212\n"
      "v_xor_b32 v215, 96, v212\n"
      "v_add_u32 v216, 0x10000, v212\n"
      "v_add_u32 v217, 0x10000, v213\n"
      "v_add_u32 v218, 0x10000, v214\n"
      "v_add_u32 v219, 0x10000, v215\n"
      "s_lshl_b32 s68, s50, 7\n"
      "s_add_u32 s68, s68, 160768\n"
      "v_lshl_add_u32 v45, v40, 3, s68\n"
      "v_lshrrev_b32 v40, 2, v4\n"
      "v_lshl_add_u32 v41, v5, 3, v40\n"
      "v_bfe_u32 v42, v1, 1, 1\n"
      "v_lshl_add_u32 v41, v42, 5, v41\n"
      "s_lshl_b32 s68, s50, 13\n"
      "v_lshl_add_u32 v41, v41, 7, s68\n"
      "v_and_b32 v42, 1, v1\n"
      "v_lshl_add_u32 v41, v42, 3, v41\n"
      "v_lshrrev_b32 v40, 1, v40\n"
      "v_and_b32 v43, 1, v5\n"
      "v_lshl_or_b32 v40, v43, 2, v40\n"
      "s_lshl_b32 s68, s51, 2\n"
      "v_xor_b32 v40, s68, v40\n"
      "v_lshl_add_u32 v31, v40, 4, v41\n"
      "v_xor_b32 v32, 16, v31\n"
      "v_xor_b32 v33, 32, v31\n"
      "v_xor_b32 v34, 48, v31\n"
      "v_lshlrev_b32 v36, 6, v0\n"
      "v_bfe_u32 v40, v0, 2, 3\n"
      "v_lshlrev_b32 v42, 2, v42\n"
      "v_xor_b32 v40, v40, v42\n"
      "v_lshlrev_b32 v40, 4, v40\n"
      "v_add_u32 v14, 161792, v40\n"
      "v_xor_b32 v15, 16, v14\n"
      "v_xor_b32 v16, 32, v14\n"
      "v_xor_b32 v17, 48, v14\n"
      "v_add_u32 v58, 512, v33\n"
      "v_add_u32 v59, 512, v34\n"
      "v_add_u32 v60, 512, v31\n"
      "v_add_u32 v61, 512, v32\n"
      "s_mul_i32 s68, s43, 17408\n"
      "s_add_u32 s68, s68, 65536\n"
      "v_mul_u32_u24 v40, 272, v1\n"
      "v_add_u32 v22, s68, v40\n"
      "v_add_u32 v28, s68, v2\n"
      "v_lshrrev_b32 v40, 1, v5\n"
      "v_and_b32 v41, 1, v5\n"
      "v_lshl_or_b32 v40, v40, 1, v41\n"
      "v_bfe_u32 v41, v1, 3, 1\n"
      "v_lshl_or_b32 v40, v40, 1, v41\n"
      "v_mul_u32_u24 v40, 544, v40\n"
      "v_bfe_u32 v41, v1, 2, 1\n"
      "v_lshl_add_u32 v40, v41, 7, v40\n"
      "v_and_b32 v41, 3, v1\n"
      "v_lshl_add_u32 v40, v41, 3, v40\n"
      "v_add_u32 v23, s68, v40\n"
      "v_lshlrev_b32 v40, 4, v4\n"
      "v_lshl_add_u32 v41, v5, 8, v40\n"
      "v_sub_u32 v41, 3568, v41\n"
      "v_lshl_add_u32 v42, v5, 9, v40\n"
      "v_sub_u32 v42, 3824, v42\n"
      "v_cmp_gt_u32 vcc, 2, v5\n"
      "s_nop 1\n"
      "v_cndmask_b32 v41, v42, v41, vcc\n"
      "v_lshl_add_u32 v42, v5, 10, v40\n"
      "v_sub_u32 v42, 1264, v42\n"
      "s_cmp_lg_u32 s50, 0\n"
      "s_cselect_b64 vcc, -1, 0\n"
      "v_cndmask_b32 v24, v42, v41, vcc\n"
      "v_lshrrev_b32 v41, 1, v5\n"
      "v_lshl_add_u32 v41, v41, 8, v40\n"
      "v_and_b32 v42, 1, v5\n"
      "v_lshl_add_u32 v41, v42, 3, v41\n"
      "v_sub_u32 v25, 4088, v41\n"
      "v_lshrrev_b32 v43, 1, v1\n"
      "v_min_u32 v43, 1, v43\n"
      "s_mul_i32 s65, s51, 4608\n"
      "s_add_u32 s65, s65, 135168\n"
      "s_mov_b32 s66, 65536\n"
      "v_lshl_add_u32 v37, v43, 27, v3\n"
      "v_and_b32 v44, 7, v1\n"
      "v_mul_u32_u24 v44, 4224, v44\n"
      "v_lshl_add_u32 v44, v5, 4, v44\n"
      "v_bfe_u32 v43, v1, 3, 1\n"
      "v_lshl_add_u32 v35, v43, 6, v44\n"
      "s_waitcnt lgkmcnt(0)\n"
      "s_add_u32 s40, s40, 1\n"
      "s_addc_u32 s41, s41, 0\n"
      "v_lshrrev_b32 v46, 1, v5\n"
      "v_and_b32 v47, 1, v5\n"
      "v_lshlrev_b32 v46, 2, v46\n"
      "v_add_u32 v46, v46, v47\n"
      "v_lshrrev_b32 v47, 2, v4\n"
      "v_lshl_add_u32 v46, v46, 2, v47\n"
      "v_and_b32 v47, 3, v1\n"
      "v_lshlrev_b32 v47, 3, v47\n"
      "v_lshl_add_u32 v46, v46, 7, v47\n"
      "s_lshl_b32 s98, s43, 5\n"
      "s_add_u32 s98, s98, 144384\n"
      "v_add_u32 v39, s98, v46\n"
      "s_add_u32 s93, s92, 40960\n"
      "s_cmp_eq_u32 s101, 9\n"
      "s_cbranch_scc1 .Ljw_3\n"
      "s_lshl_b32 s71, s59, 1\n"
      "s_add_u32 s71, s71, s51\n"
      "s_mul_i32 s71, s71, 135168\n"
      "s_lshl_b32 s69, s48, 1\n"
      "s_add_u32 s69, s69, s50\n"
      "s_lshl_b32 s69, s69, 7\n"
      "s_add_u32 s71, s71, s69\n"
      "s_add_u32 s88, s71, 262272\n"
      "s_mul_i32 s70, s48, 10\n"
      "s_add_u32 s70, s70, s59\n"
      "s_lshl_b32 s70, s70, 9\n"
      "s_add_u32 s67, s70, 131072\n"
      "s_add_u32 s89, s88, 33792\n"
      "s_add_u32 s90, s88, 67584\n"
      "s_add_u32 s91, s88, 101376\n"
      "s_branch .Ljj_4\n"
      ".Ljw_3:\n"
      "v_add_u32 v31, 8192, v39\n"
      "v_add_u32 v32, 12288, v39\n"
      "v_add_u32 v33, 8192, v39\n"
      "v_add_u32 v34, 12288, v39\n"
      "v_add_u32 v58, 1024, v31\n"
      "v_add_u32 v59, 1024, v32\n"
      "v_add_u32 v60, 1024, v33\n"
      "v_add_u32 v61, 1024, v34\n"
      "v_mov_b32 v35, v2\n"
      "s_add_u32 s89, s88, 40960\n"
      "s_mov_b32 s90, 2146435072\n"
      "s_mov_b32 s91, 2146435072\n"
      "s_mov_b32 s67, 2146435072\n"
      ".Ljj_4:\n"
      "v_lshlrev_b32 v44, 1, v0\n"
      "v_and_b32 v44, -4, v44\n"
      "s_mul_i32 s98, s52, 221184\n"
      "s_add_u32 s54, s56, s98\n"
      "s_add_u32 m0, s57, 16384\n"
      "s_nop 0\n"
      "buffer_load_dwordx4 v52, s[8:11], s54 offen lds\n"
      "buffer_load_dwordx4 v53, s[8:11], s54 offen offset:1024 lds\n"
      "buffer_load_dwordx4 v54, s[8:11], s54 offen offset:2048 lds\n"
      "buffer_load_dwordx4 v55, s[8:11], s54 offen offset:3072 lds\n"
      "s_add_u32 s52, s52, 1\n"
      "s_cmp_eq_u32 s52, 10\n"
      "s_cselect_b32 s52, 0, s52\n"
      "s_waitcnt vmcnt(12)\n"
      "v_lshlrev_b32 v64, 16, v10\n"
      "v_mul_f32 v65, 0x3fb8aa00, v64\n"
      "v_mul_f32 v66, 0x36eca570, v64\n"
      "v_rndne_f32 v67, v65\n"
      "v_sub_f32 v65, v65, v67\n"
      "v_add_f32 v65, v65, v66\n"
      "v_exp_f32 v65, v65\n"
      "v_cvt_i32_f32 v67, v67\n"
      "v_ldexp_f32 v65, v65, v67\n"
      "v_cvt_pk_bf16_f32 v65, v65, v65\n"
      "v_lshlrev_b32 v68, 16, v65\n"
      "v_lshlrev_b32 v64, 16, v6\n"
      "v_mul_f32 v65, 0x3fb8aa00, v64\n"
      "v_mul_f32 v66, 0x36eca570, v64\n"
      "v_rndne_f32 v67, v65\n"
      "v_sub_f32 v65, v65, v67\n"
      "v_add_f32 v65, v65, v66\n"
      "v_exp_f32 v65, v65\n"
      "v_cvt_i32_f32 v67, v67\n"
      "v_ldexp_f32 v65, v65, v67\n"
      "v_cvt_pk_bf16_f32 v65, v65, v65\n"
      "v_lshlrev_b32 v69, 16, v65\n"
      "v_mul_f32 v70, v69, v68\n"
      "v_mul_f32 v70, 0xbfb8aa3b, v70\n"
      "v_lshlrev_b32 v71, 16, v7\n"
      "v_mul_f32 v71, v71, v68\n"
      "v_mul_f32 v71, 0x3e22f983, v71\n"
      "v_mul_f32 v64, 0x41000000, v70\n"
      "v_mul_f32 v65, 0x41000000, v71\n"
      "v_fract_f32 v66, v65\n"
      "v_exp_f32 v64, v64\n"
      "v_cos_f32 v90, v66\n"
      "v_sin_f32 v91, v66\n"
      "v_mul_f32 v90, v64, v90\n"
      "v_mul_f32 v91, v64, v91\n"
      "v_mov_b32 v88, 1.0\n"
      "v_mov_b32 v89, 0\n"
      "v_mov_b64 v[78:79], v[90:91]\n"
      "v_pk_mul_f32 v[82:83], v[90:91], v[78:79] op_sel:[0,0] op_sel_hi:[0,1]\n"
      "v_pk_fma_f32 v[92:93], v[90:91], v[78:79], v[82:83] op_sel:[1,1,0] op_sel_hi:[1,0,1] neg_lo:[1,0,0]\n"
      "v_pk_mul_f32 v[82:83], v[92:93], v[78:79] op_sel:[0,0] op_sel_hi:[0,1]\n"
      "v_pk_fma_f32 v[94:95], v[92:93], v[78:79], v[82:83] op_sel:[1,1,0] op_sel_hi:[1,0,1] neg_lo:[1,0,0]\n"
      "v_pk_mul_f32 v[82:83], v[94:95], v[78:79] op_sel:[0,0] op_sel_hi:[0,1]\n"
      "v_pk_fma_f32 v[96:97], v[94:95], v[78:79], v[82:83] op_sel:[1,1,0] op_sel_hi:[1,0,1] neg_lo:[1,0,0]\n"
      "v_pk_mul_f32 v[82:83], v[96:97], v[78:79] op_sel:[0,0] op_sel_hi:[0,1]\n"
      "v_pk_fma_f32 v[98:99], v[96:97], v[78:79], v[82:83] op_sel:[1,1,0] op_sel_hi:[1,0,1] neg_lo:[1,0,0]\n"
      "v_pk_mul_f32 v[82:83], v[98:99], v[78:79] op_sel:[0,0] op_sel_hi:[0,1]\n"
      "v_pk_fma_f32 v[100:101], v[98:99], v[78:79], v[82:83] op_sel:[1,1,0] op_sel_hi:[1,0,1] neg_lo:[1,0,0]\n"
      "v_pk_mul_f32 v[82:83], v[100:101], v[78:79] op_sel:[0,0] op_sel_hi:[0,1]\n"
      "v_pk_fma_f32 v[102:103], v[100:101], v[78:79], v[82:83] op_sel:[1,1,0] op_sel_hi:[1,0,1] neg_lo:[1,0,0]\n"
      "v_pk_mul_f32 v[82:83], v[102:103], v[78:79] op_sel:[0,0] op_sel_hi:[0,1]\n"
      "v_pk_fma_f32 v[104:105], v[102:103], v[78:79], v[82:83] op_sel:[1,1,0] op_sel_hi:[1,0,1] neg_lo:[1,0,0]\n"
      "v_cvt_pk_bf16_f32 v120, v88, v90\n"
      "v_cvt_pk_bf16_f32 v121, v92, v94\n"
      "v_cvt_pk_bf16_f32 v122, v96, v98\n"
      "v_cvt_pk_bf16_f32 v123, v100, v102\n"
      "ds_write_b128 v22, v[120:123] offset:96\n"
      "v_cvt_pk_bf16_f32 v124, -v89, -v91\n"
      "v_cvt_pk_bf16_f32 v125, -v93, -v95\n"
      "v_cvt_pk_bf16_f32 v126, -v97, -v99\n"
      "v_cvt_pk_bf16_f32 v127, -v101, -v103\n"
      "ds_write_b128 v22, v[124:127] offset:224\n"
      "v_pk_mul_f32 v[82:83], v[104:105], v[78:79] op_sel:[0,0] op_sel_hi:[0,1]\n"
      "v_pk_fma_f32 v[106:107], v[104:105], v[78:79], v[82:83] op_sel:[1,1,0] op_sel_hi:[1,0,1] neg_lo:[1,0,0]\n"
      "v_pk_mul_f32 v[82:83], v[106:107], v[78:79] op_sel:[0,0] op_sel_hi:[0,1]\n"
      "v_pk_fma_f32 v[108:109], v[106:107], v[78:79], v[82:83] op_sel:[1,1,0] op_sel_hi:[1,0,1] neg_lo:[1,0,0]\n"
      "v_pk_mul_f32 v[82:83], v[108:109], v[78:79] op_sel:[0,0] op_sel_hi:[0,1]\n"
      "v_pk_fma_f32 v[110:111], v[108:109], v[78:79], v[82:83] op_sel:[1,1,0] op_sel_hi:[1,0,1] neg_lo:[1,0,0]\n"
      "v_pk_mul_f32 v[82:83], v[110:111], v[78:79] op_sel:[0,0] op_sel_hi:[0,1]\n"
      "v_pk_fma_f32 v[112:113], v[110:111], v[78:79], v[82:83] op_sel:[1,1,0] op_sel_hi:[1,0,1] neg_lo:[1,0,0]\n"
      "v_pk_mul_f32 v[82:83], v[112:113], v[78:79] op_sel:[0,0] op_sel_hi:[0,1]\n"
      "v_pk_fma_f32 v[114:115], v[112:113], v[78:79], v[82:83] op_sel:[1,1,0] op_sel_hi:[1,0,1] neg_lo:[1,0,0]\n"
      "v_pk_mul_f32 v[82:83], v[114:115], v[78:79] op_sel:[0,0] op_sel_hi:[0,1]\n"
      "v_pk_fma_f32 v[116:117], v[114:115], v[78:79], v[82:83] op_sel:[1,1,0] op_sel_hi:[1,0,1] neg_lo:[1,0,0]\n"
      "v_pk_mul_f32 v[82:83], v[116:117], v[78:79] op_sel:[0,0] op_sel_hi:[0,1]\n"
      "v_pk_fma_f32 v[118:119], v[116:117], v[78:79], v[82:83] op_sel:[1,1,0] op_sel_hi:[1,0,1] neg_lo:[1,0,0]\n"
      "v_fract_f32 v66, v71\n"
      "v_exp_f32 v64, v70\n"
      "v_cos_f32 v72, v66\n"
      "v_sin_f32 v73, v66\n"
      "v_mul_f32 v72, v64, v72\n"
      "v_mul_f32 v73, v64, v73\n"
      "v_cvt_pk_bf16_f32 v128, v104, v106\n"
      "v_cvt_pk_bf16_f32 v129, v108, v110\n"
      "v_cvt_pk_bf16_f32 v130, v112, v114\n"
      "v_cvt_pk_bf16_f32 v131, v116, v118\n"
      "ds_write_b128 v22, v[128:131] offset:112\n"
      "v_cvt_pk_bf16_f32 v132, -v105, -v107\n"
      "v_cvt_pk_bf16_f32 v133, -v109, -v111\n"
      "v_cvt_pk_bf16_f32 v134, -v113, -v115\n"
      "v_cvt_pk_bf16_f32 v135, -v117, -v119\n"
      "ds_write_b128 v22, v[132:135] offset:240\n"
      "v_mul_f32 v64, 0x43000000, v70\n"
      "v_mul_f32 v65, 0x43000000, v71\n"
      "v_fract_f32 v66, v65\n"
      "v_exp_f32 v64, v64\n"
      "v_cos_f32 v80, v66\n"
      "v_sin_f32 v81, v66\n"
      "v_mul_f32 v80, v64, v80\n"
      "v_mul_f32 v81, v64, v81\n"
      "v_add_f32 v68, v68, v68\n"
      "v_lshlrev_b32 v88, 16, v8\n"
      "v_lshlrev_b32 v89, 16, v9\n"
      "v_mul_f32 v88, v88, v68\n"
      "v_mul_f32 v89, v89, v68\n"
      "s_waitcnt vmcnt(11)\n"
      "v_lshlrev_b32 v84, 16, v11\n"
      "v_pk_mul_f32 v[82:83], v[88:89], v[80:81] op_sel:[0,0] op_sel_hi:[0,1]\n"
      "ds_read_b64_tr_b16 v[148:149], v23 offset:96\n"
      "v_pk_fma_f32 v[104:105], v[88:89], v[80:81], v[82:83] op_sel:[1,1,0] op_sel_hi:[1,0,1] neg_lo:[1,0,0]\n"
      "v_pk_mul_f32 v[82:83], v[88:89], v[72:73] op_sel:[0,0] op_sel_hi:[0,1]\n"
      "v_pk_fma_f32 v[90:91], v[88:89], v[72:73], v[82:83] op_sel:[1,1,0] op_sel_hi:[1,0,1] neg_lo:[1,0,0]\n"
      "v_pk_mul_f32 v[82:83], v[90:91], v[72:73] op_sel:[0,0] op_sel_hi:[0,1]\n"
      "v_pk_fma_f32 v[92:93], v[90:91], v[72:73], v[82:83] op_sel:[1,1,0] op_sel_hi:[1,0,1] neg_lo:[1,0,0]\n"
      "v_pk_mul_f32 v[82:83], v[92:93], v[72:73] op_sel:[0,0] op_sel_hi:[0,1]\n"
      "ds_read_b64_tr_b16 v[150:151], v23 offset:368\n"
      "v_pk_fma_f32 v[94:95], v[92:93], v[72:73], v[82:83] op_sel:[1,1,0] op_sel_hi:[1,0,1] neg_lo:[1,0,0]\n"
      "v_pk_mul_f32 v[82:83], v[94:95], v[72:73] op_sel:[0,0] op_sel_hi:[0,1]\n"
      "v_pk_fma_f32 v[96:97], v[94:95], v[72:73], v[82:83] op_sel:[1,1,0] op_sel_hi:[1,0,1] neg_lo:[1,0,0]\n"
      "v_pk_mul_f32 v[82:83], v[96:97], v[72:73] op_sel:[0,0] op_sel_hi:[0,1]\n"
      "v_pk_fma_f32 v[98:99], v[96:97], v[72:73], v[82:83] op_sel:[1,1,0] op_sel_hi:[1,0,1] neg_lo:[1,0,0]\n"
      "v_pk_mul_f32 v[82:83], v[98:99], v[72:73] op_sel:[0,0] op_sel_hi:[0,1]\n"
      "v_pk_fma_f32 v[100:101], v[98:99], v[72:73], v[82:83] op_sel:[1,1,0] op_sel_hi:[1,0,1] neg_lo:[1,0,0]\n"
      "ds_read_b64_tr_b16 v[152:153], v23 offset:4448\n"
      "v_pk_mul_f32 v[82:83], v[100:101], v[72:73] op_sel:[0,0] op_sel_hi:[0,1]\n"
      "v_pk_fma_f32 v[102:103], v[100:101], v[72:73], v[82:83] op_sel:[1,1,0] op_sel_hi:[1,0,1] neg_lo:[1,0,0]\n"
      "v_cvt_pk_bf16_f32 v120, v88, v90\n"
      "v_cvt_pk_bf16_f32 v121, v92, v94\n"
      "v_cvt_pk_bf16_f32 v122, v96, v98\n"
      "v_cvt_pk_bf16_f32 v123, v100, v102\n"
      "ds_write_b128 v22, v[120:123] offset:0\n"
      "ds_read_b64_tr_b16 v[154:155], v23 offset:4720\n"
      "v_cvt_pk_bf16_f32 v124, v89, v91\n"
      "v_cvt_pk_bf16_f32 v125, v93, v95\n"
      "v_cvt_pk_bf16_f32 v126, v97, v99\n"
      "v_cvt_pk_bf16_f32 v127, v101, v103\n"
      "ds_write_b128 v22, v[124:127] offset:128\n"
      "v_pk_mul_f32 v[82:83], v[104:105], v[80:81] op_sel:[0,0] op_sel_hi:[0,1]\n"
      "v_pk_fma_f32 v[88:89], v[104:105], v[80:81], v[82:83] op_sel:[1,1,0] op_sel_hi:[1,0,1] neg_lo:[1,0,0]\n"
      "ds_read_b64_tr_b16 v[156:157], v23 offset:8800\n"
      "v_pk_mul_f32 v[82:83], v[104:105], v[72:73] op_sel:[0,0] op_sel_hi:[0,1]\n"
      "v_pk_fma_f32 v[106:107], v[104:105], v[72:73], v[82:83] op_sel:[1,1,0] op_sel_hi:[1,0,1] neg_lo:[1,0,0]\n"
      "v_pk_mul_f32 v[82:83], v[106:107], v[72:73] op_sel:[0,0] op_sel_hi:[0,1]\n"
      "v_pk_fma_f32 v[108:109], v[106:107], v[72:73], v[82:83] op_sel:[1,1,0] op_sel_hi:[1,0,1] neg_lo:[1,0,0]\n"
      "v_pk_mul_f32 v[82:83], v[108:109], v[72:73] op_sel:[0,0] op_sel_hi:[0,1]\n"
      "v_pk_fma_f32 v[110:111], v[108:109], v[72:73], v[82:83] op_sel:[1,1,0] op_sel_hi:[1,0,1] neg_lo:[1,0,0]\n"
      "ds_read_b64_tr_b16 v[158:159], v23 offset:9072\n"
      "v_pk_mul_f32 v[82:83], v[110:111], v[72:73] op_sel:[0,0] op_sel_hi:[0,1]\n"
      "v_pk_fma_f32 v[112:113], v[110:111], v[72:73], v[82:83] op_sel:[1,1,0] op_sel_hi:[1,0,1] neg_lo:[1,0,0]\n"
      "v_pk_mul_f32 v[82:83], v[112:113], v[72:73] op_sel:[0,0] op_sel_hi:[0,1]\n"
      "v_pk_fma_f32 v[114:115], v[112:113], v[72:73], v[82:83] op_sel:[1,1,0] op_sel_hi:[1,0,1] neg_lo:[1,0,0]\n"
      "v_pk_mul_f32 v[82:83], v[114:115], v[72:73] op_sel:[0,0] op_sel_hi:[0,1]\n"
      "v_pk_fma_f32 v[116:117], v[114:115], v[72:73], v[82:83] op_sel:[1,1,0] op_sel_hi:[1,0,1] neg_lo:[1,0,0]\n"
      "v_pk_mul_f32 v[82:83], v[116:117], v[72:73] op_sel:[0,0] op_sel_hi:[0,1]\n"
      "ds_read_b64_tr_b16 v[160:161], v23 offset:13152\n"
      "v_pk_fma_f32 v[118:119], v[116:117], v[72:73], v[82:83] op_sel:[1,1,0] op_sel_hi:[1,0,1] neg_lo:[1,0,0]\n"
      "v_pk_mul_f32 v[82:83], v[72:73], v[72:73] op_sel:[0,0] op_sel_hi:[0,1]\n"
      "v_pk_fma_f32 v[74:75], v[72:73], v[72:73], v[82:83] op_sel:[1,1,0] op_sel_hi:[1,0,1] neg_lo:[1,0,0]\n"
      "v_cvt_pk_bf16_f32 v128, v104, v106\n"
      "v_cvt_pk_bf16_f32 v129, v108, v110\n"
      "v_cvt_pk_bf16_f32 v130, v112, v114\n"
      "v_cvt_pk_bf16_f32 v131, v116, v118\n"
      "ds_read_b64_tr_b16 v[162:163], v23 offset:13424\n"
      "ds_write_b128 v22, v[128:131] offset:16\n"
      "v_cvt_pk_bf16_f32 v132, v105, v107\n"
      "v_cvt_pk_bf16_f32 v133, v109, v111\n"
      "v_cvt_pk_bf16_f32 v134, v113, v115\n"
      "v_cvt_pk_bf16_f32 v135, v117, v119\n"
      "ds_write_b128 v22, v[132:135] offset:144\n"
      "v_pk_mul_f32 v[82:83], v[88:89], v[80:81] op_sel:[0,0] op_sel_hi:[0,1]\n"
      "ds_read_b64_tr_b16 v[164:165], v23\n"
      "v_pk_fma_f32 v[96:97], v[88:89], v[80:81], v[82:83] op_sel:[1,1,0] op_sel_hi:[1,0,1] neg_lo:[1,0,0]\n"
      "v_pk_mul_f32 v[82:83], v[96:97], v[80:81] op_sel:[0,0] op_sel_hi:[0,1]\n"
      "v_pk_fma_f32 v[104:105], v[96:97], v[80:81], v[82:83] op_sel:[1,1,0] op_sel_hi:[1,0,1] neg_lo:[1,0,0]\n"
      "ds_read_b64_tr_b16 v[166:167], v23 offset:272\n"
      "v_pk_mul_f32 v[82:83], v[88:89], v[74:75] op_sel:[0,0] op_sel_hi:[0,1]\n"
      "v_pk_fma_f32 v[90:91], v[88:89], v[74:75], v[82:83] op_sel:[1,1,0] op_sel_hi:[1,0,1] neg_lo:[1,0,0]\n"
      "v_pk_mul_f32 v[82:83], v[96:97], v[74:75] op_sel:[0,0] op_sel_hi:[0,1]\n"
      "v_pk_fma_f32 v[98:99], v[96:97], v[74:75], v[82:83] op_sel:[1,1,0] op_sel_hi:[1,0,1] neg_lo:[1,0,0]\n"
      "ds_read_b64_tr_b16 v[168:169], v23 offset:4352\n"
      "v_pk_mul_f32 v[82:83], v[90:91], v[74:75] op_sel:[0,0] op_sel_hi:[0,1]\n"
      "v_pk_fma_f32 v[92:93], v[90:91], v[74:75], v[82:83] op_sel:[1,1,0] op_sel_hi:[1,0,1] neg_lo:[1,0,0]\n"
      "v_pk_mul_f32 v[82:83], v[98:99], v[74:75] op_sel:[0,0] op_sel_hi:[0,1]\n"
      "ds_read_b64_tr_b16 v[170:171], v23 offset:4624\n"
      "v_pk_fma_f32 v[100:101], v[98:99], v[74:75], v[82:83] op_sel:[1,1,0] op_sel_hi:[1,0,1] neg_lo:[1,0,0]\n"
      "v_pk_mul_f32 v[82:83], v[92:93], v[74:75] op_sel:[0,0] op_sel_hi:[0,1]\n"
      "v_pk_fma_f32 v[94:95], v[92:93], v[74:75], v[82:83] op_sel:[1,1,0] op_sel_hi:[1,0,1] neg_lo:[1,0,0]\n"
      "v_pk_mul_f32 v[82:83], v[100:101], v[74:75] op_sel:[0,0] op_sel_hi:[0,1]\n"
      "ds_read_b64_tr_b16 v[172:173], v23 offset:8704\n"
      "v_pk_fma_f32 v[102:103], v[100:101], v[74:75], v[82:83] op_sel:[1,1,0] op_sel_hi:[1,0,1] neg_lo:[1,0,0]\n"
      "v_pk_mul_f32 v[82:83], v[74:75], v[74:75] op_sel:[0,0] op_sel_hi:[0,1]\n"
      "v_pk_fma_f32 v[76:77], v[74:75], v[74:75], v[82:83] op_sel:[1,1,0] op_sel_hi:[1,0,1] neg_lo:[1,0,0]\n"
      "ds_read_b64_tr_b16 v[174:175], v23 offset:8976\n"
      "v_cvt_pk_bf16_f32 v120, v88, v90\n"
      "v_cvt_pk_bf16_f32 v121, v92, v94\n"
      "v_cvt_pk_bf16_f32 v122, v96, v98\n"
      "v_cvt_pk_bf16_f32 v123, v100, v102\n"
      "ds_read_b64_tr_b16 v[176:177], v23 offset:13056\n"
      "ds_write_b128 v22, v[120:123] offset:32\n"
      "v_cvt_pk_bf16_f32 v124, v89, v91\n"
      "v_cvt_pk_bf16_f32 v125, v93, v95\n"
      "s_waitcnt vmcnt(4) lgkmcnt(0)\n"
      "s_barrier\n"
      "ds_read_b64_tr_b16 v[232:233], v31\n"
      "ds_read_b64_tr_b16 v[234:235], v58\n"
      "ds_read_b64_tr_b16 v[236:237], v32\n"
      "ds_read_b64_tr_b16 v[238:239], v59\n"
      "ds_read_b64_tr_b16 v[240:241], v33\n"
      "ds_read_b64_tr_b16 v[242:243], v60\n"
      "ds_read_b64_tr_b16 v[244:245], v34\n"
      "ds_read_b64_tr_b16 v[246:247], v61\n"
      "ds_read_b64_tr_b16 v[248:249], v39\n"
      "ds_read_b64_tr_b16 v[250:251], v39 offset:1024\n"
      "ds_read_b64_tr_b16 v[252:253], v39 offset:4096\n"
      "ds_read_b64_tr_b16 v[254:255], v39 offset:5120\n"
      "ds_read_b128 v[180:183], v36 offset:0\n"
      "ds_read_b128 v[184:187], v36 offset:16\n"
      "ds_read_b128 v[188:191], v36 offset:32\n"
      "ds_read_b128 v[192:195], v36 offset:48\n"
      "ds_read_b128 v[196:199], v14\n"
      "ds_read_b128 v[200:203], v15\n"
      "ds_read_b128 v[204:207], v16\n"
      "ds_read_b128 v[220:223], v17\n"
      "ds_read_b64_tr_b16 v[178:179], v23 offset:13328\n"
      "s_cmp_lg_u32 s48, 0\n"
      "s_cbranch_scc1 .Lzh_5\n"
      "s_cmp_eq_u32 s101, 9\n"
      "s_cbranch_scc1 .Lzh_5\n"
      "v_mov_b32 v40, 0\n"
      "v_mov_b32 v41, 0\n"
      "v_mov_b32 v42, 0\n"
      "v_mov_b32 v43, 0\n"
      "v_bfe_u32 v46, v1, 1, 4\n"
      "v_mul_u32_u24 v46, 4224, v46\n"
      "v_and_b32 v47, 1, v1\n"
      "v_lshl_add_u32 v46, v47, 4, v46\n"
      "s_lshl_b32 s98, s59, 2\n"
      "s_add_u32 s98, s98, s43\n"
      "s_mul_i32 s98, s98, 67584\n"
      "s_add_u32 s98, s98, 262240\n"
      "buffer_store_dwordx4 v[40:43], v46, s[16:19], s98 offen sc1\n"
      "s_nop 1\n"
      ".Lzh_5:\n"
      "v_cvt_pk_bf16_f32 v126, v97, v99\n"
      "v_cvt_pk_bf16_f32 v127, v101, v103\n"
      "ds_write_b128 v22, v[124:127] offset:160\n"
      "s_waitcnt lgkmcnt(15)\n"
      "buffer_store_dwordx4 v[232:235], v35, s[16:19], s88 offen sc1\n"
      "v_pk_mul_f32 v[82:83], v[104:105], v[80:81] op_sel:[0,0] op_sel_hi:[0,1]\n"
      "v_mfma_f32_16x16x32_bf16 v[136:139], v[164:167], v[148:151], 0\n"
      "v_pk_fma_f32 v[108:109], v[104:105], v[80:81], v[82:83] op_sel:[1,1,0] op_sel_hi:[1,0,1] neg_lo:[1,0,0]\n"
      "buffer_store_dwordx4 v[236:239], v35, s[16:19], s89 offen sc1\n"
      "v_pk_mul_f32 v[82:83], v[108:109], v[80:81] op_sel:[0,0] op_sel_hi:[0,1]\n"
      "v_pk_fma_f32 v[112:113], v[108:109], v[80:81], v[82:83] op_sel:[1,1,0] op_sel_hi:[1,0,1] neg_lo:[1,0,0]\n"
      "v_pk_mul_f32 v[82:83], v[112:113], v[80:81] op_sel:[0,0] op_sel_hi:[0,1]\n"
      "buffer_store_dwordx4 v[240:243], v35, s[16:19], s90 offen sc1\n"
      "v_pk_fma_f32 v[116:117], v[112:113], v[80:81], v[82:83] op_sel:[1,1,0] op_sel_hi:[1,0,1] neg_lo:[1,0,0]\n"
      "v_pk_mul_f32 v[82:83], v[116:117], v[80:81] op_sel:[0,0] op_sel_hi:[0,1]\n"
      "v_mfma_f32_16x16x32_bf16 v[136:139], v[168:171], v[152:155], v[136:139]\n"
      "v_pk_fma_f32 v[88:89], v[116:117], v[80:81], v[82:83] op_sel:[1,1,0] op_sel_hi:[1,0,1] neg_lo:[1,0,0]\n"
      "s_waitcnt lgkmcnt(14)\n"
      "buffer_store_dwordx4 v[244:247], v35, s[16:19], s91 offen sc1\n"
      "v_pk_mul_f32 v[82:83], v[104:105], v[76:77] op_sel:[0,0] op_sel_hi:[0,1]\n"
      "v_pk_fma_f32 v[106:107], v[104:105], v[76:77], v[82:83] op_sel:[1,1,0] op_sel_hi:[1,0,1] neg_lo:[1,0,0]\n"
      "v_pk_mul_f32 v[82:83], v[108:109], v[76:77] op_sel:[0,0] op_sel_hi:[0,1]\n"
      "s_waitcnt lgkmcnt(12)\n"
      "buffer_store_dwordx4 v[248:251], v2, s[16:19], s92 offen sc1\n"
      "v_pk_fma_f32 v[110:111], v[108:109], v[76:77], v[82:83] op_sel:[1,1,0] op_sel_hi:[1,0,1] neg_lo:[1,0,0]\n"
      "v_pk_mul_f32 v[82:83], v[112:113], v[76:77] op_sel:[0,0] op_sel_hi:[0,1]\n"
      "v_pk_fma_f32 v[114:115], v[112:113], v[76:77], v[82:83] op_sel:[1,1,0] op_sel_hi:[1,0,1] neg_lo:[1,0,0]\n"
      "s_waitcnt lgkmcnt(10)\n"
      "buffer_store_dwordx4 v[252:255], v2, s[16:19], s93 offen sc1\n"
      "v_mfma_f32_16x16x32_bf16 v[136:139], v[172:175], v[156:159], v[136:139]\n"
      "v_pk_mul_f32 v[82:83], v[116:117], v[76:77] op_sel:[0,0] op_sel_hi:[0,1]\n"
      "v_pk_fma_f32 v[118:119], v[116:117], v[76:77], v[82:83] op_sel:[1,1,0] op_sel_hi:[1,0,1] neg_lo:[1,0,0]\n"
      "v_cvt_pk_bf16_f32 v128, v104, v106\n"
      "v_mov_b32 v226, 0\n"
      "v_cvt_pk_bf16_f32 v129, v108, v110\n"
      "s_waitcnt lgkmcnt(5)\n"
      "v_dot2c_f32_bf16 v226, v180, v196\n"
      "v_cvt_pk_bf16_f32 v130, v112, v114\n"
      "v_dot2c_f32_bf16 v226, v181, v197\n"
      "v_cvt_pk_bf16_f32 v131, v116, v118\n"
      "v_dot2c_f32_bf16 v226, v182, v198\n"
      "s_waitcnt lgkmcnt(1)\n"
      "v_mfma_f32_16x16x32_bf16 v[136:139], v[176:179], v[160:163], v[136:139]\n"
      "ds_write_b128 v22, v[128:131] offset:48\n"
      "v_dot2c_f32_bf16 v226, v183, v199\n"
      "v_cvt_pk_bf16_f32 v132, v105, v107\n"
      "v_dot2c_f32_bf16 v226, v184, v200\n"
      "v_cvt_pk_bf16_f32 v133, v109, v111\n"
      "v_dot2c_f32_bf16 v226, v185, v201\n"
      "v_cvt_pk_bf16_f32 v134, v113, v115\n"
      "v_dot2c_f32_bf16 v226, v186, v202\n"
      "v_cvt_pk_bf16_f32 v135, v117, v119\n"
      "ds_write_b128 v22, v[132:135] offset:176\n"
      "v_dot2c_f32_bf16 v226, v187, v203\n"
      "v_pk_mul_f32 v[82:83], v[88:89], v[80:81] op_sel:[0,0] op_sel_hi:[0,1]\n"
      "v_dot2c_f32_bf16 v226, v188, v204\n"
      "ds_read_b64_tr_b16 v[164:165], v23 offset:32\n"
      "v_dot2c_f32_bf16 v226, v189, v205\n"
      "v_pk_fma_f32 v[90:91], v[88:89], v[80:81], v[82:83] op_sel:[1,1,0] op_sel_hi:[1,0,1] neg_lo:[1,0,0]\n"
      "v_pk_mul_f32 v[82:83], v[90:91], v[80:81] op_sel:[0,0] op_sel_hi:[0,1]\n"
      "v_dot2c_f32_bf16 v226, v190, v206\n"
      "v_pk_fma_f32 v[92:93], v[90:91], v[80:81], v[82:83] op_sel:[1,1,0] op_sel_hi:[1,0,1] neg_lo:[1,0,0]\n"
      "v_dot2c_f32_bf16 v226, v191, v207\n"
      "ds_read_b64_tr_b16 v[166:167], v23 offset:304\n"
      "v_dot2c_f32_bf16 v226, v192, v220\n"
      "v_pk_mul_f32 v[82:83], v[92:93], v[80:81] op_sel:[0,0] op_sel_hi:[0,1]\n"
      "v_dot2c_f32_bf16 v226, v193, v221\n"
      "v_pk_fma_f32 v[94:95], v[92:93], v[80:81], v[82:83] op_sel:[1,1,0] op_sel_hi:[1,0,1] neg_lo:[1,0,0]\n"
      "v_pk_mul_f32 v[82:83], v[94:95], v[80:81] op_sel:[0,0] op_sel_hi:[0,1]\n"
      "v_dot2c_f32_bf16 v226, v194, v222\n"
      "v_pk_fma_f32 v[96:97], v[94:95], v[80:81], v[82:83] op_sel:[1,1,0] op_sel_hi:[1,0,1] neg_lo:[1,0,0]\n"
      "v_dot2c_f32_bf16 v226, v195, v223\n"
      "ds_read_b64_tr_b16 v[168:169], v23 offset:4384\n"
      "s_nop 2\n"
      "v_add_f32_dpp v226, v226, v226 quad_perm:[1,0,3,2] row_mask:0xf bank_mask:0xf\n"
      "v_pk_mul_f32 v[82:83], v[96:97], v[80:81] op_sel:[0,0] op_sel_hi:[0,1]\n"
      "v_pk_fma_f32 v[98:99], v[96:97], v[80:81], v[82:83] op_sel:[1,1,0] op_sel_hi:[1,0,1] neg_lo:[1,0,0]\n"
      "v_pk_mul_f32 v[82:83], v[98:99], v[80:81] op_sel:[0,0] op_sel_hi:[0,1]\n"
      "ds_read_b64_tr_b16 v[170:171], v23 offset:4656\n"
      "v_pk_fma_f32 v[100:101], v[98:99], v[80:81], v[82:83] op_sel:[1,1,0] op_sel_hi:[1,0,1] neg_lo:[1,0,0]\n"
      "v_pk_mul_f32 v[82:83], v[100:101], v[80:81] op_sel:[0,0] op_sel_hi:[0,1]\n"
      "v_pk_fma_f32 v[102:103], v[100:101], v[80:81], v[82:83] op_sel:[1,1,0] op_sel_hi:[1,0,1] neg_lo:[1,0,0]\n"
      "v_pk_mul_f32 v[82:83], v[102:103], v[80:81] op_sel:[0,0] op_sel_hi:[0,1]\n"
      "ds_read_b64_tr_b16 v[172:173], v23 offset:8736\n"
      "v_pk_fma_f32 v[104:105], v[102:103], v[80:81], v[82:83] op_sel:[1,1,0] op_sel_hi:[1,0,1] neg_lo:[1,0,0]\n"
      "v_cvt_pk_bf16_f32 v120, v88, v90\n"
      "v_cvt_pk_bf16_f32 v121, v92, v94\n"
      "v_cvt_pk_bf16_f32 v122, v96, v98\n"
      "ds_read_b64_tr_b16 v[174:175], v23 offset:9008\n"
      "v_cvt_pk_bf16_f32 v123, v100, v102\n"
      "ds_write_b128 v22, v[120:123] offset:64\n"
      "v_cvt_pk_bf16_f32 v124, v89, v91\n"
      "ds_read_b64_tr_b16 v[176:177], v23 offset:13088\n"
      "v_cvt_pk_bf16_f32 v125, v93, v95\n"
      "v_cvt_pk_bf16_f32 v126, v97, v99\n"
      "v_cvt_pk_bf16_f32 v127, v101, v103\n"
      "ds_write_b128 v22, v[124:127] offset:192\n"
      "ds_read_b64_tr_b16 v[178:179], v23 offset:13360\n"
      "v_cvt_pk_bf16_f32 v64, v104, v105\n"
      "ds_write_b16 v22, v64 offset:80\n"
      "ds_write_b16_d16_hi v22, v64 offset:208\n"
      "s_lshl_b32 s98, s52, 7\n"
      "s_add_u32 s53, s55, s98\n"
      "s_add_u32 m0, s57, 32768\n"
      "s_nop 0\n"
      "buffer_load_dwordx4 v48, s[4:7], s53 offen lds\n"
      "buffer_load_dwordx4 v49, s[4:7], s53 offen offset:1024 lds\n"
      "buffer_load_dwordx4 v50, s[4:7], s53 offen offset:2048 lds\n"
      "buffer_load_dwordx4 v51, s[4:7], s53 offen offset:3072 lds\n"
      "s_mul_i32 s98, s52, 221184\n"
      "s_add_u32 s54, s56, s98\n"
      "s_add_u32 m0, s57, 49152\n"
      "s_nop 0\n"
      "buffer_load_dwordx4 v52, s[8:11], s54 offen lds\n"
      "buffer_load_dwordx4 v53, s[8:11], s54 offen offset:1024 lds\n"
      "buffer_load_dwordx4 v54, s[8:11], s54 offen offset:2048 lds\n"
      "buffer_load_dwordx4 v55, s[8:11], s54 offen offset:3072 lds\n"
      "s_add_u32 s52, s52, 1\n"
      "s_cmp_eq_u32 s52, 10\n"
      "s_cselect_b32 s52, 0, s52\n"
      "ds_read_b64_tr_b16 v[88:89], v23 offset:64\n"
      "ds_read_b64_tr_b16 v[90:91], v23 offset:336\n"
      "ds_read_b64_tr_b16 v[92:93], v23 offset:4416\n"
      "ds_read_b64_tr_b16 v[94:95], v23 offset:4688\n"
      "ds_read_b64_tr_b16 v[96:97], v23 offset:8768\n"
      "ds_read_b64_tr_b16 v[98:99], v23 offset:9040\n"
      "ds_read_b64_tr_b16 v[100:101], v23 offset:13120\n"
      "ds_read_b64_tr_b16 v[102:103], v23 offset:13392\n"
      "v_add_u32 v64, s65, v24\n"
      "v_add_u32 v65, s65, v25\n"
      "v_add_u32 v67, s65, v2\n"
      "s_waitcnt lgkmcnt(15)\n"
      "v_mfma_f32_16x16x32_bf16 v[140:143], v[164:167], v[148:151], 0\n"
      "v_mfma_f32_16x16x32_bf16 v[140:143], v[168:171], v[152:155], v[140:143]\n"
      "s_waitcnt lgkmcnt(14)\n"
      "v_mfma_f32_16x16x32_bf16 v[140:143], v[172:175], v[156:159], v[140:143]\n"
      "s_waitcnt lgkmcnt(10)\n"
      "v_mfma_f32_16x16x32_bf16 v[140:143], v[176:179], v[160:163], v[140:143]\n"
      "s_mul_i32 s68, s50, -34816\n"
      "s_add_u32 s68, s68, 17408\n"
      "v_add_u32 v66, s68, v28\n"
      "s_waitcnt lgkmcnt(6)\n"
      "v_mfma_f32_16x16x32_bf16 v[144:147], v[88:91], v[148:151], 0\n"
      "s_waitcnt lgkmcnt(4)\n"
      "v_mfma_f32_16x16x32_bf16 v[144:147], v[92:95], v[152:155], v[144:147]\n"
      "s_waitcnt lgkmcnt(2)\n"
      "v_mfma_f32_16x16x32_bf16 v[144:147], v[96:99], v[156:159], v[144:147]\n"
      "s_waitcnt lgkmcnt(0)\n"
      "v_mfma_f32_16x16x32_bf16 v[144:147], v[100:103], v[160:163], v[144:147]\n"
      "s_cmp_lg_u32 s50, 0\n"
      "s_cbranch_scc1 .Lkx_6\n"
      "ds_write_b128 v28, v[136:139]\n"
      "ds_write_b128 v28, v[140:143] offset:1024\n"
      "v_mov_b32 v120, 0\n"
      "v_mov_b32 v121, 0\n"
      "v_mov_b32 v122, 0\n"
      "v_mov_b32 v123, 0\n"
      "s_mov_b64 exec, 0x3fffffff\n"
      "ds_write_b128 v67, v[120:123] offset:4096\n"
      "s_mov_b64 exec, -1\n"
      "s_waitcnt lgkmcnt(0)\n"
      "s_barrier\n"
      "ds_read_b128 v[120:123], v66\n"
      "s_waitcnt lgkmcnt(0)\n"
      "v_add_f32 v144, v144, v120\n"
      "v_add_f32 v145, v145, v121\n"
      "v_add_f32 v146, v146, v122\n"
      "v_add_f32 v147, v147, v123\n"
      "v_cvt_pk_bf16_f32 v120, v144, v144\n"
      "v_mov_b32 v121, v120\n"
      "v_cvt_pk_bf16_f32 v122, v145, v145\n"
      "v_mov_b32 v123, v122\n"
      "v_cvt_pk_bf16_f32 v124, v146, v146\n"
      "v_mov_b32 v125, v124\n"
      "v_cvt_pk_bf16_f32 v126, v147, v147\n"
      "v_mov_b32 v127, v126\n"
      "s_mov_b32 exec_hi, 0\n"
      "ds_write_b64 v64, v[120:121] offset:776\n"
      "ds_write_b64 v64, v[122:123] offset:520\n"
      "ds_write_b64 v64, v[124:125] offset:264\n"
      "ds_write_b64 v64, v[126:127] offset:8\n"
      "ds_write_b64 v64, v[122:123] offset:528\n"
      "ds_write_b64 v64, v[124:125] offset:272\n"
      "ds_write_b64 v64, v[126:127] offset:16\n"
      "s_mov_b32 exec_lo, 0xfffffffe\n"
      "s_mov_b32 exec_hi, 1\n"
      "ds_write_b64 v64, v[120:121] offset:784\n"
      "s_mov_b64 exec, 1\n"
      "ds_write_b32 v64, v120 offset:784\n"
      "s_mov_b32 exec_lo, 0x55555555\n"
      "ds_write_b16 v64, v120 offset:774\n"
      "ds_write_b16 v64, v122 offset:518\n"
      "ds_write_b16 v64, v124 offset:262\n"
      "ds_write_b16 v64, v126 offset:6\n"
      "s_mov_b64 exec, -1\n"
      "s_branch .Lkx_7\n"
      ".Lkx_6:\n"
      "s_mov_b64 exec, 1\n"
      "v_add_f32 v136, v136, v84\n"
      "s_mov_b64 exec, -1\n"
      "s_nop 2\n"
      "ds_write_b128 v28, v[144:147]\n"
      "s_waitcnt lgkmcnt(0)\n"
      "s_barrier\n"
      "ds_read_b128 v[124:127], v66 offset:1024\n"
      "ds_read_b128 v[120:123], v66\n"
      "s_waitcnt lgkmcnt(1)\n"
      "v_add_f32 v140, v140, v124\n"
      "v_add_f32 v141, v141, v125\n"
      "v_add_f32 v142, v142, v126\n"
      "v_add_f32 v143, v143, v127\n"
      "s_waitcnt lgkmcnt(0)\n"
      "v_add_f32 v136, v136, v120\n"
      "v_add_f32 v137, v137, v121\n"
      "v_add_f32 v138, v138, v122\n"
      "v_add_f32 v139, v139, v123\n"
      "v_cvt_pk_bf16_f32 v120, v141, v140\n"
      "v_cvt_pk_bf16_f32 v121, v140, v140\n"
      "v_cvt_pk_bf16_f32 v122, v141, v141\n"
      "v_cvt_pk_bf16_f32 v124, v143, v142\n"
      "v_cvt_pk_bf16_f32 v125, v142, v142\n"
      "v_cvt_pk_bf16_f32 v126, v143, v143\n"
      "s_mov_b32 exec_lo, 0\n"
      "ds_write_b32 v64, v121 offset:272\n"
      "ds_write_b64 v64, v[120:121] offset:264\n"
      "ds_write_b32 v64, v122 offset:260\n"
      "ds_write_b32 v64, v125 offset:16\n"
      "ds_write_b64 v64, v[124:125] offset:8\n"
      "ds_write_b32 v64, v126 offset:4\n"
      "s_mov_b32 exec_lo, -1\n"
      "v_cvt_pk_bf16_f32 v128, v142, v141\n"
      "v_mov_b32 v129, v121\n"
      "v_cvt_pk_bf16_f32 v130, v139, v138\n"
      "v_cvt_pk_bf16_f32 v131, v137, v136\n"
      "s_mov_b32 exec_hi, 0\n"
      "ds_write_b16 v64, v121 offset:16\n"
      "ds_write_b64 v64, v[128:129] offset:8\n"
      "ds_write_b32 v64, v125 offset:4\n"
      "ds_write_b16 v64, v126 offset:2\n"
      "s_mov_b64 exec, -1\n"
      "ds_write_b64 v65, v[130:131]\n"
      ".Lkx_7:\n"
      "s_waitcnt vmcnt(8) lgkmcnt(0)\n"
      "s_barrier\n"
      "s_cmp_lg_u32 s43, 0\n"
      "s_cbranch_scc1 .Lw0_8\n"
      "s_lshl_b32 s98, s42, 7\n"
      "s_add_u32 s98, s98, 0\n"
      "v_mov_b32 v14, s98\n"
      "v_mov_b32 v16, s40\n"
      "v_mov_b32 v17, s41\n"
      "global_store_dwordx2 v14, v[16:17], s[16:17] sc1\n"
      "s_nop 0\n"
      ".Lw0_8:\n"
      "ds_read_b64 v[96:97], v45 offset:0\n"
      "ds_read_b64 v[98:99], v45 offset:16\n"
      "ds_read_b64 v[100:101], v45 offset:32\n"
      "ds_read_b64 v[102:103], v45 offset:48\n"
      "ds_read_b64 v[104:105], v45 offset:64\n"
      "ds_read_b64 v[106:107], v45 offset:80\n"
      "ds_read_b64 v[108:109], v45 offset:96\n"
      "ds_read_b64 v[110:111], v45 offset:112\n"
      "ds_read_b128 v[168:171], v212 offset:0\n"
      "ds_read_b128 v[172:175], v212 offset:4096\n"
      "ds_read_b64_tr_b16 v[160:161], v208\n"
      "ds_read_b64_tr_b16 v[162:163], v208 offset:1024\n"
      "ds_read_b64_tr_b16 v[164:165], v209\n"
      "ds_read_b64_tr_b16 v[166:167], v209 offset:1024\n"
      "ds_read_b128 v[184:187], v213 offset:0\n"
      "ds_read_b128 v[188:191], v213 offset:4096\n"
      "ds_read_b64_tr_b16 v[176:177], v208 offset:4096\n"
      "ds_read_b64_tr_b16 v[178:179], v208 offset:5120\n"
      "ds_read_b64_tr_b16 v[180:181], v209 offset:4096\n"
      "ds_read_b64_tr_b16 v[182:183], v209 offset:5120\n"
      "s_waitcnt lgkmcnt(15)\n"
      "v_lshlrev_b32 v64, 16, v96\n"
      "v_and_b32 v65, 0xffff0000, v96\n"
      "v_lshlrev_b32 v66, 16, v97\n"
      "v_and_b32 v67, 0xffff0000, v97\n"
      "v_lshlrev_b32 v68, 16, v98\n"
      "v_and_b32 v69, 0xffff0000, v98\n"
      "v_lshlrev_b32 v70, 16, v99\n"
      "v_and_b32 v71, 0xffff0000, v99\n"
      "v_lshlrev_b32 v72, 16, v100\n"
      "v_and_b32 v73, 0xffff0000, v100\n"
      "v_lshlrev_b32 v74, 16, v101\n"
      "v_and_b32 v75, 0xffff0000, v101\n"
      "v_lshlrev_b32 v76, 16, v102\n"
      "v_and_b32 v77, 0xffff0000, v102\n"
      "v_lshlrev_b32 v78, 16, v103\n"
      "v_and_b32 v79, 0xffff0000, v103\n"
      "v_lshlrev_b32 v80, 16, v104\n"
      "v_and_b32 v81, 0xffff0000, v104\n"
      "v_lshlrev_b32 v82, 16, v105\n"
      "v_and_b32 v83, 0xffff0000, v105\n"
      "s_waitcnt lgkmcnt(14)\n"
      "v_lshlrev_b32 v84, 16, v106\n"
      "v_and_b32 v85, 0xffff0000, v106\n"
      "v_lshlrev_b32 v86, 16, v107\n"
      "v_and_b32 v87, 0xffff0000, v107\n"
      "s_waitcnt lgkmcnt(13)\n"
      "v_lshlrev_b32 v88, 16, v108\n"
      "v_and_b32 v89, 0xffff0000, v108\n"
      "v_lshlrev_b32 v90, 16, v109\n"
      "v_and_b32 v91, 0xffff0000, v109\n"
      "s_waitcnt lgkmcnt(12)\n"
      "v_lshlrev_b32 v92, 16, v110\n"
      "v_and_b32 v93, 0xffff0000, v110\n"
      "v_lshlrev_b32 v94, 16, v111\n"
      "v_and_b32 v95, 0xffff0000, v111\n"
      "s_waitcnt lgkmcnt(8)\n"
      "v_mfma_f32_32x32x16_bf16 v[96:111], v[160:163], v[168:171], v[64:79]\n"
      "ds_read_b128 v[200:203], v214 offset:0\n"
      "ds_read_b128 v[204:207], v214 offset:4096\n"
      "s_mul_i32 s68, s47, 5\n"
      "s_lshr_b32 s68, s68, 2\n"
      "s_mul_i32 s68, s68, 27\n"
      "v_min_u32 v40, 53, v1\n"
      "s_lshl_b32 s98, s52, 7\n"
      "s_add_u32 s53, s55, s98\n"
      "s_add_u32 m0, s57, 65536\n"
      "s_nop 0\n"
      "buffer_load_dwordx4 v48, s[4:7], s53 offen lds\n"
      "v_mfma_f32_32x32x16_bf16 v[112:127], v[160:163], v[172:175], v[64:79]\n"
      "ds_read_b64_tr_b16 v[192:193], v208 offset:8192\n"
      "v_add_u32 v40, s68, v40\n"
      "v_bfe_u32 v41, v40, 3, 2\n"
      "v_bfe_u32 v42, v40, 1, 2\n"
      "v_mad_u32_u24 v41, v41, 7, v42\n"
      "buffer_load_dwordx4 v49, s[4:7], s53 offen offset:1024 lds\n"
      "s_waitcnt lgkmcnt(9)\n"
      "v_mfma_f32_32x32x16_bf16 v[128:143], v[164:167], v[168:171], v[80:95]\n"
      "ds_read_b64_tr_b16 v[194:195], v208 offset:9216\n"
      "ds_read_b64_tr_b16 v[196:197], v209 offset:8192\n"
      "v_lshrrev_b32 v42, 5, v40\n"
      "v_lshlrev_b32 v41, 3, v41\n"
      "v_lshl_add_u32 v41, v42, 1, v41\n"
      "v_add_u32 v41, 25, v41\n"
      "buffer_load_dwordx4 v50, s[4:7], s53 offen offset:2048 lds\n"
      "v_mfma_f32_32x32x16_bf16 v[144:159], v[164:167], v[172:175], v[80:95]\n"
      "ds_read_b64_tr_b16 v[198:199], v209 offset:9216\n"
      "v_add_u32 v43, -128, v40\n"
      "v_mul_u32_u24 v42, 205, v43\n"
      "v_lshrrev_b32 v42, 11, v42\n"
      "v_mul_u32_u24 v47, 10, v42\n"
      "buffer_load_dwordx4 v51, s[4:7], s53 offen offset:3072 lds\n"
      "s_waitcnt lgkmcnt(8)\n"
      "v_mfma_f32_32x32x16_bf16 v[96:111], v[176:179], v[184:187], v[96:111]\n"
      "ds_read_b128 v[168:171], v215 offset:0\n"
      "ds_read_b128 v[172:175], v215 offset:4096\n"
      "v_sub_u32 v43, v43, v47\n"
      "v_and_b32 v47, 3, v42\n"
      "v_mad_u32_u24 v47, v47, 7, v43\n"
      "v_lshlrev_b32 v47, 3, v47\n"
      "v_lshrrev_b32 v42, 2, v42\n"
      "s_mul_i32 s98, s52, 221184\n"
      "s_add_u32 s54, s56, s98\n"
      "s_add_u32 m0, s57, 81920\n"
      "s_nop 0\n"
      "buffer_load_dwordx4 v52, s[8:11], s54 offen lds\n"
      "v_mfma_f32_32x32x16_bf16 v[112:127], v[176:179], v[188:191], v[112:127]\n"
      "ds_read_b64_tr_b16 v[160:161], v208 offset:12288\n"
      "v_lshl_add_u32 v42, v42, 1, v47\n"
      "v_add_u32 v47, -55, v42\n"
      "v_cmp_lt_u32 vcc, 6, v43\n"
      "s_nop 1\n"
      "v_cndmask_b32 v42, v42, v47, vcc\n"
      "v_cmp_gt_u32 vcc, 128, v40\n"
      "s_nop 1\n"
      "v_cndmask_b32 v40, v42, v41, vcc\n"
      "buffer_load_dwordx4 v53, s[8:11], s54 offen offset:1024 lds\n"
      "s_waitcnt lgkmcnt(9)\n"
      "v_mfma_f32_32x32x16_bf16 v[128:143], v[180:183], v[184:187], v[128:143]\n"
      "ds_read_b64_tr_b16 v[162:163], v208 offset:13312\n"
      "ds_read_b64_tr_b16 v[164:165], v209 offset:12288\n"
      "v_lshlrev_b32 v18, 7, v40\n"
      "s_sub_u32 s68, s63, s51\n"
      "s_mul_i32 s69, s44, 80\n"
      "s_min_u32 s70, s45, 23\n"
      "buffer_load_dwordx4 v54, s[8:11], s54 offen offset:2048 lds\n"
      "buffer_load_dwordx4 v55, s[8:11], s54 offen offset:3072 lds\n"
      "v_mfma_f32_32x32x16_bf16 v[144:159], v[180:183], v[188:191], v[144:159]\n"
      "ds_read_b64_tr_b16 v[166:167], v209 offset:13312\n"
      "s_add_u32 s69, s69, s70\n"
      "s_add_u32 s69, s69, 56\n"
      "s_lshr_b32 s68, s68, 6\n"
      "s_lshr_b32 s69, s69, 6\n"
      "s_add_u32 s52, s52, 1\n"
      "s_cmp_eq_u32 s52, 10\n"
      "s_cselect_b32 s52, 0, s52\n"
      "s_waitcnt vmcnt(8) lgkmcnt(0)\n"
      "s_barrier\n"
      "v_mfma_f32_32x32x16_bf16 v[96:111], v[192:195], v[200:203], v[96:111]\n"
      "ds_read_b128 v[184:187], v212 offset:32768\n"
      "ds_read_b128 v[188:191], v212 offset:36864\n"
      "v_mov_b32 v40, s68\n"
      "v_mov_b32 v41, s69\n"
      "v_and_b32 v42, 1, v5\n"
      "v_cmp_eq_u32 vcc, 0, v42\n"
      "s_nop 1\n"
      "v_cndmask_b32 v40, v41, v40, vcc\n"
      "v_and_b32 v41, 3, v1\n"
      "s_lshl_b32 s98, s52, 7\n"
      "s_add_u32 s53, s55, s98\n"
      "s_add_u32 m0, s57, 98304\n"
      "s_nop 0\n"
      "buffer_load_dwordx4 v48, s[4:7], s53 offen lds\n"
      "v_mfma_f32_32x32x16_bf16 v[112:127], v[192:195], v[204:207], v[112:127]\n"
      "ds_read_b64_tr_b16 v[176:177], v208 offset:32768\n"
      "v_mul_u32_u24 v42, 2, v41\n"
      "v_sub_u32 v40, v40, v42\n"
      "v_add_u32 v42, 10, v40\n"
      "v_cmp_gt_i32 vcc, 0, v40\n"
      "s_nop 1\n"
      "v_cndmask_b32 v40, v40, v42, vcc\n"
      "buffer_load_dwordx4 v49, s[4:7], s53 offen offset:1024 lds\n"
      "v_mfma_f32_32x32x16_bf16 v[128:143], v[196:199], v[200:203], v[128:143]\n"
      "ds_read_b64_tr_b16 v[178:179], v208 offset:33792\n"
      "ds_read_b64_tr_b16 v[180:181], v209 offset:32768\n"
      "v_mad_u32_u24 v41, v41, 7, v40\n"
      "v_lshlrev_b32 v41, 3, v41\n"
      "v_add_u32 v42, -55, v41\n"
      "v_cmp_lt_u32 vcc, 6, v40\n"
      "s_nop 1\n"
      "v_cndmask_b32 v41, v41, v42, vcc\n"
      "buffer_load_dwordx4 v50, s[4:7], s53 offen offset:2048 lds\n"
      "v_mfma_f32_32x32x16_bf16 v[144:159], v[196:199], v[204:207], v[144:159]\n"
      "ds_read_b64_tr_b16 v[182:183], v209 offset:33792\n"
      "v_bfe_u32 v42, v1, 2, 2\n"
      "v_lshl_add_u32 v41, v42, 1, v41\n"
      "v_lshlrev_b32 v41, 7, v41\n"
      "v_add_u32 v19, 0, v41\n"
      "buffer_load_dwordx4 v51, s[4:7], s53 offen offset:3072 lds\n"
      "v_mfma_f32_32x32x16_bf16 v[96:111], v[160:163], v[168:171], v[96:111]\n"
      "ds_read_b128 v[200:203], v213 offset:32768\n"
      "ds_read_b128 v[204:207], v213 offset:36864\n"
      "v_min_u32 v40, 13, v1\n"
      "v_lshlrev_b32 v41, 3, v40\n"
      "v_add_u32 v42, -55, v41\n"
      "v_cmp_lt_u32 vcc, 6, v40\n"
      "s_nop 1\n"
      "v_cndmask_b32 v41, v41, v42, vcc\n"
      "s_bfe_u32 s69, s46, 0x20001\n"
      "s_mul_i32 s98, s52, 221184\n"
      "s_add_u32 s54, s56, s98\n"
      "s_add_u32 m0, s57, 114688\n"
      "s_nop 0\n"
      "buffer_load_dwordx4 v52, s[8:11], s54 offen lds\n"
      "v_mfma_f32_32x32x16_bf16 v[112:127], v[160:163], v[172:175], v[112:127]\n"
      "ds_read_b64_tr_b16 v[192:193], v208 offset:36864\n"
      "s_mul_i32 s69, s69, 56\n"
      "s_and_b32 s70, s44, 6\n"
      "s_add_u32 s69, s69, s70\n"
      "s_lshl_b32 s69, s69, 7\n"
      "buffer_load_dwordx4 v53, s[8:11], s54 offen offset:1024 lds\n"
      "v_mfma_f32_32x32x16_bf16 v[128:143], v[164:167], v[168:171], v[128:143]\n"
      "ds_read_b64_tr_b16 v[194:195], v208 offset:37888\n"
      "ds_read_b64_tr_b16 v[196:197], v209 offset:36864\n"
      "s_add_u32 s69, s69, 32768\n"
      "v_lshl_add_u32 v20, v41, 7, s69\n"
      "v_and_b32 v40, 31, v1\n"
      "v_lshl_add_u32 v40, v40, 3, s47\n"
      "buffer_load_dwordx4 v54, s[8:11], s54 offen offset:2048 lds\n"
      "buffer_load_dwordx4 v55, s[8:11], s54 offen offset:3072 lds\n"
      "v_mfma_f32_32x32x16_bf16 v[144:159], v[164:167], v[172:175], v[144:159]\n"
      "ds_read_b64_tr_b16 v[198:199], v209 offset:37888\n"
      "v_lshlrev_b32 v40, 7, v40\n"
      "v_add_u32 v21, 65536, v40\n"
      "v_add_u32 v41, 1, v4\n"
      "v_and_b32 v42, 3, v41\n"
      "s_add_u32 s52, s52, 1\n"
      "s_cmp_eq_u32 s52, 10\n"
      "s_cselect_b32 s52, 0, s52\n"
      "s_waitcnt lgkmcnt(8)\n"
      "v_mfma_f32_32x32x16_bf16 v[96:111], v[176:179], v[184:187], v[96:111]\n"
      "ds_read_b128 v[168:171], v214 offset:32768\n"
      "ds_read_b128 v[172:175], v214 offset:36864\n"
      "v_mul_u32_u24 v42, 5184, v42\n"
      "v_lshrrev_b32 v41, 2, v41\n"
      "v_lshl_add_u32 v42, v41, 3, v42\n"
      "v_lshl_add_u32 v42, v5, 4, v42\n"
      "s_lshl_b32 s98, s52, 7\n"
      "s_add_u32 s53, s55, s98\n"
      "s_add_u32 m0, s57, 0\n"
      "s_nop 0\n"
      "buffer_load_dwordx4 v48, s[4:7], s53 offen lds\n"
      "v_mfma_f32_32x32x16_bf16 v[112:127], v[176:179], v[188:191], v[112:127]\n"
      "ds_read_b64_tr_b16 v[160:161], v208 offset:40960\n"
      "s_mul_i32 s69, s51, 20736\n"
      "s_lshl_b32 s70, s50, 8\n"
      "s_add_u32 s68, s69, s70\n"
      "v_add_u32 v26, s68, v42\n"
      "buffer_load_dwordx4 v49, s[4:7], s53 offen offset:1024 lds\n"
      "s_waitcnt lgkmcnt(9)\n"
      "v_mfma_f32_32x32x16_bf16 v[128:143], v[180:183], v[184:187], v[128:143]\n"
      "ds_read_b64_tr_b16 v[162:163], v208 offset:41984\n"
      "ds_read_b64_tr_b16 v[164:165], v209 offset:40960\n"
      "v_lshlrev_b32 v41, 5, v4\n"
      "v_lshlrev_b32 v42, 4, v5\n"
      "v_sub_u32 v42, v42, v41\n"
      "s_add_u32 s70, s70, 480\n"
      "buffer_load_dwordx4 v50, s[4:7], s53 offen offset:2048 lds\n"
      "v_mfma_f32_32x32x16_bf16 v[144:159], v[180:183], v[188:191], v[144:159]\n"
      "ds_read_b64_tr_b16 v[166:167], v209 offset:41984\n"
      "v_add_u32 v27, s70, v42\n"
      "s_lshl_b32 s68, s43, 12\n"
      "s_add_u32 s68, s68, 0\n"
      "v_add_u32 v29, s68, v2\n"
      "buffer_load_dwordx4 v51, s[4:7], s53 offen offset:3072 lds\n"
      "s_waitcnt lgkmcnt(8)\n"
      "v_mfma_f32_32x32x16_bf16 v[96:111], v[192:195], v[200:203], v[96:111]\n"
      "ds_read_b128 v[184:187], v215 offset:32768\n"
      "ds_read_b128 v[188:191], v215 offset:36864\n"
      "v_and_b32 v42, 1, v5\n"
      "v_lshl_add_u32 v41, v42, 9, v41\n"
      "v_lshrrev_b32 v42, 1, v5\n"
      "v_lshl_add_u32 v41, v42, 4, v41\n"
      "s_lshl_b32 s68, s50, 11\n"
      "s_mul_i32 s98, s52, 221184\n"
      "s_add_u32 s54, s56, s98\n"
      "s_add_u32 m0, s57, 16384\n"
      "s_nop 0\n"
      "buffer_load_dwordx4 v52, s[8:11], s54 offen lds\n"
      "v_mfma_f32_32x32x16_bf16 v[112:127], v[192:195], v[204:207], v[112:127]\n"
      "ds_read_b64_tr_b16 v[176:177], v208 offset:45056\n"
      "v_add_u32 v30, s68, v41\n"
      "s_mul_i32 s68, s43, 14\n"
      "s_min_u32 s68, s68, 41\n"
      "s_cmp_ge_u32 s43, 2\n"
      "s_cselect_b32 s61, 13, 99\n"
      "buffer_load_dwordx4 v53, s[8:11], s54 offen offset:1024 lds\n"
      "s_waitcnt lgkmcnt(9)\n"
      "v_mfma_f32_32x32x16_bf16 v[128:143], v[196:199], v[200:203], v[128:143]\n"
      "ds_read_b64_tr_b16 v[178:179], v208 offset:46080\n"
      "ds_read_b64_tr_b16 v[180:181], v209 offset:45056\n"
      "s_and_b32 s69, s46, 7\n"
      "s_lshl_b32 s60, s69, 1\n"
      "s_sub_u32 s69, s60, 14\n"
      "s_min_u32 s60, s60, s69\n"
      "buffer_load_dwordx4 v54, s[8:11], s54 offen offset:2048 lds\n"
      "buffer_load_dwordx4 v55, s[8:11], s54 offen offset:3072 lds\n"
      "v_mfma_f32_32x32x16_bf16 v[144:159], v[196:199], v[204:207], v[144:159]\n"
      "ds_read_b64_tr_b16 v[182:183], v209 offset:46080\n"
      "s_mov_b32 s60, 0\n"
      "s_mul_i32 s69, s68, 40\n"
      "s_mul_i32 s70, s47, 5\n"
      "s_add_u32 s69, s69, s70\n"
      "s_add_u32 s52, s52, 1\n"
      "s_cmp_eq_u32 s52, 10\n"
      "s_cselect_b32 s52, 0, s52\n"
      "s_waitcnt vmcnt(16) lgkmcnt(0)\n"
      "s_barrier\n"
      "v_mfma_f32_32x32x16_bf16 v[96:111], v[160:163], v[168:171], v[96:111]\n"
      "ds_read_b128 v[200:203], v216 offset:0\n"
      "ds_read_b128 v[204:207], v216 offset:4096\n"
      "buffer_store_dword v226, v44, s[16:19], s67 offen sc1\n"
      "s_lshl_b32 s69, s69, 10\n"
      "s_add_u32 s64, s69, 3145728\n"
      "s_add_u32 s69, s43, 1\n"
      "s_and_b32 s69, s69, 3\n"
      "s_lshl_b32 s70, s46, 2\n"
      "s_lshl_b32 s98, s52, 7\n"
      "s_add_u32 s53, s55, s98\n"
      "s_add_u32 m0, s57, 32768\n"
      "v_mfma_f32_32x32x16_bf16 v[112:127], v[160:163], v[172:175], v[112:127]\n"
      "ds_read_b64_tr_b16 v[192:193], v210\n"
      "s_add_u32 s69, s69, s70\n"
      "s_mul_i32 s69, s69, 54\n"
      "s_add_u32 s69, s69, s68\n"
      "s_lshl_b32 s69, s69, 10\n"
      "v_mfma_f32_32x32x16_bf16 v[128:143], v[164:167], v[168:171], v[128:143]\n"
      "ds_read_b64_tr_b16 v[194:195], v210 offset:1024\n"
      "ds_read_b64_tr_b16 v[196:197], v211\n"
      "s_add_u32 s84, s69, 9437184\n"
      "s_add_u32 s69, s43, 2\n"
      "s_and_b32 s69, s69, 3\n"
      "s_lshl_b32 s70, s46, 2\n"
      "buffer_load_dwordx4 v48, s[4:7], s53 offen lds\n"
      "v_mfma_f32_32x32x16_bf16 v[144:159], v[164:167], v[172:175], v[144:159]\n"
      "ds_read_b64_tr_b16 v[198:199], v211 offset:1024\n"
      "s_add_u32 s69, s69, s70\n"
      "s_mul_i32 s69, s69, 54\n"
      "s_add_u32 s69, s69, s68\n"
      "s_lshl_b32 s69, s69, 10\n"
      "v_mfma_f32_32x32x16_bf16 v[96:111], v[176:179], v[184:187], v[96:111]\n"
      "ds_read_b128 v[168:171], v217 offset:0\n"
      "ds_read_b128 v[172:175], v217 offset:4096\n"
      "s_add_u32 s85, s69, 9437184\n"
      "s_add_u32 s69, s43, 3\n"
      "s_and_b32 s69, s69, 3\n"
      "s_lshl_b32 s70, s46, 2\n"
      "s_add_u32 s69, s69, s70\n"
      "buffer_load_dwordx4 v49, s[4:7], s53 offen offset:1024 lds\n"
      "v_mfma_f32_32x32x16_bf16 v[112:127], v[176:179], v[188:191], v[112:127]\n"
      "ds_read_b64_tr_b16 v[160:161], v210 offset:4096\n"
      "s_mul_i32 s69, s69, 54\n"
      "s_add_u32 s69, s69, s68\n"
      "s_lshl_b32 s69, s69, 10\n"
      "s_add_u32 s86, s69, 9437184\n"
      "buffer_load_dwordx4 v50, s[4:7], s53 offen offset:2048 lds\n"
      "v_mfma_f32_32x32x16_bf16 v[128:143], v[180:183], v[184:187], v[128:143]\n"
      "ds_read_b64_tr_b16 v[162:163], v210 offset:5120\n"
      "ds_read_b64_tr_b16 v[164:165], v211 offset:4096\n"
      "s_add_u32 s69, s43, 4\n"
      "s_and_b32 s69, s69, 3\n"
      "s_lshl_b32 s70, s46, 2\n"
      "s_add_u32 s69, s69, s70\n"
      "buffer_load_dwordx4 v51, s[4:7], s53 offen offset:3072 lds\n"
      "v_mfma_f32_32x32x16_bf16 v[144:159], v[180:183], v[188:191], v[144:159]\n"
      "ds_read_b64_tr_b16 v[166:167], v211 offset:5120\n"
      "s_mul_i32 s69, s69, 54\n"
      "s_add_u32 s69, s69, s68\n"
      "s_lshl_b32 s69, s69, 10\n"
      "s_add_u32 s87, s69, 9437184\n"
      "s_waitcnt lgkmcnt(8)\n"
      "v_mfma_f32_32x32x16_bf16 v[96:111], v[192:195], v[200:203], v[96:111]\n"
      "ds_read_b128 v[184:187], v218 offset:0\n"
      "ds_read_b128 v[188:191], v218 offset:4096\n"
      "s_mul_i32 s98, s52, 221184\n"
      "s_add_u32 s54, s56, s98\n"
      "s_add_u32 m0, s57, 49152\n"
      "v_mfma_f32_32x32x16_bf16 v[112:127], v[192:195], v[204:207], v[112:127]\n"
      "ds_read_b64_tr_b16 v[176:177], v210 offset:8192\n"
      "buffer_load_dwordx4 v52, s[8:11], s54 offen lds\n"
      "s_waitcnt lgkmcnt(9)\n"
      "v_mfma_f32_32x32x16_bf16 v[128:143], v[196:199], v[200:203], v[128:143]\n"
      "ds_read_b64_tr_b16 v[178:179], v210 offset:9216\n"
      "ds_read_b64_tr_b16 v[180:181], v211 offset:8192\n"
      "buffer_load_dwordx4 v53, s[8:11], s54 offen offset:1024 lds\n"
      "v_mfma_f32_32x32x16_bf16 v[144:159], v[196:199], v[204:207], v[144:159]\n"
      "ds_read_b64_tr_b16 v[182:183], v211 offset:9216\n"
      "s_waitcnt lgkmcnt(8)\n"
      "v_mfma_f32_32x32x16_bf16 v[96:111], v[160:163], v[168:171], v[96:111]\n"
      "ds_read_b128 v[200:203], v219 offset:0\n"
      "ds_read_b128 v[204:207], v219 offset:4096\n"
      "buffer_load_dwordx4 v54, s[8:11], s54 offen offset:2048 lds\n"
      "v_bfe_u32 v40, v1, 4, 1\n"
      "v_mfma_f32_32x32x16_bf16 v[112:127], v[160:163], v[172:175], v[112:127]\n"
      "ds_read_b64_tr_b16 v[192:193], v210 offset:12288\n"
      "buffer_load_dwordx4 v55, s[8:11], s54 offen offset:3072 lds\n"
      "v_mul_u32_u24 v40, 55296, v40\n"
      "s_waitcnt lgkmcnt(9)\n"
      "v_mfma_f32_32x32x16_bf16 v[128:143], v[164:167], v[168:171], v[128:143]\n"
      "ds_read_b64_tr_b16 v[194:195], v210 offset:13312\n"
      "ds_read_b64_tr_b16 v[196:197], v211 offset:12288\n"
      "s_add_u32 s52, s52, 1\n"
      "s_cmp_eq_u32 s52, 10\n"
      "s_cselect_b32 s52, 0, s52\n"
      "v_lshrrev_b32 v41, 5, v1\n"
      "v_mfma_f32_32x32x16_bf16 v[144:159], v[164:167], v[172:175], v[144:159]\n"
      "ds_read_b64_tr_b16 v[198:199], v211 offset:13312\n"
      "s_waitcnt vmcnt(17) lgkmcnt(0)\n"
      "s_barrier\n"
      "v_mfma_f32_32x32x16_bf16 v[96:111], v[176:179], v[184:187], v[96:111]\n"
      "ds_read_b128 v[168:171], v216 offset:32768\n"
      "ds_read_b128 v[172:175], v216 offset:36864\n"
      "s_lshl_b32 s98, s52, 7\n"
      "s_add_u32 s53, s55, s98\n"
      "s_add_u32 m0, s57, 65536\n"
      "v_lshl_add_u32 v41, v41, 4, v4\n"
      "v_mfma_f32_32x32x16_bf16 v[112:127], v[176:179], v[188:191], v[112:127]\n"
      "ds_read_b64_tr_b16 v[160:161], v210 offset:32768\n"
      "v_lshl_add_u32 v35, v41, 4, v40\n"
      "v_mfma_f32_32x32x16_bf16 v[128:143], v[180:183], v[184:187], v[128:143]\n"
      "ds_read_b64_tr_b16 v[162:163], v210 offset:33792\n"
      "ds_read_b64_tr_b16 v[164:165], v211 offset:32768\n"
      "buffer_load_dwordx4 v48, s[4:7], s53 offen lds\n"
      "s_lshl_b32 s70, s48, 1\n"
      "s_add_u32 s70, s70, s51\n"
      "s_mul_i32 s70, s70, 216\n"
      "v_mfma_f32_32x32x16_bf16 v[144:159], v[180:183], v[188:191], v[144:159]\n"
      "ds_read_b64_tr_b16 v[166:167], v211 offset:33792\n"
      "s_add_u32 s71, s58, s50\n"
      "s_lshl_b32 s72, s71, 1\n"
      "s_add_u32 s70, s70, s72\n"
      "v_mfma_f32_32x32x16_bf16 v[96:111], v[192:195], v[200:203], v[96:111]\n"
      "ds_read_b128 v[184:187], v217 offset:32768\n"
      "ds_read_b128 v[188:191], v217 offset:36864\n"
      "buffer_load_dwordx4 v49, s[4:7], s53 offen offset:1024 lds\n"
      "s_lshl_b32 s70, s70, 10\n"
      "s_add_u32 s70, s70, 9437184\n"
      "v_mfma_f32_32x32x16_bf16 v[112:127], v[192:195], v[204:207], v[112:127]\n"
      "ds_read_b64_tr_b16 v[176:177], v210 offset:36864\n"
      "buffer_load_dwordx4 v50, s[4:7], s53 offen offset:2048 lds\n"
      "s_bfe_u32 s72, s44, 0x10001\n"
      "s_mul_i32 s72, s72, 14\n"
      "s_sub_u32 s72, 27, s72\n"
      "v_mfma_f32_32x32x16_bf16 v[128:143], v[196:199], v[200:203], v[128:143]\n"
      "ds_read_b64_tr_b16 v[178:179], v210 offset:37888\n"
      "ds_read_b64_tr_b16 v[180:181], v211 offset:36864\n"
      "buffer_load_dwordx4 v51, s[4:7], s53 offen offset:3072 lds\n"
      "s_lshl_b32 s98, s49, 1\n"
      "s_add_u32 s98, s98, s50\n"
      "s_cmp_eq_u32 s98, s72\n"
      "s_cselect_b32 s70, 2146435072, s70\n"
      "v_mfma_f32_32x32x16_bf16 v[144:159], v[196:199], v[204:207], v[144:159]\n"
      "ds_read_b64_tr_b16 v[182:183], v211 offset:37888\n"
      "s_add_u32 s71, s70, 110592\n"
      "s_waitcnt lgkmcnt(8)\n"
      "v_mfma_f32_32x32x16_bf16 v[96:111], v[160:163], v[168:171], v[96:111]\n"
      "ds_read_b128 v[200:203], v218 offset:32768\n"
      "ds_read_b128 v[204:207], v218 offset:36864\n"
      "s_mul_i32 s98, s52, 221184\n"
      "s_add_u32 s54, s56, s98\n"
      "s_add_u32 m0, s57, 81920\n"
      "v_mfma_f32_32x32x16_bf16 v[112:127], v[160:163], v[172:175], v[112:127]\n"
      "ds_read_b64_tr_b16 v[192:193], v210 offset:40960\n"
      "buffer_load_dwordx4 v52, s[8:11], s54 offen lds\n"
      "s_waitcnt lgkmcnt(9)\n"
      "v_mfma_f32_32x32x16_bf16 v[128:143], v[164:167], v[168:171], v[128:143]\n"
      "ds_read_b64_tr_b16 v[194:195], v210 offset:41984\n"
      "ds_read_b64_tr_b16 v[196:197], v211 offset:40960\n"
      "buffer_load_dwordx4 v53, s[8:11], s54 offen offset:1024 lds\n"
      "v_mfma_f32_32x32x16_bf16 v[144:159], v[164:167], v[172:175], v[144:159]\n"
      "ds_read_b64_tr_b16 v[198:199], v211 offset:41984\n"
      "s_waitcnt lgkmcnt(8)\n"
      "v_mfma_f32_32x32x16_bf16 v[96:111], v[176:179], v[184:187], v[96:111]\n"
      "ds_read_b128 v[168:171], v219 offset:32768\n"
      "ds_read_b128 v[172:175], v219 offset:36864\n"
      "buffer_load_dwordx4 v54, s[8:11], s54 offen offset:2048 lds\n"
      "v_mfma_f32_32x32x16_bf16 v[112:127], v[176:179], v[188:191], v[112:127]\n"
      "ds_read_b64_tr_b16 v[160:161], v210 offset:45056\n"
      "buffer_load_dwordx4 v55, s[8:11], s54 offen offset:3072 lds\n"
      "s_waitcnt lgkmcnt(9)\n"
      "v_mfma_f32_32x32x16_bf16 v[128:143], v[180:183], v[184:187], v[128:143]\n"
      "ds_read_b64_tr_b16 v[162:163], v210 offset:46080\n"
      "ds_read_b64_tr_b16 v[164:165], v211 offset:45056\n"
      "s_add_u32 s52, s52, 1\n"
      "s_cmp_eq_u32 s52, 10\n"
      "s_cselect_b32 s52, 0, s52\n"
      "v_mfma_f32_32x32x16_bf16 v[144:159], v[180:183], v[188:191], v[144:159]\n"
      "ds_read_b64_tr_b16 v[166:167], v211 offset:46080\n"
      "s_waitcnt vmcnt(17) lgkmcnt(0)\n"
      "s_barrier\n"
      "v_mfma_f32_32x32x16_bf16 v[96:111], v[192:195], v[200:203], v[96:111]\n"
      "ds_read_b128 v[184:187], v212 offset:0\n"
      "ds_read_b128 v[188:191], v212 offset:4096\n"
      "s_lshl_b32 s98, s52, 7\n"
      "s_add_u32 s53, s55, s98\n"
      "s_add_u32 m0, s57, 98304\n"
      "v_mfma_f32_32x32x16_bf16 v[112:127], v[192:195], v[204:207], v[112:127]\n"
      "ds_read_b64_tr_b16 v[176:177], v208\n"
      "v_mfma_f32_32x32x16_bf16 v[128:143], v[196:199], v[200:203], v[128:143]\n"
      "ds_read_b64_tr_b16 v[178:179], v208 offset:1024\n"
      "ds_read_b64_tr_b16 v[180:181], v209\n"
      "buffer_load_dwordx4 v48, s[4:7], s53 offen lds\n"
      "v_mfma_f32_32x32x16_bf16 v[144:159], v[196:199], v[204:207], v[144:159]\n"
      "ds_read_b64_tr_b16 v[182:183], v209 offset:1024\n"
      "v_mfma_f32_32x32x16_bf16 v[96:111], v[160:163], v[168:171], v[96:111]\n"
      "ds_read_b128 v[200:203], v213 offset:0\n"
      "ds_read_b128 v[204:207], v213 offset:4096\n"
      "buffer_load_dwordx4 v49, s[4:7], s53 offen offset:1024 lds\n"
      "v_mfma_f32_32x32x16_bf16 v[112:127], v[160:163], v[172:175], v[112:127]\n"
      "ds_read_b64_tr_b16 v[192:193], v208 offset:4096\n"
      "buffer_load_dwordx4 v50, s[4:7], s53 offen offset:2048 lds\n"
      "v_mfma_f32_32x32x16_bf16 v[128:143], v[164:167], v[168:171], v[128:143]\n"
      "ds_read_b64_tr_b16 v[194:195], v208 offset:5120\n"
      "ds_read_b64_tr_b16 v[196:197], v209 offset:4096\n"
      "buffer_load_dwordx4 v51, s[4:7], s53 offen offset:3072 lds\n"
      "v_mfma_f32_32x32x16_bf16 v[144:159], v[164:167], v[172:175], v[144:159]\n"
      "ds_read_b64_tr_b16 v[198:199], v209 offset:5120\n"
      "s_waitcnt lgkmcnt(8)\n"
      "v_mfma_f32_32x32x16_bf16 v[96:111], v[176:179], v[184:187], v[96:111]\n"
      "ds_read_b128 v[168:171], v214 offset:0\n"
      "ds_read_b128 v[172:175], v214 offset:4096\n"
      "s_mul_i32 s98, s52, 221184\n"
      "s_add_u32 s54, s56, s98\n"
      "s_add_u32 m0, s57, 114688\n"
      "v_mfma_f32_32x32x16_bf16 v[112:127], v[176:179], v[188:191], v[112:127]\n"
      "ds_read_b64_tr_b16 v[160:161], v208 offset:8192\n"
      "buffer_load_dwordx4 v52, s[8:11], s54 offen lds\n"
      "s_waitcnt lgkmcnt(9)\n"
      "v_mfma_f32_32x32x16_bf16 v[128:143], v[180:183], v[184:187], v[128:143]\n"
      "ds_read_b64_tr_b16 v[162:163], v208 offset:9216\n"
      "ds_read_b64_tr_b16 v[164:165], v209 offset:8192\n"
      "buffer_load_dwordx4 v53, s[8:11], s54 offen offset:1024 lds\n"
      "v_mfma_f32_32x32x16_bf16 v[144:159], v[180:183], v[188:191], v[144:159]\n"
      "ds_read_b64_tr_b16 v[166:167], v209 offset:9216\n"
      "s_waitcnt lgkmcnt(8)\n"
      "v_mfma_f32_32x32x16_bf16 v[96:111], v[192:195], v[200:203], v[96:111]\n"
      "ds_read_b128 v[184:187], v215 offset:0\n"
      "ds_read_b128 v[188:191], v215 offset:4096\n"
      "buffer_load_dwordx4 v54, s[8:11], s54 offen offset:2048 lds\n"
      "v_mfma_f32_32x32x16_bf16 v[112:127], v[192:195], v[204:207], v[112:127]\n"
      "ds_read_b64_tr_b16 v[176:177], v208 offset:12288\n"
      "buffer_load_dwordx4 v55, s[8:11], s54 offen offset:3072 lds\n"
      "s_waitcnt lgkmcnt(9)\n"
      "v_mfma_f32_32x32x16_bf16 v[128:143], v[196:199], v[200:203], v[128:143]\n"
      "ds_read_b64_tr_b16 v[178:179], v208 offset:13312\n"
      "ds_read_b64_tr_b16 v[180:181], v209 offset:12288\n"
      "s_add_u32 s52, s52, 1\n"
      "s_cmp_eq_u32 s52, 10\n"
      "s_cselect_b32 s52, 0, s52\n"
      "v_mfma_f32_32x32x16_bf16 v[144:159], v[196:199], v[204:207], v[144:159]\n"
      "ds_read_b64_tr_b16 v[182:183], v209 offset:13312\n"
      "s_waitcnt vmcnt(16) lgkmcnt(0)\n"
      "s_barrier\n"
      "s_cmp_lg_u32 s43, 0\n"
      "s_cbranch_scc1 .Lw0_9\n"
      "global_load_dwordx2 v[12:13], v19, s[16:17] sc1\n"
      "global_load_dwordx2 v[14:15], v18, s[16:17] sc1\n"
      "s_nop 4\n"
      ".Lw0_9:\n"
      "v_mfma_f32_32x32x16_bf16 v[96:111], v[160:163], v[168:171], v[96:111]\n"
      "ds_read_b128 v[200:203], v212 offset:32768\n"
      "ds_read_b128 v[204:207], v212 offset:36864\n"
      "s_lshl_b32 s98, s52, 7\n"
      "s_add_u32 s53, s55, s98\n"
      "s_add_u32 m0, s57, 0\n"
      "v_mfma_f32_32x32x16_bf16 v[112:127], v[160:163], v[172:175], v[112:127]\n"
      "ds_read_b64_tr_b16 v[192:193], v208 offset:32768\n"
      "v_mfma_f32_32x32x16_bf16 v[128:143], v[164:167], v[168:171], v[128:143]\n"
      "ds_read_b64_tr_b16 v[194:195], v208 offset:33792\n"
      "ds_read_b64_tr_b16 v[196:197], v209 offset:32768\n"
      "buffer_load_dwordx4 v48, s[4:7], s53 offen lds\n"
      "v_mfma_f32_32x32x16_bf16 v[144:159], v[164:167], v[172:175], v[144:159]\n"
      "ds_read_b64_tr_b16 v[198:199], v209 offset:33792\n"
      "v_mfma_f32_32x32x16_bf16 v[96:111], v[176:179], v[184:187], v[96:111]\n"
      "ds_read_b128 v[168:171], v213 offset:32768\n"
      "ds_read_b128 v[172:175], v213 offset:36864\n"
      "buffer_load_dwordx4 v49, s[4:7], s53 offen offset:1024 lds\n"
      "v_mfma_f32_32x32x16_bf16 v[112:127], v[176:179], v[188:191], v[112:127]\n"
      "ds_read_b64_tr_b16 v[160:161], v208 offset:36864\n"
      "buffer_load_dwordx4 v50, s[4:7], s53 offen offset:2048 lds\n"
      "v_mfma_f32_32x32x16_bf16 v[128:143], v[180:183], v[184:187], v[128:143]\n"
      "ds_read_b64_tr_b16 v[162:163], v208 offset:37888\n"
      "ds_read_b64_tr_b16 v[164:165], v209 offset:36864\n"
      "buffer_load_dwordx4 v51, s[4:7], s53 offen offset:3072 lds\n"
      "v_mfma_f32_32x32x16_bf16 v[144:159], v[180:183], v[188:191], v[144:159]\n"
      "ds_read_b64_tr_b16 v[166:167], v209 offset:37888\n"
      "s_waitcnt lgkmcnt(8)\n"
      "v_mfma_f32_32x32x16_bf16 v[96:111], v[192:195], v[200:203], v[96:111]\n"
      "ds_read_b128 v[184:187], v214 offset:32768\n"
      "ds_read_b128 v[188:191], v214 offset:36864\n"
      "s_mul_i32 s98, s52, 221184\n"
      "s_add_u32 s54, s56, s98\n"
      "s_add_u32 m0, s57, 16384\n"
      "v_mfma_f32_32x32x16_bf16 v[112:127], v[192:195], v[204:207], v[112:127]\n"
      "ds_read_b64_tr_b16 v[176:177], v208 offset:40960\n"
      "buffer_load_dwordx4 v52, s[8:11], s54 offen lds\n"
      "s_waitcnt lgkmcnt(9)\n"
      "v_mfma_f32_32x32x16_bf16 v[128:143], v[196:199], v[200:203], v[128:143]\n"
      "ds_read_b64_tr_b16 v[178:179], v208 offset:41984\n"
      "ds_read_b64_tr_b16 v[180:181], v209 offset:40960\n"
      "buffer_load_dwordx4 v53, s[8:11], s54 offen offset:1024 lds\n"
      "v_mfma_f32_32x32x16_bf16 v[144:159], v[196:199], v[204:207], v[144:159]\n"
      "ds_read_b64_tr_b16 v[182:183], v209 offset:41984\n"
      "s_waitcnt lgkmcnt(8)\n"
      "v_mfma_f32_32x32x16_bf16 v[96:111], v[160:163], v[168:171], v[96:111]\n"
      "ds_read_b128 v[200:203], v215 offset:32768\n"
      "ds_read_b128 v[204:207], v215 offset:36864\n"
      "buffer_load_dwordx4 v54, s[8:11], s54 offen offset:2048 lds\n"
      "v_mfma_f32_32x32x16_bf16 v[112:127], v[160:163], v[172:175], v[112:127]\n"
      "ds_read_b64_tr_b16 v[192:193], v208 offset:45056\n"
      "buffer_load_dwordx4 v55, s[8:11], s54 offen offset:3072 lds\n"
      "s_waitcnt lgkmcnt(9)\n"
      "v_mfma_f32_32x32x16_bf16 v[128:143], v[164:167], v[168:171], v[128:143]\n"
      "ds_read_b64_tr_b16 v[194:195], v208 offset:46080\n"
      "ds_read_b64_tr_b16 v[196:197], v209 offset:45056\n"
      "s_add_u32 s52, s52, 1\n"
      "s_cmp_eq_u32 s52, 10\n"
      "s_cselect_b32 s52, 0, s52\n"
      "v_mfma_f32_32x32x16_bf16 v[144:159], v[164:167], v[172:175], v[144:159]\n"
      "ds_read_b64_tr_b16 v[198:199], v209 offset:46080\n"
      "s_waitcnt vmcnt(16) lgkmcnt(0)\n"
      "s_barrier\n"
      "v_mfma_f32_32x32x16_bf16 v[96:111], v[176:179], v[184:187], v[96:111]\n"
      "ds_read_b128 v[168:171], v216 offset:0\n"
      "ds_read_b128 v[172:175], v216 offset:4096\n"
      "s_lshl_b32 s98, s52, 7\n"
      "s_add_u32 s53, s55, s98\n"
      "s_add_u32 m0, s57, 32768\n"
      "v_mfma_f32_32x32x16_bf16 v[112:127], v[176:179], v[188:191], v[112:127]\n"
      "ds_read_b64_tr_b16 v[160:161], v210\n"
      "v_mfma_f32_32x32x16_bf16 v[128:143], v[180:183], v[184:187], v[128:143]\n"
      "ds_read_b64_tr_b16 v[162:163], v210 offset:1024\n"
      "ds_read_b64_tr_b16 v[164:165], v211\n"
      "buffer_load_dwordx4 v48, s[4:7], s53 offen lds\n"
      "v_mfma_f32_32x32x16_bf16 v[144:159], v[180:183], v[188:191], v[144:159]\n"
      "ds_read_b64_tr_b16 v[166:167], v211 offset:1024\n"
      "v_mfma_f32_32x32x16_bf16 v[96:111], v[192:195], v[200:203], v[96:111]\n"
      "ds_read_b128 v[184:187], v217 offset:0\n"
      "ds_read_b128 v[188:191], v217 offset:4096\n"
      "buffer_load_dwordx4 v49, s[4:7], s53 offen offset:1024 lds\n"
      "v_mfma_f32_32x32x16_bf16 v[112:127], v[192:195], v[204:207], v[112:127]\n"
      "ds_read_b64_tr_b16 v[176:177], v210 offset:4096\n"
      "buffer_load_dwordx4 v50, s[4:7], s53 offen offset:2048 lds\n"
      "v_mfma_f32_32x32x16_bf16 v[128:143], v[196:199], v[200:203], v[128:143]\n"
      "ds_read_b64_tr_b16 v[178:179], v210 offset:5120\n"
      "ds_read_b64_tr_b16 v[180:181], v211 offset:4096\n"
      "buffer_load_dwordx4 v51, s[4:7], s53 offen offset:3072 lds\n"
      "v_mfma_f32_32x32x16_bf16 v[144:159], v[196:199], v[204:207], v[144:159]\n"
      "ds_read_b64_tr_b16 v[182:183], v211 offset:5120\n"
      "s_waitcnt lgkmcnt(8)\n"
      "v_mfma_f32_32x32x16_bf16 v[96:111], v[160:163], v[168:171], v[96:111]\n"
      "ds_read_b128 v[200:203], v218 offset:0\n"
      "ds_read_b128 v[204:207], v218 offset:4096\n"
      "s_mul_i32 s98, s52, 221184\n"
      "s_add_u32 s54, s56, s98\n"
      "s_add_u32 m0, s57, 49152\n"
      "v_mfma_f32_32x32x16_bf16 v[112:127], v[160:163], v[172:175], v[112:127]\n"
      "ds_read_b64_tr_b16 v[192:193], v210 offset:8192\n"
      "buffer_load_dwordx4 v52, s[8:11], s54 offen lds\n"
      "s_waitcnt lgkmcnt(9)\n"
      "v_mfma_f32_32x32x16_bf16 v[128:143], v[164:167], v[168:171], v[128:143]\n"
      "ds_read_b64_tr_b16 v[194:195], v210 offset:9216\n"
      "ds_read_b64_tr_b16 v[196:197], v211 offset:8192\n"
      "buffer_load_dwordx4 v53, s[8:11], s54 offen offset:1024 lds\n"
      "v_mfma_f32_32x32x16_bf16 v[144:159], v[164:167], v[172:175], v[144:159]\n"
      "ds_read_b64_tr_b16 v[198:199], v211 offset:9216\n"
      "s_waitcnt lgkmcnt(8)\n"
      "v_mfma_f32_32x32x16_bf16 v[96:111], v[176:179], v[184:187], v[96:111]\n"
      "ds_read_b128 v[168:171], v219 offset:0\n"
      "ds_read_b128 v[172:175], v219 offset:4096\n"
      "buffer_load_dwordx4 v54, s[8:11], s54 offen offset:2048 lds\n"
      "v_mfma_f32_32x32x16_bf16 v[112:127], v[176:179], v[188:191], v[112:127]\n"
      "ds_read_b64_tr_b16 v[160:161], v210 offset:12288\n"
      "buffer_load_dwordx4 v55, s[8:11], s54 offen offset:3072 lds\n"
      "s_waitcnt lgkmcnt(9)\n"
      "v_mfma_f32_32x32x16_bf16 v[128:143], v[180:183], v[184:187], v[128:143]\n"
      "ds_read_b64_tr_b16 v[162:163], v210 offset:13312\n"
      "ds_read_b64_tr_b16 v[164:165], v211 offset:12288\n"
      "s_add_u32 s52, s52, 1\n"
      "s_cmp_eq_u32 s52, 10\n"
      "s_cselect_b32 s52, 0, s52\n"
      "v_mfma_f32_32x32x16_bf16 v[144:159], v[180:183], v[188:191], v[144:159]\n"
      "ds_read_b64_tr_b16 v[166:167], v211 offset:13312\n"
      "s_waitcnt vmcnt(16) lgkmcnt(0)\n"
      "s_barrier\n"
      "v_mfma_f32_32x32x16_bf16 v[96:111], v[192:195], v[200:203], v[96:111]\n"
      "ds_read_b128 v[184:187], v216 offset:32768\n"
      "ds_read_b128 v[188:191], v216 offset:36864\n"
      "v_mfma_f32_32x32x16_bf16 v[112:127], v[192:195], v[204:207], v[112:127]\n"
      "ds_read_b64_tr_b16 v[176:177], v210 offset:32768\n"
      "v_mfma_f32_32x32x16_bf16 v[128:143], v[196:199], v[200:203], v[128:143]\n"
      "ds_read_b64_tr_b16 v[178:179], v210 offset:33792\n"
      "ds_read_b64_tr_b16 v[180:181], v211 offset:32768\n"
      "v_mfma_f32_32x32x16_bf16 v[144:159], v[196:199], v[204:207], v[144:159]\n"
      "ds_read_b64_tr_b16 v[182:183], v211 offset:33792\n"
      "v_mfma_f32_32x32x16_bf16 v[96:111], v[160:163], v[168:171], v[96:111]\n"
      "ds_read_b128 v[200:203], v217 offset:32768\n"
      "ds_read_b128 v[204:207], v217 offset:36864\n"
      "v_mfma_f32_32x32x16_bf16 v[112:127], v[160:163], v[172:175], v[112:127]\n"
      "ds_read_b64_tr_b16 v[192:193], v210 offset:36864\n"
      "v_mfma_f32_32x32x16_bf16 v[128:143], v[164:167], v[168:171], v[128:143]\n"
      "ds_read_b64_tr_b16 v[194:195], v210 offset:37888\n"
      "ds_read_b64_tr_b16 v[196:197], v211 offset:36864\n"
      "v_mfma_f32_32x32x16_bf16 v[144:159], v[164:167], v[172:175], v[144:159]\n"
      "ds_read_b64_tr_b16 v[198:199], v211 offset:37888\n"
      "s_waitcnt lgkmcnt(8)\n"
      "v_mfma_f32_32x32x16_bf16 v[96:111], v[176:179], v[184:187], v[96:111]\n"
      "ds_read_b128 v[168:171], v218 offset:32768\n"
      "ds_read_b128 v[172:175], v218 offset:36864\n"
      "v_mfma_f32_32x32x16_bf16 v[112:127], v[176:179], v[188:191], v[112:127]\n"
      "ds_read_b64_tr_b16 v[160:161], v210 offset:40960\n"
      "s_waitcnt lgkmcnt(9)\n"
      "v_mfma_f32_32x32x16_bf16 v[128:143], v[180:183], v[184:187], v[128:143]\n"
      "ds_read_b64_tr_b16 v[162:163], v210 offset:41984\n"
      "ds_read_b64_tr_b16 v[164:165], v211 offset:40960\n"
      "v_mfma_f32_32x32x16_bf16 v[144:159], v[180:183], v[188:191], v[144:159]\n"
      "ds_read_b64_tr_b16 v[166:167], v211 offset:41984\n"
      "s_waitcnt lgkmcnt(8)\n"
      "v_mfma_f32_32x32x16_bf16 v[96:111], v[192:195], v[200:203], v[96:111]\n"
      "ds_read_b128 v[184:187], v219 offset:32768\n"
      "ds_read_b128 v[188:191], v219 offset:36864\n"
      "v_mfma_f32_32x32x16_bf16 v[112:127], v[192:195], v[204:207], v[112:127]\n"
      "ds_read_b64_tr_b16 v[176:177], v210 offset:45056\n"
      "s_waitcnt lgkmcnt(9)\n"
      "v_mfma_f32_32x32x16_bf16 v[128:143], v[196:199], v[200:203], v[128:143]\n"
      "ds_read_b64_tr_b16 v[178:179], v210 offset:46080\n"
      "ds_read_b64_tr_b16 v[180:181], v211 offset:45056\n"
      "v_mfma_f32_32x32x16_bf16 v[144:159], v[196:199], v[204:207], v[144:159]\n"
      "ds_read_b64_tr_b16 v[182:183], v211 offset:46080\n"
      "s_cmp_lg_u32 s43, 0\n"
      "s_cbranch_scc1 .Lw0_10\n"
      "v_cmp_eq_u64 vcc, s[40:41], v[12:13]\n"
      "s_nop 0\n"
      "s_cmp_eq_u64 vcc, -1\n"
      "s_cbranch_scc1 .Lok_11\n"
      "s_mov_b32 s82, 0x20000\n"
      ".Lsp_12:\n"
      "s_sleep 4\n"
      "global_load_dwordx2 v[12:13], v19, s[16:17] sc1\n"
      "s_waitcnt vmcnt(0)\n"
      "v_cmp_eq_u64 vcc, s[40:41], v[12:13]\n"
      "s_nop 0\n"
      "s_cmp_eq_u64 vcc, -1\n"
      "s_cbranch_scc1 .Lok_11\n"
      "s_sub_u32 s82, s82, 1\n"
      "s_cmp_lg_u32 s82, 0\n"
      "s_cbranch_scc1 .Lsp_12\n"
      ".Lok_11:\n"
      "v_cmp_eq_u64 vcc, s[40:41], v[14:15]\n"
      "s_nop 0\n"
      "s_cmp_eq_u64 vcc, -1\n"
      "s_cbranch_scc1 .Lok_13\n"
      "s_mov_b32 s82, 0x20000\n"
      ".Lsp_14:\n"
      "s_sleep 4\n"
      "global_load_dwordx2 v[14:15], v18, s[16:17] sc1\n"
      "s_waitcnt vmcnt(0)\n"
      "v_cmp_eq_u64 vcc, s[40:41], v[14:15]\n"
      "s_nop 0\n"
      "s_cmp_eq_u64 vcc, -1\n"
      "s_cbranch_scc1 .Lok_13\n"
      "s_sub_u32 s82, s82, 1\n"
      "s_cmp_lg_u32 s82, 0\n"
      "s_cbranch_scc1 .Lsp_14\n"
      ".Lok_13:\n"
      ".Lw0_10:\n"
      "s_waitcnt vmcnt(8) lgkmcnt(0)\n"
      "s_barrier\n"
      "v_mfma_f32_32x32x16_bf16 v[96:111], v[160:163], v[168:171], v[96:111]\n"
      "ds_read_b128 v[200:203], v212 offset:0\n"
      "ds_read_b128 v[204:207], v212 offset:4096\n"
      "s_mul_i32 s68, s63, 4224\n"
      "s_lshl_b32 s69, s50, 2\n"
      "s_add_u32 s68, s68, s69\n"
      "s_add_u32 s68, s68, 262240\n"
      "v_mfma_f32_32x32x16_bf16 v[112:127], v[160:163], v[172:175], v[112:127]\n"
      "ds_read_b64_tr_b16 v[192:193], v208\n"
      "s_mul_i32 s69, s43, 10368\n"
      "s_add_u32 s69, s69, s66\n"
      "s_mov_b32 m0, s69\n"
      "v_mfma_f32_32x32x16_bf16 v[128:143], v[164:167], v[168:171], v[128:143]\n"
      "ds_read_b64_tr_b16 v[194:195], v208 offset:1024\n"
      "ds_read_b64_tr_b16 v[196:197], v209\n"
      "buffer_load_dwordx4 v2, s[16:19], s68 offen sc1 lds\n"
      "v_mfma_f32_32x32x16_bf16 v[144:159], v[164:167], v[172:175], v[144:159]\n"
      "ds_read_b64_tr_b16 v[198:199], v209 offset:1024\n"
      "v_mfma_f32_32x32x16_bf16 v[96:111], v[176:179], v[184:187], v[96:111]\n"
      "ds_read_b128 v[168:171], v213 offset:0\n"
      "ds_read_b128 v[172:175], v213 offset:4096\n"
      "buffer_load_dwordx4 v2, s[16:19], s68 offen offset:1024 sc1 lds\n"
      "v_mfma_f32_32x32x16_bf16 v[112:127], v[176:179], v[188:191], v[112:127]\n"
      "ds_read_b64_tr_b16 v[160:161], v208 offset:4096\n"
      "buffer_load_dwordx4 v2, s[16:19], s68 offen offset:2048 sc1 lds\n"
      "v_mfma_f32_32x32x16_bf16 v[128:143], v[180:183], v[184:187], v[128:143]\n"
      "ds_read_b64_tr_b16 v[162:163], v208 offset:5120\n"
      "ds_read_b64_tr_b16 v[164:165], v209 offset:4096\n"
      "buffer_load_dwordx4 v2, s[16:19], s68 offen offset:3072 sc1 lds\n"
      "v_mfma_f32_32x32x16_bf16 v[144:159], v[180:183], v[188:191], v[144:159]\n"
      "ds_read_b64_tr_b16 v[166:167], v209 offset:5120\n"
      "s_add_u32 m0, s69, 4096\n"
      "s_nop 0\n"
      "buffer_load_dwordx4 v37, s[16:19], s68 offen sc1 lds\n"
      "s_waitcnt lgkmcnt(8)\n"
      "v_mfma_f32_32x32x16_bf16 v[96:111], v[192:195], v[200:203], v[96:111]\n"
      "ds_read_b128 v[184:187], v214 offset:0\n"
      "ds_read_b128 v[188:191], v214 offset:4096\n"
      "s_add_u32 s68, s68, 2\n"
      "s_add_u32 s69, s69, 5184\n"
      "s_mov_b32 m0, s69\n"
      "v_mfma_f32_32x32x16_bf16 v[112:127], v[192:195], v[204:207], v[112:127]\n"
      "ds_read_b64_tr_b16 v[176:177], v208 offset:8192\n"
      "buffer_load_dwordx4 v2, s[16:19], s68 offen sc1 lds\n"
      "s_waitcnt lgkmcnt(9)\n"
      "v_mfma_f32_32x32x16_bf16 v[128:143], v[196:199], v[200:203], v[128:143]\n"
      "ds_read_b64_tr_b16 v[178:179], v208 offset:9216\n"
      "ds_read_b64_tr_b16 v[180:181], v209 offset:8192\n"
      "buffer_load_dwordx4 v2, s[16:19], s68 offen offset:1024 sc1 lds\n"
      "v_mfma_f32_32x32x16_bf16 v[144:159], v[196:199], v[204:207], v[144:159]\n"
      "ds_read_b64_tr_b16 v[182:183], v209 offset:9216\n"
      "s_waitcnt lgkmcnt(8)\n"
      "v_mfma_f32_32x32x16_bf16 v[96:111], v[160:163], v[168:171], v[96:111]\n"
      "ds_read_b128 v[200:203], v215 offset:0\n"
      "ds_read_b128 v[204:207], v215 offset:4096\n"
      "buffer_load_dwordx4 v2, s[16:19], s68 offen offset:2048 sc1 lds\n"
      "v_mfma_f32_32x32x16_bf16 v[112:127], v[160:163], v[172:175], v[112:127]\n"
      "ds_read_b64_tr_b16 v[192:193], v208 offset:12288\n"
      "buffer_load_dwordx4 v2, s[16:19], s68 offen offset:3072 sc1 lds\n"
      "s_waitcnt lgkmcnt(9)\n"
      "v_mfma_f32_32x32x16_bf16 v[128:143], v[164:167], v[168:171], v[128:143]\n"
      "ds_read_b64_tr_b16 v[194:195], v208 offset:13312\n"
      "ds_read_b64_tr_b16 v[196:197], v209 offset:12288\n"
      "s_add_u32 m0, s69, 4096\n"
      "s_nop 0\n"
      "buffer_load_dwordx4 v37, s[16:19], s68 offen sc1 lds\n"
      "v_mfma_f32_32x32x16_bf16 v[144:159], v[164:167], v[172:175], v[144:159]\n"
      "ds_read_b64_tr_b16 v[198:199], v209 offset:13312\n"
      "s_mul_i32 s59, s44, 80\n"
      "s_min_u32 s98, s45, 23\n"
      "s_add_u32 s59, s59, s98\n"
      "s_add_u32 s59, s59, 56\n"
      "s_waitcnt vmcnt(10) lgkmcnt(0)\n"
      "s_barrier\n"
      "v_mfma_f32_32x32x16_bf16 v[96:111], v[176:179], v[184:187], v[96:111]\n"
      "ds_read_b128 v[168:171], v212 offset:32768\n"
      "ds_read_b128 v[172:175], v212 offset:36864\n"
      "s_mul_i32 s98, s59, 4224\n"
      "s_lshl_b32 s99, s43, 1\n"
      "s_add_u32 s98, s98, s99\n"
      "s_add_u32 s53, s98, 262240\n"
      "v_mfma_f32_32x32x16_bf16 v[112:127], v[176:179], v[188:191], v[112:127]\n"
      "ds_read_b64_tr_b16 v[160:161], v208 offset:32768\n"
      "s_mul_i32 s98, s43, 5184\n"
      "s_add_u32 s54, s98, 107008\n"
      "s_mov_b32 m0, s54\n"
      "v_mfma_f32_32x32x16_bf16 v[128:143], v[180:183], v[184:187], v[128:143]\n"
      "ds_read_b64_tr_b16 v[162:163], v208 offset:33792\n"
      "ds_read_b64_tr_b16 v[164:165], v209 offset:32768\n"
      "buffer_load_dwordx4 v2, s[16:19], s53 offen sc1 lds\n"
      "v_mfma_f32_32x32x16_bf16 v[144:159], v[180:183], v[188:191], v[144:159]\n"
      "ds_read_b64_tr_b16 v[166:167], v209 offset:33792\n"
      "v_mfma_f32_32x32x16_bf16 v[96:111], v[192:195], v[200:203], v[96:111]\n"
      "ds_read_b128 v[184:187], v213 offset:32768\n"
      "ds_read_b128 v[188:191], v213 offset:36864\n"
      "buffer_load_dwordx4 v2, s[16:19], s53 offen offset:1024 sc1 lds\n"
      "v_mfma_f32_32x32x16_bf16 v[112:127], v[192:195], v[204:207], v[112:127]\n"
      "ds_read_b64_tr_b16 v[176:177], v208 offset:36864\n"
      "buffer_load_dwordx4 v2, s[16:19], s53 offen offset:2048 sc1 lds\n"
      "v_mfma_f32_32x32x16_bf16 v[128:143], v[196:199], v[200:203], v[128:143]\n"
      "ds_read_b64_tr_b16 v[178:179], v208 offset:37888\n"
      "ds_read_b64_tr_b16 v[180:181], v209 offset:36864\n"
      "buffer_load_dwordx4 v2, s[16:19], s53 offen offset:3072 sc1 lds\n"
      "v_mfma_f32_32x32x16_bf16 v[144:159], v[196:199], v[204:207], v[144:159]\n"
      "ds_read_b64_tr_b16 v[182:183], v209 offset:37888\n"
      "s_add_u32 m0, s54, 4096\n"
      "s_nop 0\n"
      "buffer_load_dwordx4 v37, s[16:19], s53 offen sc1 lds\n"
      "s_waitcnt lgkmcnt(8)\n"
      "v_mfma_f32_32x32x16_bf16 v[96:111], v[160:163], v[168:171], v[96:111]\n"
      "ds_read_b128 v[200:203], v214 offset:32768\n"
      "ds_read_b128 v[204:207], v214 offset:36864\n"
      "ds_read_b64_tr_b16 v[192:193], v208 offset:40960\n"
      "ds_read_b64_tr_b16 v[194:195], v208 offset:41984\n"
      "ds_read_b64_tr_b16 v[196:197], v209 offset:40960\n"
      "ds_read_b64_tr_b16 v[198:199], v209 offset:41984\n"
      "s_waitcnt lgkmcnt(8)\n"
      "v_mfma_f32_32x32x16_bf16 v[96:111], v[176:179], v[184:187], v[96:111]\n"
      "ds_read_b128 v[72:75], v215 offset:32768\n"
      "ds_read_b128 v[76:79], v215 offset:36864\n"
      "ds_read_b64_tr_b16 v[64:65], v208 offset:45056\n"
      "ds_read_b64_tr_b16 v[66:67], v208 offset:46080\n"
      "ds_read_b64_tr_b16 v[68:69], v209 offset:45056\n"
      "ds_read_b64_tr_b16 v[70:71], v209 offset:46080\n"
      "s_waitcnt lgkmcnt(0)\n"
      "s_barrier\n"
      "v_mfma_f32_32x32x16_bf16 v[96:111], v[192:195], v[200:203], v[96:111]\n"
      "s_min_u32 s98, s45, 23\n"
      "s_mul_i32 s98, s98, 43\n"
      "s_lshr_b32 s98, s98, 8\n"
      "s_add_u32 s98, s98, 28\n"
      "s_lshl_b32 s98, s98, 3\n"
      "s_add_u32 s98, s98, s44\n"
      "s_lshl_b32 s98, s98, 7\n"
      "s_add_u32 s98, s98, 229376\n"
      "v_mov_b32 v18, s98\n"
      "global_load_dwordx2 v[10:11], v18, s[16:17] sc1\n"
      "s_add_u32 s62, s60, 0\n"
      "s_sub_u32 s73, s62, 14\n"
      "s_min_u32 s62, s62, s73\n"
      "v_mfma_f32_32x32x16_bf16 v[96:111], v[64:67], v[72:75], v[96:111]\n"
      "s_mul_i32 s73, s62, 40960\n"
      "s_add_u32 s73, s73, s64\n"
      "s_cmp_eq_u32 s62, s61\n"
      "s_cselect_b32 s73, 2146435072, s73\n"
      "v_mfma_f32_32x32x16_bf16 v[112:127], v[160:163], v[172:175], v[112:127]\n"
      "buffer_load_dwordx4 a[0:3], v2, s[16:19], s73 offen sc1\n"
      "v_mfma_f32_32x32x16_bf16 v[112:127], v[176:179], v[188:191], v[112:127]\n"
      "buffer_load_dwordx4 a[4:7], v2, s[16:19], s73 offen offset:1024 sc1\n"
      "v_mfma_f32_32x32x16_bf16 v[112:127], v[192:195], v[204:207], v[112:127]\n"
      "buffer_load_dwordx4 a[8:11], v2, s[16:19], s73 offen offset:2048 sc1\n"
      "v_mfma_f32_32x32x16_bf16 v[112:127], v[64:67], v[76:79], v[112:127]\n"
      "buffer_load_dwordx4 a[12:15], v2, s[16:19], s73 offen offset:3072 sc1\n"
      "v_mfma_f32_32x32x16_bf16 v[128:143], v[164:167], v[168:171], v[128:143]\n"
      "v_max_f32 v96, 0, v96\n"
      "v_max_f32 v97, 0, v97\n"
      "v_max_f32 v98, 0, v98\n"
      "v_max_f32 v99, 0, v99\n"
      "v_max_f32 v100, 0, v100\n"
      "buffer_load_dwordx4 a[16:19], v3, s[16:19], s73 offen sc1\n"
      "v_mfma_f32_32x32x16_bf16 v[128:143], v[180:183], v[184:187], v[128:143]\n"
      "v_max_f32 v101, 0, v101\n"
      "v_max_f32 v102, 0, v102\n"
      "v_max_f32 v103, 0, v103\n"
      "v_mul_f32 v96, v96, v96\n"
      "v_mul_f32 v97, v97, v97\n"
      "s_add_u32 s62, s60, 1\n"
      "s_sub_u32 s73, s62, 14\n"
      "s_min_u32 s62, s62, s73\n"
      "v_mfma_f32_32x32x16_bf16 v[128:143], v[196:199], v[200:203], v[128:143]\n"
      "v_mul_f32 v98, v98, v98\n"
      "v_mul_f32 v99, v99, v99\n"
      "v_mul_f32 v100, v100, v100\n"
      "v_mul_f32 v101, v101, v101\n"
      "v_mul_f32 v102, v102, v102\n"
      "s_mul_i32 s73, s62, 40960\n"
      "s_add_u32 s73, s73, s64\n"
      "s_cmp_eq_u32 s62, s61\n"
      "s_cselect_b32 s73, 2146435072, s73\n"
      "v_mfma_f32_32x32x16_bf16 v[128:143], v[68:71], v[72:75], v[128:143]\n"
      "v_mul_f32 v103, v103, v103\n"
      "v_cvt_pk_bf16_f32 v96, v96, v97\n"
      "v_cvt_pk_bf16_f32 v97, v98, v99\n"
      "v_cvt_pk_bf16_f32 v98, v100, v101\n"
      "v_cvt_pk_bf16_f32 v99, v102, v103\n"
      "buffer_load_dwordx4 a[20:23], v2, s[16:19], s73 offen sc1\n"
      "v_mfma_f32_32x32x16_bf16 v[144:159], v[164:167], v[172:175], v[144:159]\n"
      "buffer_load_dwordx4 a[24:27], v2, s[16:19], s73 offen offset:1024 sc1\n"
      "buffer_store_dwordx4 v[96:99], v35, s[16:19], s70 offen sc1\n"
      "v_max_f32 v104, 0, v104\n"
      "v_max_f32 v105, 0, v105\n"
      "v_max_f32 v106, 0, v106\n"
      "v_max_f32 v107, 0, v107\n"
      "v_max_f32 v108, 0, v108\n"
      "v_mfma_f32_32x32x16_bf16 v[144:159], v[180:183], v[188:191], v[144:159]\n"
      "buffer_load_dwordx4 a[28:31], v2, s[16:19], s73 offen offset:2048 sc1\n"
      "v_max_f32 v109, 0, v109\n"
      "v_max_f32 v110, 0, v110\n"
      "v_max_f32 v111, 0, v111\n"
      "v_mul_f32 v104, v104, v104\n"
      "v_mul_f32 v105, v105, v105\n"
      "v_mfma_f32_32x32x16_bf16 v[144:159], v[196:199], v[204:207], v[144:159]\n"
      "buffer_load_dwordx4 a[32:35], v2, s[16:19], s73 offen offset:3072 sc1\n"
      "v_mul_f32 v106, v106, v106\n"
      "v_mul_f32 v107, v107, v107\n"
      "v_mul_f32 v108, v108, v108\n"
      "v_mul_f32 v109, v109, v109\n"
      "v_mul_f32 v110, v110, v110\n"
      "v_mfma_f32_32x32x16_bf16 v[144:159], v[68:71], v[76:79], v[144:159]\n"
      "buffer_load_dwordx4 a[36:39], v3, s[16:19], s73 offen sc1\n"
      "v_mul_f32 v111, v111, v111\n"
      "v_cvt_pk_bf16_f32 v104, v104, v105\n"
      "v_cvt_pk_bf16_f32 v105, v106, v107\n"
      "v_cvt_pk_bf16_f32 v106, v108, v109\n"
      "v_cvt_pk_bf16_f32 v107, v110, v111\n"
      "buffer_store_dwordx4 v[104:107], v35, s[16:19], s70 offen offset:512 sc1\n"
      "v_max_f32 v112, 0, v112\n"
      "v_max_f32 v113, 0, v113\n"
      "v_max_f32 v114, 0, v114\n"
      "v_max_f32 v115, 0, v115\n"
      "v_max_f32 v116, 0, v116\n"
      "v_max_f32 v117, 0, v117\n"
      "v_max_f32 v118, 0, v118\n"
      "v_max_f32 v119, 0, v119\n"
      "v_pk_mul_f32 v[112:113], v[112:113], v[112:113]\n"
      "v_pk_mul_f32 v[114:115], v[114:115], v[114:115]\n"
      "v_pk_mul_f32 v[116:117], v[116:117], v[116:117]\n"
      "v_pk_mul_f32 v[118:119], v[118:119], v[118:119]\n"
      "v_cvt_pk_bf16_f32 v112, v112, v113\n"
      "v_cvt_pk_bf16_f32 v113, v114, v115\n"
      "v_cvt_pk_bf16_f32 v114, v116, v117\n"
      "v_cvt_pk_bf16_f32 v115, v118, v119\n"
      "buffer_store_dwordx4 v[112:115], v35, s[16:19], s71 offen sc1\n"
      "v_max_f32 v120, 0, v120\n"
      "v_max_f32 v121, 0, v121\n"
      "v_max_f32 v122, 0, v122\n"
      "v_max_f32 v123, 0, v123\n"
      "v_max_f32 v124, 0, v124\n"
      "v_max_f32 v125, 0, v125\n"
      "v_max_f32 v126, 0, v126\n"
      "v_max_f32 v127, 0, v127\n"
      "v_pk_mul_f32 v[120:121], v[120:121], v[120:121]\n"
      "v_pk_mul_f32 v[122:123], v[122:123], v[122:123]\n"
      "v_pk_mul_f32 v[124:125], v[124:125], v[124:125]\n"
      "v_pk_mul_f32 v[126:127], v[126:127], v[126:127]\n"
      "v_cvt_pk_bf16_f32 v120, v120, v121\n"
      "v_cvt_pk_bf16_f32 v121, v122, v123\n"
      "v_cvt_pk_bf16_f32 v122, v124, v125\n"
      "v_cvt_pk_bf16_f32 v123, v126, v127\n"
      "buffer_store_dwordx4 v[120:123], v35, s[16:19], s71 offen offset:512 sc1\n"
      "v_max_f32 v128, 0, v128\n"
      "v_max_f32 v129, 0, v129\n"
      "v_max_f32 v130, 0, v130\n"
      "v_max_f32 v131, 0, v131\n"
      "v_max_f32 v132, 0, v132\n"
      "v_max_f32 v133, 0, v133\n"
      "v_max_f32 v134, 0, v134\n"
      "v_max_f32 v135, 0, v135\n"
      "v_pk_mul_f32 v[128:129], v[128:129], v[128:129]\n"
      "v_pk_mul_f32 v[130:131], v[130:131], v[130:131]\n"
      "v_pk_mul_f32 v[132:133], v[132:133], v[132:133]\n"
      "v_pk_mul_f32 v[134:135], v[134:135], v[134:135]\n"
      "v_cvt_pk_bf16_f32 v128, v128, v129\n"
      "v_cvt_pk_bf16_f32 v129, v130, v131\n"
      "v_cvt_pk_bf16_f32 v130, v132, v133\n"
      "v_cvt_pk_bf16_f32 v131, v134, v135\n"
      "buffer_store_dwordx4 v[128:131], v35, s[16:19], s70 offen offset:1024 sc1\n"
      "v_max_f32 v136, 0, v136\n"
      "v_max_f32 v137, 0, v137\n"
      "v_max_f32 v138, 0, v138\n"
      "v_max_f32 v139, 0, v139\n"
      "v_max_f32 v140, 0, v140\n"
      "v_max_f32 v141, 0, v141\n"
      "v_max_f32 v142, 0, v142\n"
      "v_max_f32 v143, 0, v143\n"
      "v_pk_mul_f32 v[136:137], v[136:137], v[136:137]\n"
      "v_pk_mul_f32 v[138:139], v[138:139], v[138:139]\n"
      "v_pk_mul_f32 v[140:141], v[140:141], v[140:141]\n"
      "v_pk_mul_f32 v[142:143], v[142:143], v[142:143]\n"
      "v_cvt_pk_bf16_f32 v136, v136, v137\n"
      "v_cvt_pk_bf16_f32 v137, v138, v139\n"
      "v_cvt_pk_bf16_f32 v138, v140, v141\n"
      "v_cvt_pk_bf16_f32 v139, v142, v143\n"
      "buffer_store_dwordx4 v[136:139], v35, s[16:19], s70 offen offset:1536 sc1\n"
      "v_max_f32 v144, 0, v144\n"
      "v_max_f32 v145, 0, v145\n"
      "v_max_f32 v146, 0, v146\n"
      "v_max_f32 v147, 0, v147\n"
      "v_max_f32 v148, 0, v148\n"
      "v_max_f32 v149, 0, v149\n"
      "v_max_f32 v150, 0, v150\n"
      "v_max_f32 v151, 0, v151\n"
      "v_pk_mul_f32 v[144:145], v[144:145], v[144:145]\n"
      "v_pk_mul_f32 v[146:147], v[146:147], v[146:147]\n"
      "v_pk_mul_f32 v[148:149], v[148:149], v[148:149]\n"
      "v_pk_mul_f32 v[150:151], v[150:151], v[150:151]\n"
      "v_cvt_pk_bf16_f32 v144, v144, v145\n"
      "v_cvt_pk_bf16_f32 v145, v146, v147\n"
      "v_cvt_pk_bf16_f32 v146, v148, v149\n"
      "v_cvt_pk_bf16_f32 v147, v150, v151\n"
      "buffer_store_dwordx4 v[144:147], v35, s[16:19], s71 offen offset:1024 sc1\n"
      "v_max_f32 v152, 0, v152\n"
      "v_max_f32 v153, 0, v153\n"
      "v_max_f32 v154, 0, v154\n"
      "v_max_f32 v155, 0, v155\n"
      "v_max_f32 v156, 0, v156\n"
      "v_max_f32 v157, 0, v157\n"
      "v_max_f32 v158, 0, v158\n"
      "v_max_f32 v159, 0, v159\n"
      "v_pk_mul_f32 v[152:153], v[152:153], v[152:153]\n"
      "v_pk_mul_f32 v[154:155], v[154:155], v[154:155]\n"
      "v_pk_mul_f32 v[156:157], v[156:157], v[156:157]\n"
      "v_pk_mul_f32 v[158:159], v[158:159], v[158:159]\n"
      "v_cvt_pk_bf16_f32 v152, v152, v153\n"
      "v_cvt_pk_bf16_f32 v153, v154, v155\n"
      "v_cvt_pk_bf16_f32 v154, v156, v157\n"
      "v_cvt_pk_bf16_f32 v155, v158, v159\n"
      "buffer_store_dwordx4 v[152:155], v35, s[16:19], s71 offen offset:1536 sc1\n"
      "v_add_u32 v6, s84, v2\n"
      "v_add_u32 v7, s85, v2\n"
      "v_add_u32 v8, s86, v2\n"
      "v_add_u32 v9, s87, v2\n"
      "s_waitcnt vmcnt(19)\n"
      "s_barrier\n"
      "v_add_u32 v38, s66, v26\n"
      "v_add_u32 v39, s65, v27\n"
      "v_add_u32 v40, 8, v38\n"
      "ds_read_b64 v[144:145], v38 offset:0\n"
      "ds_read_b64 v[146:147], v40 offset:0\n"
      "ds_read_b128 v[176:179], v39 offset:3584\n"
      "ds_read_b64 v[148:149], v38 offset:512\n"
      "ds_read_b64 v[150:151], v40 offset:512\n"
      "ds_read_b128 v[180:183], v39 offset:3072\n"
      "ds_read_b64 v[152:153], v38 offset:1024\n"
      "ds_read_b64 v[154:155], v40 offset:1024\n"
      "ds_read_b128 v[184:187], v39 offset:2560\n"
      "s_waitcnt lgkmcnt(6)\n"
      "v_mfma_f32_16x16x32_bf16 v[48:51], v[144:147], v[176:179], 0\n"
      "ds_read_b64 v[156:157], v38 offset:1536\n"
      "ds_read_b64 v[158:159], v40 offset:1536\n"
      "s_add_u32 s62, s60, 2\n"
      "s_sub_u32 s73, s62, 14\n"
      "s_min_u32 s62, s62, s73\n"
      "s_waitcnt lgkmcnt(5)\n"
      "v_mfma_f32_16x16x32_bf16 v[52:55], v[144:147], v[180:183], 0\n"
      "ds_read_b128 v[188:191], v39 offset:2048\n"
      "ds_read_b64 v[160:161], v38 offset:2048\n"
      "v_mfma_f32_16x16x32_bf16 v[52:55], v[148:151], v[176:179], v[52:55]\n"
      "ds_read_b64 v[162:163], v40 offset:2048\n"
      "ds_read_b128 v[192:195], v39 offset:1536\n"
      "v_mfma_f32_16x16x32_bf16 v[56:59], v[148:151], v[180:183], 0\n"
      "ds_read_b64 v[164:165], v38 offset:2560\n"
      "ds_read_b64 v[166:167], v40 offset:2560\n"
      "s_waitcnt lgkmcnt(8)\n"
      "v_mfma_f32_16x16x32_bf16 v[56:59], v[144:147], v[184:187], v[56:59]\n"
      "ds_read_b128 v[196:199], v39 offset:1024\n"
      "ds_read_b64 v[168:169], v38 offset:3072\n"
      "v_mfma_f32_16x16x32_bf16 v[60:63], v[148:151], v[184:187], 0\n"
      "ds_read_b64 v[170:171], v40 offset:3072\n"
      "ds_read_b128 v[200:203], v39 offset:512\n"
      "s_mul_i32 s73, s62, 40960\n"
      "s_add_u32 s73, s73, s64\n"
      "s_cmp_eq_u32 s62, s61\n"
      "s_cselect_b32 s73, 2146435072, s73\n"
      "v_mfma_f32_16x16x32_bf16 v[56:59], v[152:155], v[176:179], v[56:59]\n"
      "ds_read_b64 v[172:173], v38 offset:3584\n"
      "ds_read_b64 v[174:175], v40 offset:3584\n"
      "v_mfma_f32_16x16x32_bf16 v[60:63], v[152:155], v[180:183], v[60:63]\n"
      "ds_read_b128 v[204:207], v39 offset:0\n"
      "v_mfma_f32_16x16x32_bf16 v[64:67], v[152:155], v[184:187], 0\n"
      "ds_read_b64 v[80:81], v38 offset:64\n"
      "s_waitcnt lgkmcnt(13)\n"
      "v_mfma_f32_16x16x32_bf16 v[60:63], v[144:147], v[188:191], v[60:63]\n"
      "ds_read_b64 v[82:83], v40 offset:64\n"
      "v_mfma_f32_16x16x32_bf16 v[64:67], v[148:151], v[188:191], v[64:67]\n"
      "ds_read_b128 v[112:115], v39 offset:3648\n"
      "buffer_load_dwordx4 a[40:43], v2, s[16:19], s73 offen sc1\n"
      "v_mfma_f32_16x16x32_bf16 v[68:71], v[152:155], v[188:191], 0\n"
      "ds_read_b64 v[84:85], v38 offset:576\n"
      "v_mfma_f32_16x16x32_bf16 v[60:63], v[156:159], v[176:179], v[60:63]\n"
      "ds_read_b64 v[86:87], v40 offset:576\n"
      "v_mfma_f32_16x16x32_bf16 v[64:67], v[156:159], v[180:183], v[64:67]\n"
      "ds_read_b128 v[116:119], v39 offset:3136\n"
      "v_mfma_f32_16x16x32_bf16 v[68:71], v[156:159], v[184:187], v[68:71]\n"
      "ds_read_b64 v[88:89], v38 offset:1088\n"
      "v_mfma_f32_16x16x32_bf16 v[72:75], v[156:159], v[188:191], 0\n"
      "ds_read_b64 v[90:91], v40 offset:1088\n"
      "buffer_load_dwordx4 a[44:47], v2, s[16:19], s73 offen offset:1024 sc1\n"
      "s_waitcnt lgkmcnt(15)\n"
      "v_mfma_f32_16x16x32_bf16 v[64:67], v[144:147], v[192:195], v[64:67]\n"
      "ds_read_b128 v[120:123], v39 offset:2624\n"
      "v_mfma_f32_16x16x32_bf16 v[68:71], v[148:151], v[192:195], v[68:71]\n"
      "ds_read_b64 v[92:93], v38 offset:1600\n"
      "v_mfma_f32_16x16x32_bf16 v[72:75], v[152:155], v[192:195], v[72:75]\n"
      "ds_read_b64 v[94:95], v40 offset:1600\n"
      "v_mfma_f32_16x16x32_bf16 v[76:79], v[156:159], v[192:195], 0\n"
      "ds_read_b128 v[124:127], v39 offset:2112\n"
      "v_mfma_f32_16x16x32_bf16 v[64:67], v[160:163], v[176:179], v[64:67]\n"
      "ds_read_b64 v[96:97], v38 offset:2112\n"
      "buffer_load_dwordx4 a[48:51], v2, s[16:19], s73 offen offset:2048 sc1\n"
      "v_mfma_f32_16x16x32_bf16 v[68:71], v[160:163], v[180:183], v[68:71]\n"
      "ds_read_b64 v[98:99], v40 offset:2112\n"
      "v_mfma_f32_16x16x32_bf16 v[72:75], v[160:163], v[184:187], v[72:75]\n"
      "ds_read_b128 v[128:131], v39 offset:1600\n"
      "v_mfma_f32_16x16x32_bf16 v[76:79], v[160:163], v[188:191], v[76:79]\n"
      "ds_read_b64 v[100:101], v38 offset:2624\n"
      "s_waitcnt lgkmcnt(15)\n"
      "v_mfma_f32_16x16x32_bf16 v[68:71], v[144:147], v[196:199], v[68:71]\n"
      "ds_read_b64 v[102:103], v40 offset:2624\n"
      "v_mfma_f32_16x16x32_bf16 v[72:75], v[148:151], v[196:199], v[72:75]\n"
      "ds_read_b128 v[132:135], v39 offset:1088\n"
      "buffer_load_dwordx4 a[52:55], v2, s[16:19], s73 offen offset:3072 sc1\n"
      "v_mfma_f32_16x16x32_bf16 v[76:79], v[152:155], v[196:199], v[76:79]\n"
      "ds_read_b64 v[104:105], v38 offset:3136\n"
      "v_mfma_f32_16x16x32_bf16 v[68:71], v[164:167], v[176:179], v[68:71]\n"
      "ds_read_b64 v[106:107], v40 offset:3136\n"
      "v_mfma_f32_16x16x32_bf16 v[72:75], v[164:167], v[180:183], v[72:75]\n"
      "ds_read_b128 v[136:139], v39 offset:576\n"
      "v_mfma_f32_16x16x32_bf16 v[76:79], v[164:167], v[184:187], v[76:79]\n"
      "ds_read_b64 v[108:109], v38 offset:3648\n"
      "v_mfma_f32_16x16x32_bf16 v[72:75], v[144:147], v[200:203], v[72:75]\n"
      "ds_read_b64 v[110:111], v40 offset:3648\n"
      "buffer_load_dwordx4 a[56:59], v3, s[16:19], s73 offen sc1\n"
      "v_mfma_f32_16x16x32_bf16 v[76:79], v[148:151], v[200:203], v[76:79]\n"
      "ds_read_b128 v[140:143], v39 offset:64\n"
      "v_mfma_f32_16x16x32_bf16 v[72:75], v[168:171], v[176:179], v[72:75]\n"
      "v_mfma_f32_16x16x32_bf16 v[76:79], v[168:171], v[180:183], v[76:79]\n"
      "v_mfma_f32_16x16x32_bf16 v[76:79], v[144:147], v[204:207], v[76:79]\n"
      "v_mfma_f32_16x16x32_bf16 v[76:79], v[172:175], v[176:179], v[76:79]\n"
      "s_waitcnt lgkmcnt(0)\n"
      "v_mfma_f32_16x16x32_bf16 v[48:51], v[80:83], v[112:115], v[48:51]\n"
      "ds_read_b64 v[144:145], v38 offset:128\n"
      "s_add_u32 s62, s60, 3\n"
      "s_sub_u32 s73, s62, 14\n"
      "s_min_u32 s62, s62, s73\n"
      "v_mfma_f32_16x16x32_bf16 v[52:55], v[80:83], v[116:119], v[52:55]\n"
      "ds_read_b64 v[146:147], v40 offset:128\n"
      "v_mfma_f32_16x16x32_bf16 v[56:59], v[80:83], v[120:123], v[56:59]\n"
      "ds_read_b128 v[176:179], v39 offset:3712\n"
      "v_mfma_f32_16x16x32_bf16 v[60:63], v[80:83], v[124:127], v[60:63]\n"
      "ds_read_b64 v[148:149], v38 offset:640\n"
      "v_mfma_f32_16x16x32_bf16 v[64:67], v[80:83], v[128:131], v[64:67]\n"
      "ds_read_b64 v[150:151], v40 offset:640\n"
      "v_mfma_f32_16x16x32_bf16 v[68:71], v[80:83], v[132:135], v[68:71]\n"
      "ds_read_b128 v[180:183], v39 offset:3200\n"
      "s_mul_i32 s73, s62, 40960\n"
      "s_add_u32 s73, s73, s64\n"
      "s_cmp_eq_u32 s62, s61\n"
      "s_cselect_b32 s73, 2146435072, s73\n"
      "v_mfma_f32_16x16x32_bf16 v[72:75], v[80:83], v[136:139], v[72:75]\n"
      "ds_read_b64 v[152:153], v38 offset:1152\n"
      "v_mfma_f32_16x16x32_bf16 v[76:79], v[80:83], v[140:143], v[76:79]\n"
      "ds_read_b64 v[154:155], v40 offset:1152\n"
      "v_mfma_f32_16x16x32_bf16 v[52:55], v[84:87], v[112:115], v[52:55]\n"
      "ds_read_b128 v[184:187], v39 offset:2688\n"
      "v_mfma_f32_16x16x32_bf16 v[56:59], v[84:87], v[116:119], v[56:59]\n"
      "ds_read_b64 v[156:157], v38 offset:1664\n"
      "v_mfma_f32_16x16x32_bf16 v[60:63], v[84:87], v[120:123], v[60:63]\n"
      "ds_read_b64 v[158:159], v40 offset:1664\n"
      "buffer_load_dwordx4 a[60:63], v2, s[16:19], s73 offen sc1\n"
      "v_mfma_f32_16x16x32_bf16 v[64:67], v[84:87], v[124:127], v[64:67]\n"
      "ds_read_b128 v[188:191], v39 offset:2176\n"
      "v_mfma_f32_16x16x32_bf16 v[68:71], v[84:87], v[128:131], v[68:71]\n"
      "ds_read_b64 v[160:161], v38 offset:2176\n"
      "v_mfma_f32_16x16x32_bf16 v[72:75], v[84:87], v[132:135], v[72:75]\n"
      "ds_read_b64 v[162:163], v40 offset:2176\n"
      "v_mfma_f32_16x16x32_bf16 v[76:79], v[84:87], v[136:139], v[76:79]\n"
      "ds_read_b128 v[192:195], v39 offset:1664\n"
      "v_mfma_f32_16x16x32_bf16 v[56:59], v[88:91], v[112:115], v[56:59]\n"
      "ds_read_b64 v[164:165], v38 offset:2688\n"
      "buffer_load_dwordx4 a[64:67], v2, s[16:19], s73 offen offset:1024 sc1\n"
      "v_mfma_f32_16x16x32_bf16 v[60:63], v[88:91], v[116:119], v[60:63]\n"
      "ds_read_b64 v[166:167], v40 offset:2688\n"
      "v_mfma_f32_16x16x32_bf16 v[64:67], v[88:91], v[120:123], v[64:67]\n"
      "ds_read_b128 v[196:199], v39 offset:1152\n"
      "v_mfma_f32_16x16x32_bf16 v[68:71], v[88:91], v[124:127], v[68:71]\n"
      "ds_read_b64 v[168:169], v38 offset:3200\n"
      "v_mfma_f32_16x16x32_bf16 v[72:75], v[88:91], v[128:131], v[72:75]\n"
      "ds_read_b64 v[170:171], v40 offset:3200\n"
      "v_mfma_f32_16x16x32_bf16 v[76:79], v[88:91], v[132:135], v[76:79]\n"
      "ds_read_b128 v[200:203], v39 offset:640\n"
      "buffer_load_dwordx4 a[68:71], v2, s[16:19], s73 offen offset:2048 sc1\n"
      "v_mfma_f32_16x16x32_bf16 v[60:63], v[92:95], v[112:115], v[60:63]\n"
      "ds_read_b64 v[172:173], v38 offset:3712\n"
      "v_mfma_f32_16x16x32_bf16 v[64:67], v[92:95], v[116:119], v[64:67]\n"
      "ds_read_b64 v[174:175], v40 offset:3712\n"
      "v_mfma_f32_16x16x32_bf16 v[68:71], v[92:95], v[120:123], v[68:71]\n"
      "ds_read_b128 v[204:207], v39 offset:128\n"
      "v_mfma_f32_16x16x32_bf16 v[72:75], v[92:95], v[124:127], v[72:75]\n"
      "v_mfma_f32_16x16x32_bf16 v[76:79], v[92:95], v[128:131], v[76:79]\n"
      "buffer_load_dwordx4 a[72:75], v2, s[16:19], s73 offen offset:3072 sc1\n"
      "v_mfma_f32_16x16x32_bf16 v[64:67], v[96:99], v[112:115], v[64:67]\n"
      "v_mfma_f32_16x16x32_bf16 v[68:71], v[96:99], v[116:119], v[68:71]\n"
      "v_mfma_f32_16x16x32_bf16 v[72:75], v[96:99], v[120:123], v[72:75]\n"
      "v_mfma_f32_16x16x32_bf16 v[76:79], v[96:99], v[124:127], v[76:79]\n"
      "v_mfma_f32_16x16x32_bf16 v[68:71], v[100:103], v[112:115], v[68:71]\n"
      "buffer_load_dwordx4 a[76:79], v3, s[16:19], s73 offen sc1\n"
      "v_mfma_f32_16x16x32_bf16 v[72:75], v[100:103], v[116:119], v[72:75]\n"
      "v_mfma_f32_16x16x32_bf16 v[76:79], v[100:103], v[120:123], v[76:79]\n"
      "v_mfma_f32_16x16x32_bf16 v[72:75], v[104:107], v[112:115], v[72:75]\n"
      "v_mfma_f32_16x16x32_bf16 v[76:79], v[104:107], v[116:119], v[76:79]\n"
      "v_mfma_f32_16x16x32_bf16 v[76:79], v[108:111], v[112:115], v[76:79]\n"
      "s_waitcnt vmcnt(10) lgkmcnt(0)\n"
      "s_barrier\n"
      "s_cmp_lg_u32 s43, 0\n"
      "s_cbranch_scc1 .Lw0_15\n"
      "s_lshl_b32 s98, s42, 7\n"
      "s_add_u32 s98, s98, 32768\n"
      "v_mov_b32 v14, s98\n"
      "v_mov_b32 v16, s40\n"
      "v_mov_b32 v17, s41\n"
      "global_store_dwordx2 v14, v[16:17], s[16:17] sc1\n"
      "s_nop 0\n"
      ".Lw0_15:\n"
      "v_cmp_eq_u64 vcc, s[40:41], v[10:11]\n"
      "s_nop 0\n"
      "s_cmp_eq_u64 vcc, -1\n"
      "s_cbranch_scc1 .Lok_16\n"
      "s_mov_b32 s82, 0x20000\n"
      ".Lsp_17:\n"
      "s_sleep 4\n"
      "global_load_dwordx2 v[10:11], v18, s[16:17] sc1\n"
      "s_waitcnt vmcnt(0)\n"
      "v_cmp_eq_u64 vcc, s[40:41], v[10:11]\n"
      "s_nop 0\n"
      "s_cmp_eq_u64 vcc, -1\n"
      "s_cbranch_scc1 .Lok_16\n"
      "s_sub_u32 s82, s82, 1\n"
      "s_cmp_lg_u32 s82, 0\n"
      "s_cbranch_scc1 .Lsp_17\n"
      ".Lok_16:\n"
      "v_mfma_f32_16x16x32_bf16 v[48:51], v[144:147], v[176:179], v[48:51]\n"
      "ds_read_b64 v[80:81], v38 offset:192\n"
      "s_mul_i32 s98, s44, 24\n"
      "s_min_u32 s99, s45, 23\n"
      "s_add_u32 s98, s98, s99\n"
      "s_mul_i32 s98, s98, 4608\n"
      "s_add_u32 s55, s98, 16908288\n"
      "v_mfma_f32_16x16x32_bf16 v[52:55], v[144:147], v[180:183], v[52:55]\n"
      "ds_read_b64 v[82:83], v40 offset:192\n"
      "v_mfma_f32_16x16x32_bf16 v[56:59], v[144:147], v[184:187], v[56:59]\n"
      "ds_read_b128 v[112:115], v39 offset:3776\n"
      "s_lshl_b32 s98, s43, 10\n"
      "s_add_u32 m0, s98, 144384\n"
      "s_add_u32 s56, s55, s98\n"
      "buffer_load_dwordx4 v2, s[16:19], s56 offen sc1 lds\n"
      "v_mfma_f32_16x16x32_bf16 v[60:63], v[144:147], v[188:191], v[60:63]\n"
      "ds_read_b64 v[84:85], v38 offset:704\n"
      "v_mfma_f32_16x16x32_bf16 v[64:67], v[144:147], v[192:195], v[64:67]\n"
      "ds_read_b64 v[86:87], v40 offset:704\n"
      "s_mov_b32 m0, 148480\n"
      "s_nop 0\n"
      "buffer_load_dwordx4 v3, s[16:19], s55 offen sc1 lds\n"
      "v_mfma_f32_16x16x32_bf16 v[68:71], v[144:147], v[196:199], v[68:71]\n"
      "ds_read_b128 v[116:119], v39 offset:3264\n"
      "v_mfma_f32_16x16x32_bf16 v[72:75], v[144:147], v[200:203], v[72:75]\n"
      "ds_read_b64 v[88:89], v38 offset:1216\n"
      "s_add_u32 s62, s60, 4\n"
      "s_sub_u32 s73, s62, 14\n"
      "s_min_u32 s62, s62, s73\n"
      "v_mfma_f32_16x16x32_bf16 v[76:79], v[144:147], v[204:207], v[76:79]\n"
      "ds_read_b64 v[90:91], v40 offset:1216\n"
      "v_mfma_f32_16x16x32_bf16 v[52:55], v[148:151], v[176:179], v[52:55]\n"
      "ds_read_b128 v[120:123], v39 offset:2752\n"
      "s_mul_i32 s73, s62, 40960\n"
      "s_add_u32 s73, s73, s64\n"
      "s_cmp_eq_u32 s62, s61\n"
      "s_cselect_b32 s73, 2146435072, s73\n"
      "v_mfma_f32_16x16x32_bf16 v[56:59], v[148:151], v[180:183], v[56:59]\n"
      "ds_read_b64 v[92:93], v38 offset:1728\n"
      "v_mfma_f32_16x16x32_bf16 v[60:63], v[148:151], v[184:187], v[60:63]\n"
      "ds_read_b64 v[94:95], v40 offset:1728\n"
      "buffer_load_dwordx4 a[80:83], v2, s[16:19], s73 offen sc1\n"
      "v_mfma_f32_16x16x32_bf16 v[64:67], v[148:151], v[188:191], v[64:67]\n"
      "ds_read_b128 v[124:127], v39 offset:2240\n"
      "v_mfma_f32_16x16x32_bf16 v[68:71], v[148:151], v[192:195], v[68:71]\n"
      "ds_read_b64 v[96:97], v38 offset:2240\n"
      "buffer_load_dwordx4 a[84:87], v2, s[16:19], s73 offen offset:1024 sc1\n"
      "v_mfma_f32_16x16x32_bf16 v[72:75], v[148:151], v[196:199], v[72:75]\n"
      "ds_read_b64 v[98:99], v40 offset:2240\n"
      "v_mfma_f32_16x16x32_bf16 v[76:79], v[148:151], v[200:203], v[76:79]\n"
      "ds_read_b128 v[128:131], v39 offset:1728\n"
      "buffer_load_dwordx4 a[88:91], v2, s[16:19], s73 offen offset:2048 sc1\n"
      "v_mfma_f32_16x16x32_bf16 v[56:59], v[152:155], v[176:179], v[56:59]\n"
      "ds_read_b64 v[100:101], v38 offset:2752\n"
      "v_mfma_f32_16x16x32_bf16 v[60:63], v[152:155], v[180:183], v[60:63]\n"
      "ds_read_b64 v[102:103], v40 offset:2752\n"
      "buffer_load_dwordx4 a[92:95], v2, s[16:19], s73 offen offset:3072 sc1\n"
      "v_mfma_f32_16x16x32_bf16 v[64:67], v[152:155], v[184:187], v[64:67]\n"
      "ds_read_b128 v[132:135], v39 offset:1216\n"
      "v_mfma_f32_16x16x32_bf16 v[68:71], v[152:155], v[188:191], v[68:71]\n"
      "ds_read_b64 v[104:105], v38 offset:3264\n"
      "buffer_load_dwordx4 a[96:99], v3, s[16:19], s73 offen sc1\n"
      "v_mfma_f32_16x16x32_bf16 v[72:75], v[152:155], v[192:195], v[72:75]\n"
      "ds_read_b64 v[106:107], v40 offset:3264\n"
      "v_mfma_f32_16x16x32_bf16 v[76:79], v[152:155], v[196:199], v[76:79]\n"
      "ds_read_b128 v[136:139], v39 offset:704\n"
      "s_add_u32 s62, s60, 5\n"
      "s_sub_u32 s73, s62, 14\n"
      "s_min_u32 s62, s62, s73\n"
      "v_mfma_f32_16x16x32_bf16 v[60:63], v[156:159], v[176:179], v[60:63]\n"
      "ds_read_b64 v[108:109], v38 offset:3776\n"
      "v_mfma_f32_16x16x32_bf16 v[64:67], v[156:159], v[180:183], v[64:67]\n"
      "ds_read_b64 v[110:111], v40 offset:3776\n"
      "s_mul_i32 s73, s62, 40960\n"
      "s_add_u32 s73, s73, s64\n"
      "s_cmp_eq_u32 s62, s61\n"
      "s_cselect_b32 s73, 2146435072, s73\n"
      "v_mfma_f32_16x16x32_bf16 v[68:71], v[156:159], v[184:187], v[68:71]\n"
      "ds_read_b128 v[140:143], v39 offset:192\n"
      "v_mfma_f32_16x16x32_bf16 v[72:75], v[156:159], v[188:191], v[72:75]\n"
      "buffer_load_dwordx4 a[100:103], v2, s[16:19], s73 offen sc1\n"
      "v_mfma_f32_16x16x32_bf16 v[76:79], v[156:159], v[192:195], v[76:79]\n"
      "v_mfma_f32_16x16x32_bf16 v[64:67], v[160:163], v[176:179], v[64:67]\n"
      "buffer_load_dwordx4 a[104:107], v2, s[16:19], s73 offen offset:1024 sc1\n"
      "v_mfma_f32_16x16x32_bf16 v[68:71], v[160:163], v[180:183], v[68:71]\n"
      "v_mfma_f32_16x16x32_bf16 v[72:75], v[160:163], v[184:187], v[72:75]\n"
      "buffer_load_dwordx4 a[108:111], v2, s[16:19], s73 offen offset:2048 sc1\n"
      "v_mfma_f32_16x16x32_bf16 v[76:79], v[160:163], v[188:191], v[76:79]\n"
      "v_mfma_f32_16x16x32_bf16 v[68:71], v[164:167], v[176:179], v[68:71]\n"
      "buffer_load_dwordx4 a[112:115], v2, s[16:19], s73 offen offset:3072 sc1\n"
      "v_mfma_f32_16x16x32_bf16 v[72:75], v[164:167], v[180:183], v[72:75]\n"
      "v_mfma_f32_16x16x32_bf16 v[76:79], v[164:167], v[184:187], v[76:79]\n"
      "buffer_load_dwordx4 a[116:119], v3, s[16:19], s73 offen sc1\n"
      "v_mfma_f32_16x16x32_bf16 v[72:75], v[168:171], v[176:179], v[72:75]\n"
      "v_mfma_f32_16x16x32_bf16 v[76:79], v[168:171], v[180:183], v[76:79]\n"
      "s_add_u32 s62, s60, 6\n"
      "s_sub_u32 s73, s62, 14\n"
      "s_min_u32 s62, s62, s73\n"
      "v_mfma_f32_16x16x32_bf16 v[76:79], v[172:175], v[176:179], v[76:79]\n"
      "s_waitcnt lgkmcnt(0)\n"
      "v_mfma_f32_16x16x32_bf16 v[48:51], v[80:83], v[112:115], v[48:51]\n"
      "s_mul_i32 s73, s62, 40960\n"
      "s_add_u32 s73, s73, s64\n"
      "s_cmp_eq_u32 s62, s61\n"
      "s_cselect_b32 s73, 2146435072, s73\n"
      "v_mfma_f32_16x16x32_bf16 v[52:55], v[80:83], v[116:119], v[52:55]\n"
      "v_mfma_f32_16x16x32_bf16 v[56:59], v[80:83], v[120:123], v[56:59]\n"
      "buffer_load_dwordx4 a[120:123], v2, s[16:19], s73 offen sc1\n"
      "v_mfma_f32_16x16x32_bf16 v[60:63], v[80:83], v[124:127], v[60:63]\n"
      "v_mfma_f32_16x16x32_bf16 v[64:67], v[80:83], v[128:131], v[64:67]\n"
      "buffer_load_dwordx4 a[124:127], v2, s[16:19], s73 offen offset:1024 sc1\n"
      "v_mfma_f32_16x16x32_bf16 v[68:71], v[80:83], v[132:135], v[68:71]\n"
      "v_mfma_f32_16x16x32_bf16 v[72:75], v[80:83], v[136:139], v[72:75]\n"
      "v_mfma_f32_16x16x32_bf16 v[76:79], v[80:83], v[140:143], v[76:79]\n"
      "buffer_load_dwordx4 a[128:131], v2, s[16:19], s73 offen offset:2048 sc1\n"
      "v_mfma_f32_16x16x32_bf16 v[52:55], v[84:87], v[112:115], v[52:55]\n"
      "v_mfma_f32_16x16x32_bf16 v[56:59], v[84:87], v[116:119], v[56:59]\n"
      "buffer_load_dwordx4 a[132:135], v2, s[16:19], s73 offen offset:3072 sc1\n"
      "v_mfma_f32_16x16x32_bf16 v[60:63], v[84:87], v[120:123], v[60:63]\n"
      "v_mfma_f32_16x16x32_bf16 v[64:67], v[84:87], v[124:127], v[64:67]\n"
      "v_mfma_f32_16x16x32_bf16 v[68:71], v[84:87], v[128:131], v[68:71]\n"
      "buffer_load_dwordx4 a[136:139], v3, s[16:19], s73 offen sc1\n"
      "v_mfma_f32_16x16x32_bf16 v[72:75], v[84:87], v[132:135], v[72:75]\n"
      "v_mfma_f32_16x16x32_bf16 v[76:79], v[84:87], v[136:139], v[76:79]\n"
      "s_add_u32 s62, s60, 7\n"
      "s_sub_u32 s73, s62, 14\n"
      "s_min_u32 s62, s62, s73\n"
      "v_mfma_f32_16x16x32_bf16 v[56:59], v[88:91], v[112:115], v[56:59]\n"
      "v_mfma_f32_16x16x32_bf16 v[60:63], v[88:91], v[116:119], v[60:63]\n"
      "s_mul_i32 s73, s62, 40960\n"
      "s_add_u32 s73, s73, s64\n"
      "s_cmp_eq_u32 s62, s61\n"
      "s_cselect_b32 s73, 2146435072, s73\n"
      "v_mfma_f32_16x16x32_bf16 v[64:67], v[88:91], v[120:123], v[64:67]\n"
      "v_mfma_f32_16x16x32_bf16 v[68:71], v[88:91], v[124:127], v[68:71]\n"
      "v_mfma_f32_16x16x32_bf16 v[72:75], v[88:91], v[128:131], v[72:75]\n"
      "buffer_load_dwordx4 a[140:143], v2, s[16:19], s73 offen sc1\n"
      "v_mfma_f32_16x16x32_bf16 v[76:79], v[88:91], v[132:135], v[76:79]\n"
      "v_mfma_f32_16x16x32_bf16 v[60:63], v[92:95], v[112:115], v[60:63]\n"
      "buffer_load_dwordx4 a[144:147], v2, s[16:19], s73 offen offset:1024 sc1\n"
      "v_mfma_f32_16x16x32_bf16 v[64:67], v[92:95], v[116:119], v[64:67]\n"
      "v_mfma_f32_16x16x32_bf16 v[68:71], v[92:95], v[120:123], v[68:71]\n"
      "v_mfma_f32_16x16x32_bf16 v[72:75], v[92:95], v[124:127], v[72:75]\n"
      "buffer_load_dwordx4 a[148:151], v2, s[16:19], s73 offen offset:2048 sc1\n"
      "v_mfma_f32_16x16x32_bf16 v[76:79], v[92:95], v[128:131], v[76:79]\n"
      "v_mfma_f32_16x16x32_bf16 v[64:67], v[96:99], v[112:115], v[64:67]\n"
      "buffer_load_dwordx4 a[152:155], v2, s[16:19], s73 offen offset:3072 sc1\n"
      "v_mfma_f32_16x16x32_bf16 v[68:71], v[96:99], v[116:119], v[68:71]\n"
      "v_mfma_f32_16x16x32_bf16 v[72:75], v[96:99], v[120:123], v[72:75]\n"
      "buffer_load_dwordx4 a[156:159], v3, s[16:19], s73 offen sc1\n"
      "v_mfma_f32_16x16x32_bf16 v[76:79], v[96:99], v[124:127], v[76:79]\n"
      "v_mfma_f32_16x16x32_bf16 v[68:71], v[100:103], v[112:115], v[68:71]\n"
      "v_mfma_f32_16x16x32_bf16 v[72:75], v[100:103], v[116:119], v[72:75]\n"
      "s_add_u32 s62, s60, 8\n"
      "s_sub_u32 s73, s62, 14\n"
      "s_min_u32 s62, s62, s73\n"
      "v_mfma_f32_16x16x32_bf16 v[76:79], v[100:103], v[120:123], v[76:79]\n"
      "v_mfma_f32_16x16x32_bf16 v[72:75], v[104:107], v[112:115], v[72:75]\n"
      "s_mul_i32 s73, s62, 40960\n"
      "s_add_u32 s73, s73, s64\n"
      "s_cmp_eq_u32 s62, s61\n"
      "s_cselect_b32 s73, 2146435072, s73\n"
      "v_mfma_f32_16x16x32_bf16 v[76:79], v[104:107], v[116:119], v[76:79]\n"
      "v_mfma_f32_16x16x32_bf16 v[76:79], v[108:111], v[112:115], v[76:79]\n"
      "s_lshl_b32 s68, s63, 12\n"
      "s_add_u32 s68, s68, 6291456\n"
      "v_xor_b32 v39, 4096, v29\n"
      "s_cmp_lg_u32 s50, 0\n"
      "s_cbranch_scc1 .Lcx_18\n"
      "ds_write_b128 v29, v[64:67] offset:0\n"
      "ds_write_b128 v29, v[68:71] offset:1024\n"
      "ds_write_b128 v29, v[72:75] offset:2048\n"
      "ds_write_b128 v29, v[76:79] offset:3072\n"
      "buffer_load_dwordx4 a[160:163], v2, s[16:19], s73 offen sc1\n"
      "buffer_load_dwordx4 a[164:167], v2, s[16:19], s73 offen offset:1024 sc1\n"
      "buffer_load_dwordx4 a[168:171], v2, s[16:19], s73 offen offset:2048 sc1\n"
      "buffer_load_dwordx4 a[172:175], v2, s[16:19], s73 offen offset:3072 sc1\n"
      "buffer_load_dwordx4 a[176:179], v3, s[16:19], s73 offen sc1\n"
      "s_waitcnt vmcnt(25) lgkmcnt(0)\n"
      "s_barrier\n"
      "ds_read_b128 v[80:83], v39 offset:0\n"
      "ds_read_b128 v[84:87], v39 offset:1024\n"
      "ds_read_b128 v[88:91], v39 offset:2048\n"
      "ds_read_b128 v[92:95], v39 offset:3072\n"
      "s_add_u32 s62, s60, 9\n"
      "s_sub_u32 s73, s62, 14\n"
      "s_min_u32 s62, s62, s73\n"
      "s_mul_i32 s73, s62, 40960\n"
      "s_add_u32 s73, s73, s64\n"
      "s_cmp_eq_u32 s62, s61\n"
      "s_cselect_b32 s73, 2146435072, s73\n"
      "buffer_load_dwordx4 a[180:183], v2, s[16:19], s73 offen sc1\n"
      "buffer_load_dwordx4 a[184:187], v2, s[16:19], s73 offen offset:1024 sc1\n"
      "buffer_load_dwordx4 a[188:191], v2, s[16:19], s73 offen offset:2048 sc1\n"
      "s_waitcnt lgkmcnt(3)\n"
      "v_pk_add_f32 v[48:49], v[48:49], v[80:81]\n"
      "v_pk_add_f32 v[50:51], v[50:51], v[82:83]\n"
      "v_cvt_pk_bf16_f32 v208, v48, v49\n"
      "v_cvt_pk_bf16_f32 v209, v50, v51\n"
      "s_waitcnt lgkmcnt(2)\n"
      "v_pk_add_f32 v[52:53], v[52:53], v[84:85]\n"
      "v_pk_add_f32 v[54:55], v[54:55], v[86:87]\n"
      "v_cvt_pk_bf16_f32 v210, v52, v53\n"
      "v_cvt_pk_bf16_f32 v211, v54, v55\n"
      "s_waitcnt lgkmcnt(1)\n"
      "v_pk_add_f32 v[56:57], v[56:57], v[88:89]\n"
      "v_pk_add_f32 v[58:59], v[58:59], v[90:91]\n"
      "v_cvt_pk_bf16_f32 v212, v56, v57\n"
      "v_cvt_pk_bf16_f32 v213, v58, v59\n"
      "s_waitcnt lgkmcnt(0)\n"
      "v_pk_add_f32 v[60:61], v[60:61], v[92:93]\n"
      "v_pk_add_f32 v[62:63], v[62:63], v[94:95]\n"
      "v_cvt_pk_bf16_f32 v214, v60, v61\n"
      "v_cvt_pk_bf16_f32 v215, v62, v63\n"
      "v_permlane16_swap_b32 v208, v210\n"
      "v_permlane16_swap_b32 v209, v211\n"
      "v_permlane16_swap_b32 v212, v214\n"
      "v_permlane16_swap_b32 v213, v215\n"
      "s_nop 1\n"
      "s_branch .Lcx_19\n"
      ".Lcx_18:\n"
      "ds_write_b128 v29, v[48:51] offset:0\n"
      "ds_write_b128 v29, v[52:55] offset:1024\n"
      "ds_write_b128 v29, v[56:59] offset:2048\n"
      "ds_write_b128 v29, v[60:63] offset:3072\n"
      "buffer_load_dwordx4 a[160:163], v2, s[16:19], s73 offen sc1\n"
      "buffer_load_dwordx4 a[164:167], v2, s[16:19], s73 offen offset:1024 sc1\n"
      "buffer_load_dwordx4 a[168:171], v2, s[16:19], s73 offen offset:2048 sc1\n"
      "buffer_load_dwordx4 a[172:175], v2, s[16:19], s73 offen offset:3072 sc1\n"
      "buffer_load_dwordx4 a[176:179], v3, s[16:19], s73 offen sc1\n"
      "s_waitcnt vmcnt(25) lgkmcnt(0)\n"
      "s_barrier\n"
      "ds_read_b128 v[80:83], v39 offset:0\n"
      "ds_read_b128 v[84:87], v39 offset:1024\n"
      "ds_read_b128 v[88:91], v39 offset:2048\n"
      "ds_read_b128 v[92:95], v39 offset:3072\n"
      "s_add_u32 s62, s60, 9\n"
      "s_sub_u32 s73, s62, 14\n"
      "s_min_u32 s62, s62, s73\n"
      "s_mul_i32 s73, s62, 40960\n"
      "s_add_u32 s73, s73, s64\n"
      "s_cmp_eq_u32 s62, s61\n"
      "s_cselect_b32 s73, 2146435072, s73\n"
      "buffer_load_dwordx4 a[180:183], v2, s[16:19], s73 offen sc1\n"
      "buffer_load_dwordx4 a[184:187], v2, s[16:19], s73 offen offset:1024 sc1\n"
      "buffer_load_dwordx4 a[188:191], v2, s[16:19], s73 offen offset:2048 sc1\n"
      "s_waitcnt lgkmcnt(3)\n"
      "v_pk_add_f32 v[64:65], v[64:65], v[80:81]\n"
      "v_pk_add_f32 v[66:67], v[66:67], v[82:83]\n"
      "v_cvt_pk_bf16_f32 v208, v64, v65\n"
      "v_cvt_pk_bf16_f32 v209, v66, v67\n"
      "s_waitcnt lgkmcnt(2)\n"
      "v_pk_add_f32 v[68:69], v[68:69], v[84:85]\n"
      "v_pk_add_f32 v[70:71], v[70:71], v[86:87]\n"
      "v_cvt_pk_bf16_f32 v210, v68, v69\n"
      "v_cvt_pk_bf16_f32 v211, v70, v71\n"
      "s_waitcnt lgkmcnt(1)\n"
      "v_pk_add_f32 v[72:73], v[72:73], v[88:89]\n"
      "v_pk_add_f32 v[74:75], v[74:75], v[90:91]\n"
      "v_cvt_pk_bf16_f32 v212, v72, v73\n"
      "v_cvt_pk_bf16_f32 v213, v74, v75\n"
      "s_waitcnt lgkmcnt(0)\n"
      "v_pk_add_f32 v[76:77], v[76:77], v[92:93]\n"
      "v_pk_add_f32 v[78:79], v[78:79], v[94:95]\n"
      "v_cvt_pk_bf16_f32 v214, v76, v77\n"
      "v_cvt_pk_bf16_f32 v215, v78, v79\n"
      "v_permlane16_swap_b32 v208, v210\n"
      "v_permlane16_swap_b32 v209, v211\n"
      "v_permlane16_swap_b32 v212, v214\n"
      "v_permlane16_swap_b32 v213, v215\n"
      "s_nop 1\n"
      ".Lcx_19:\n"
      "s_mul_i32 s68, s51, 20736\n"
      "s_lshl_b32 s69, s50, 8\n"
      "s_lshl_b32 s70, s43, 7\n"
      "s_sub_u32 s70, s70, s69\n"
      "s_sub_u32 s68, s70, s68\n"
      "s_add_u32 s68, s68, 107008\n"
      "s_add_u32 s70, s70, 144384\n"
      "v_add_u32 v38, s68, v26\n"
      "v_add_u32 v39, s70, v27\n"
      "v_add_u32 v40, 8, v38\n"
      "ds_read_b64 v[144:145], v38 offset:0\n"
      "ds_read_b64 v[146:147], v40 offset:0\n"
      "ds_read_b128 v[176:179], v39 offset:3584\n"
      "ds_read_b64 v[148:149], v38 offset:512\n"
      "ds_read_b64 v[150:151], v40 offset:512\n"
      "ds_read_b128 v[180:183], v39 offset:3072\n"
      "ds_read_b64 v[152:153], v38 offset:1024\n"
      "ds_read_b64 v[154:155], v40 offset:1024\n"
      "ds_read_b128 v[184:187], v39 offset:2560\n"
      "s_waitcnt lgkmcnt(6)\n"
      "v_mfma_f32_16x16x32_bf16 v[48:51], v[144:147], v[176:179], 0\n"
      "ds_read_b64 v[156:157], v38 offset:1536\n"
      "ds_read_b64 v[158:159], v40 offset:1536\n"
      "buffer_load_dwordx4 a[192:195], v2, s[16:19], s73 offen offset:3072 sc1\n"
      "s_waitcnt lgkmcnt(5)\n"
      "v_mfma_f32_16x16x32_bf16 v[52:55], v[144:147], v[180:183], 0\n"
      "ds_read_b128 v[188:191], v39 offset:2048\n"
      "ds_read_b64 v[160:161], v38 offset:2048\n"
      "v_mfma_f32_16x16x32_bf16 v[52:55], v[148:151], v[176:179], v[52:55]\n"
      "ds_read_b64 v[162:163], v40 offset:2048\n"
      "ds_read_b128 v[192:195], v39 offset:1536\n"
      "buffer_load_dwordx4 a[196:199], v3, s[16:19], s73 offen sc1\n"
      "v_mfma_f32_16x16x32_bf16 v[56:59], v[148:151], v[180:183], 0\n"
      "ds_read_b64 v[164:165], v38 offset:2560\n"
      "ds_read_b64 v[166:167], v40 offset:2560\n"
      "s_waitcnt lgkmcnt(8)\n"
      "v_mfma_f32_16x16x32_bf16 v[56:59], v[144:147], v[184:187], v[56:59]\n"
      "ds_read_b128 v[196:199], v39 offset:1024\n"
      "ds_read_b64 v[168:169], v38 offset:3072\n"
      "s_add_u32 s62, s60, 10\n"
      "s_sub_u32 s73, s62, 14\n"
      "s_min_u32 s62, s62, s73\n"
      "v_mfma_f32_16x16x32_bf16 v[60:63], v[148:151], v[184:187], 0\n"
      "ds_read_b64 v[170:171], v40 offset:3072\n"
      "ds_read_b128 v[200:203], v39 offset:512\n"
      "v_mfma_f32_16x16x32_bf16 v[56:59], v[152:155], v[176:179], v[56:59]\n"
      "ds_read_b64 v[172:173], v38 offset:3584\n"
      "ds_read_b64 v[174:175], v40 offset:3584\n"
      "s_mul_i32 s73, s62, 40960\n"
      "s_add_u32 s73, s73, s64\n"
      "s_cmp_eq_u32 s62, s61\n"
      "s_cselect_b32 s73, 2146435072, s73\n"
      "v_mfma_f32_16x16x32_bf16 v[60:63], v[152:155], v[180:183], v[60:63]\n"
      "ds_read_b128 v[204:207], v39 offset:0\n"
      "v_mfma_f32_16x16x32_bf16 v[64:67], v[152:155], v[184:187], 0\n"
      "ds_read_b64 v[80:81], v38 offset:64\n"
      "buffer_load_dwordx4 a[200:203], v2, s[16:19], s73 offen sc1\n"
      "s_waitcnt lgkmcnt(13)\n"
      "v_mfma_f32_16x16x32_bf16 v[60:63], v[144:147], v[188:191], v[60:63]\n"
      "ds_read_b64 v[82:83], v40 offset:64\n"
      "v_mfma_f32_16x16x32_bf16 v[64:67], v[148:151], v[188:191], v[64:67]\n"
      "ds_read_b128 v[112:115], v39 offset:3648\n"
      "buffer_load_dwordx4 a[204:207], v2, s[16:19], s73 offen offset:1024 sc1\n"
      "v_mfma_f32_16x16x32_bf16 v[68:71], v[152:155], v[188:191], 0\n"
      "ds_read_b64 v[84:85], v38 offset:576\n"
      "v_mfma_f32_16x16x32_bf16 v[60:63], v[156:159], v[176:179], v[60:63]\n"
      "ds_read_b64 v[86:87], v40 offset:576\n"
      "buffer_load_dwordx4 a[208:211], v2, s[16:19], s73 offen offset:2048 sc1\n"
      "v_mfma_f32_16x16x32_bf16 v[64:67], v[156:159], v[180:183], v[64:67]\n"
      "ds_read_b128 v[116:119], v39 offset:3136\n"
      "v_mfma_f32_16x16x32_bf16 v[68:71], v[156:159], v[184:187], v[68:71]\n"
      "ds_read_b64 v[88:89], v38 offset:1088\n"
      "buffer_load_dwordx4 a[212:215], v2, s[16:19], s73 offen offset:3072 sc1\n"
      "v_mfma_f32_16x16x32_bf16 v[72:75], v[156:159], v[188:191], 0\n"
      "ds_read_b64 v[90:91], v40 offset:1088\n"
      "s_waitcnt lgkmcnt(15)\n"
      "v_mfma_f32_16x16x32_bf16 v[64:67], v[144:147], v[192:195], v[64:67]\n"
      "ds_read_b128 v[120:123], v39 offset:2624\n"
      "buffer_load_dwordx4 a[216:219], v3, s[16:19], s73 offen sc1\n"
      "v_mfma_f32_16x16x32_bf16 v[68:71], v[148:151], v[192:195], v[68:71]\n"
      "ds_read_b64 v[92:93], v38 offset:1600\n"
      "v_mfma_f32_16x16x32_bf16 v[72:75], v[152:155], v[192:195], v[72:75]\n"
      "ds_read_b64 v[94:95], v40 offset:1600\n"
      "v_mfma_f32_16x16x32_bf16 v[76:79], v[156:159], v[192:195], 0\n"
      "ds_read_b128 v[124:127], v39 offset:2112\n"
      "s_add_u32 s62, s60, 11\n"
      "s_sub_u32 s73, s62, 14\n"
      "s_min_u32 s62, s62, s73\n"
      "v_mfma_f32_16x16x32_bf16 v[64:67], v[160:163], v[176:179], v[64:67]\n"
      "ds_read_b64 v[96:97], v38 offset:2112\n"
      "v_mfma_f32_16x16x32_bf16 v[68:71], v[160:163], v[180:183], v[68:71]\n"
      "ds_read_b64 v[98:99], v40 offset:2112\n"
      "s_mul_i32 s73, s62, 40960\n"
      "s_add_u32 s73, s73, s64\n"
      "s_cmp_eq_u32 s62, s61\n"
      "s_cselect_b32 s73, 2146435072, s73\n"
      "v_mfma_f32_16x16x32_bf16 v[72:75], v[160:163], v[184:187], v[72:75]\n"
      "ds_read_b128 v[128:131], v39 offset:1600\n"
      "v_mfma_f32_16x16x32_bf16 v[76:79], v[160:163], v[188:191], v[76:79]\n"
      "ds_read_b64 v[100:101], v38 offset:2624\n"
      "buffer_load_dwordx4 a[220:223], v2, s[16:19], s73 offen sc1\n"
      "s_waitcnt lgkmcnt(15)\n"
      "v_mfma_f32_16x16x32_bf16 v[68:71], v[144:147], v[196:199], v[68:71]\n"
      "ds_read_b64 v[102:103], v40 offset:2624\n"
      "v_mfma_f32_16x16x32_bf16 v[72:75], v[148:151], v[196:199], v[72:75]\n"
      "ds_read_b128 v[132:135], v39 offset:1088\n"
      "buffer_load_dwordx4 a[224:227], v2, s[16:19], s73 offen offset:1024 sc1\n"
      "v_mfma_f32_16x16x32_bf16 v[76:79], v[152:155], v[196:199], v[76:79]\n"
      "ds_read_b64 v[104:105], v38 offset:3136\n"
      "v_mfma_f32_16x16x32_bf16 v[68:71], v[164:167], v[176:179], v[68:71]\n"
      "ds_read_b64 v[106:107], v40 offset:3136\n"
      "buffer_load_dwordx4 a[228:231], v2, s[16:19], s73 offen offset:2048 sc1\n"
      "v_mfma_f32_16x16x32_bf16 v[72:75], v[164:167], v[180:183], v[72:75]\n"
      "ds_read_b128 v[136:139], v39 offset:576\n"
      "v_mfma_f32_16x16x32_bf16 v[76:79], v[164:167], v[184:187], v[76:79]\n"
      "ds_read_b64 v[108:109], v38 offset:3648\n"
      "buffer_load_dwordx4 a[232:235], v2, s[16:19], s73 offen offset:3072 sc1\n"
      "v_mfma_f32_16x16x32_bf16 v[72:75], v[144:147], v[200:203], v[72:75]\n"
      "ds_read_b64 v[110:111], v40 offset:3648\n"
      "v_mfma_f32_16x16x32_bf16 v[76:79], v[148:151], v[200:203], v[76:79]\n"
      "ds_read_b128 v[140:143], v39 offset:64\n"
      "buffer_load_dwordx4 a[236:239], v3, s[16:19], s73 offen sc1\n"
      "v_mfma_f32_16x16x32_bf16 v[72:75], v[168:171], v[176:179], v[72:75]\n"
      "v_mfma_f32_16x16x32_bf16 v[76:79], v[168:171], v[180:183], v[76:79]\n"
      "global_load_dwordx2 v[12:13], v20, s[16:17] sc1\n"
      "v_mfma_f32_16x16x32_bf16 v[76:79], v[144:147], v[204:207], v[76:79]\n"
      "s_lshl_b32 s68, s43, 13\n"
      "s_add_u32 s68, s68, 16384\n"
      "v_add_u32 v41, s68, v2\n"
      "v_mfma_f32_16x16x32_bf16 v[76:79], v[172:175], v[176:179], v[76:79]\n"
      "s_lshl_b32 s68, s43, 11\n"
      "s_add_u32 s68, s68, 16384\n"
      "v_add_u32 v42, s68, v2\n"
      "s_waitcnt lgkmcnt(0)\n"
      "v_mfma_f32_16x16x32_bf16 v[48:51], v[80:83], v[112:115], v[48:51]\n"
      "s_add_u32 s62, s60, 12\n"
      "s_sub_u32 s73, s62, 14\n"
      "s_min_u32 s62, s62, s73\n"
      "v_mfma_f32_16x16x32_bf16 v[52:55], v[80:83], v[116:119], v[52:55]\n"
      "v_mfma_f32_16x16x32_bf16 v[52:55], v[84:87], v[112:115], v[52:55]\n"
      "s_mul_i32 s73, s62, 40960\n"
      "s_add_u32 s73, s73, s64\n"
      "s_cmp_eq_u32 s62, s61\n"
      "s_cselect_b32 s73, 2146435072, s73\n"
      "v_mfma_f32_16x16x32_bf16 v[56:59], v[80:83], v[120:123], v[56:59]\n"
      "v_mfma_f32_16x16x32_bf16 v[56:59], v[84:87], v[116:119], v[56:59]\n"
      "v_mfma_f32_16x16x32_bf16 v[56:59], v[88:91], v[112:115], v[56:59]\n"
      "buffer_load_dwordx4 a[240:243], v2, s[16:19], s73 offen sc1\n"
      "v_mfma_f32_16x16x32_bf16 v[60:63], v[80:83], v[124:127], v[60:63]\n"
      "v_mfma_f32_16x16x32_bf16 v[60:63], v[84:87], v[120:123], v[60:63]\n"
      "buffer_load_dwordx4 a[244:247], v2, s[16:19], s73 offen offset:1024 sc1\n"
      "v_mfma_f32_16x16x32_bf16 v[60:63], v[88:91], v[116:119], v[60:63]\n"
      "ds_write_b128 v41, v[48:51] offset:0\n"
      "v_mfma_f32_16x16x32_bf16 v[60:63], v[92:95], v[112:115], v[60:63]\n"
      "v_mfma_f32_16x16x32_bf16 v[64:67], v[80:83], v[128:131], v[64:67]\n"
      "ds_write_b128 v41, v[52:55] offset:1024\n"
      "buffer_load_dwordx4 a[248:251], v2, s[16:19], s73 offen offset:2048 sc1\n"
      "v_mfma_f32_16x16x32_bf16 v[64:67], v[84:87], v[124:127], v[64:67]\n"
      "v_mfma_f32_16x16x32_bf16 v[64:67], v[88:91], v[120:123], v[64:67]\n"
      "buffer_load_dwordx4 a[252:255], v2, s[16:19], s73 offen offset:3072 sc1\n"
      "v_mfma_f32_16x16x32_bf16 v[64:67], v[92:95], v[116:119], v[64:67]\n"
      "ds_write_b128 v41, v[56:59] offset:2048\n"
      "v_mfma_f32_16x16x32_bf16 v[64:67], v[96:99], v[112:115], v[64:67]\n"
      "v_mfma_f32_16x16x32_bf16 v[68:71], v[80:83], v[132:135], v[68:71]\n"
      "buffer_load_dwordx4 v[232:235], v3, s[16:19], s73 offen sc1\n"
      "v_mfma_f32_16x16x32_bf16 v[68:71], v[84:87], v[128:131], v[68:71]\n"
      "v_mfma_f32_16x16x32_bf16 v[68:71], v[88:91], v[124:127], v[68:71]\n"
      "ds_write_b128 v41, v[60:63] offset:3072\n"
      "v_mfma_f32_16x16x32_bf16 v[68:71], v[92:95], v[120:123], v[68:71]\n"
      "s_add_u32 s62, s60, 13\n"
      "s_sub_u32 s73, s62, 14\n"
      "s_min_u32 s62, s62, s73\n"
      "v_mfma_f32_16x16x32_bf16 v[68:71], v[96:99], v[116:119], v[68:71]\n"
      "v_mfma_f32_16x16x32_bf16 v[68:71], v[100:103], v[112:115], v[68:71]\n"
      "s_mul_i32 s73, s62, 40960\n"
      "s_add_u32 s73, s73, s64\n"
      "s_cmp_eq_u32 s62, s61\n"
      "s_cselect_b32 s73, 2146435072, s73\n"
      "v_mfma_f32_16x16x32_bf16 v[72:75], v[80:83], v[136:139], v[72:75]\n"
      "v_mfma_f32_16x16x32_bf16 v[72:75], v[84:87], v[132:135], v[72:75]\n"
      "ds_write_b128 v41, v[64:67] offset:4096\n"
      "v_mfma_f32_16x16x32_bf16 v[72:75], v[88:91], v[128:131], v[72:75]\n"
      "buffer_load_dwordx4 v[236:239], v2, s[16:19], s73 offen sc1\n"
      "v_mfma_f32_16x16x32_bf16 v[72:75], v[92:95], v[124:127], v[72:75]\n"
      "v_mfma_f32_16x16x32_bf16 v[72:75], v[96:99], v[120:123], v[72:75]\n"
      "buffer_load_dwordx4 v[240:243], v2, s[16:19], s73 offen offset:1024 sc1\n"
      "v_mfma_f32_16x16x32_bf16 v[72:75], v[100:103], v[116:119], v[72:75]\n"
      "v_mfma_f32_16x16x32_bf16 v[72:75], v[104:107], v[112:115], v[72:75]\n"
      "v_mfma_f32_16x16x32_bf16 v[76:79], v[80:83], v[140:143], v[76:79]\n"
      "ds_write_b128 v41, v[68:71] offset:5120\n"
      "buffer_load_dwordx4 v[244:247], v2, s[16:19], s73 offen offset:2048 sc1\n"
      "v_mfma_f32_16x16x32_bf16 v[76:79], v[84:87], v[136:139], v[76:79]\n"
      "v_mfma_f32_16x16x32_bf16 v[76:79], v[88:91], v[132:135], v[76:79]\n"
      "buffer_load_dwordx4 v[248:251], v2, s[16:19], s73 offen offset:3072 sc1\n"
      "v_mfma_f32_16x16x32_bf16 v[76:79], v[92:95], v[128:131], v[76:79]\n"
      "v_mfma_f32_16x16x32_bf16 v[76:79], v[96:99], v[124:127], v[76:79]\n"
      "v_mfma_f32_16x16x32_bf16 v[76:79], v[100:103], v[120:123], v[76:79]\n"
      "buffer_load_dwordx4 v[252:255], v3, s[16:19], s73 offen sc1\n"
      "v_mfma_f32_16x16x32_bf16 v[76:79], v[104:107], v[116:119], v[76:79]\n"
      "v_mfma_f32_16x16x32_bf16 v[76:79], v[108:111], v[112:115], v[76:79]\n"
      "s_lshl_b32 s68, s59, 12\n"
      "s_lshl_b32 s69, s43, 10\n"
      "s_add_u32 s68, s68, s69\n"
      "s_lshl_b32 s69, s50, 11\n"
      "s_sub_u32 s68, s68, s69\n"
      "s_add_u32 s68, s68, 6291456\n"
      "s_cmp_ge_u32 s45, 24\n"
      "s_cselect_b32 s68, 2146435072, s68\n"
      "ds_write_b128 v41, v[72:75] offset:6144\n"
      "ds_write_b128 v41, v[76:79] offset:7168\n"
      "s_waitcnt vmcnt(10)\n"
      "v_cmp_eq_u64 vcc, s[40:41], v[12:13]\n"
      "s_nop 0\n"
      "s_cmp_eq_u64 vcc, -1\n"
      "s_cbranch_scc1 .Lok_20\n"
      "s_mov_b32 s82, 0x20000\n"
      ".Lsp_21:\n"
      "s_sleep 4\n"
      "global_load_dwordx2 v[12:13], v20, s[16:17] sc1\n"
      "s_waitcnt vmcnt(0)\n"
      "v_cmp_eq_u64 vcc, s[40:41], v[12:13]\n"
      "s_nop 0\n"
      "s_cmp_eq_u64 vcc, -1\n"
      "s_cbranch_scc1 .Lok_20\n"
      "s_sub_u32 s82, s82, 1\n"
      "s_cmp_lg_u32 s82, 0\n"
      "s_cbranch_scc1 .Lsp_21\n"
      ".Lok_20:\n"
      "s_mul_i32 s69, s47, 160\n"
      "v_add_u32 v43, s69, v2\n"
      "v_min_u32 v43, 1264, v43\n"
      "s_lshl_b32 s71, s43, 10\n"
      "s_add_u32 m0, s71, 61440\n"
      "s_nop 0\n"
      "global_load_lds_dwordx4 v43, s[34:35]\n"
      "s_add_u32 s71, s71, 61440\n"
      "v_lshl_add_u32 v34, v5, 3, s71\n"
      "s_lshr_b32 s69, s46, 1\n"
      "s_mul_i32 s69, s69, 5120\n"
      "s_and_b32 s71, s46, 1\n"
      "s_lshl_b32 s71, s71, 8\n"
      "s_add_u32 s69, s69, s71\n"
      "s_lshl_b32 s71, s43, 6\n"
      "s_add_u32 s69, s69, s71\n"
      "s_add_u32 s70, s69, 131072\n"
      "v_lshlrev_b32 v43, 2, v4\n"
      "v_lshl_add_u32 v43, v5, 9, v43\n"
      "v_and_b32 v35, 2, v5\n"
      "v_lshl_add_u32 v35, v35, 29, v43\n"
      "buffer_load_dword v22, v43, s[16:19], s70 offen sc1\n"
      "buffer_load_dword v23, v43, s[16:19], s70 offen offset:2048 sc1\n"
      "s_add_u32 s70, s70, 4096\n"
      "buffer_load_dword v35, v35, s[16:19], s70 offen sc1\n"
      "s_add_u32 s92, s60, 0\n"
      "s_sub_u32 s93, s92, 14\n"
      "s_min_u32 s92, s92, s93\n"
      "s_lshl_b32 s93, s92, 10\n"
      "s_cmp_eq_u32 s92, s61\n"
      "s_cselect_b32 s93, 2146435072, s93\n"
      "buffer_load_dwordx4 v[144:147], v6, s[16:19], s93 offen sc1\n"
      "s_waitcnt lgkmcnt(0)\n"
      "s_barrier\n"
      "ds_read_b128 v[80:83], v42 offset:0\n"
      "ds_read_b128 v[84:87], v42 offset:1024\n"
      "ds_read_b128 v[88:91], v42 offset:8192\n"
      "ds_read_b128 v[92:95], v42 offset:9216\n"
      "ds_read_b128 v[96:99], v42 offset:16384\n"
      "ds_read_b128 v[100:103], v42 offset:17408\n"
      "ds_read_b128 v[104:107], v42 offset:24576\n"
      "ds_read_b128 v[108:111], v42 offset:25600\n"
      "buffer_load_dwordx4 v[148:151], v7, s[16:19], s93 offen sc1\n"
      "buffer_load_dwordx4 v[152:155], v8, s[16:19], s93 offen sc1\n"
      "buffer_load_dwordx4 v[156:159], v9, s[16:19], s93 offen sc1\n"
      "s_add_u32 s92, s60, 1\n"
      "s_sub_u32 s93, s92, 14\n"
      "s_min_u32 s92, s92, s93\n"
      "s_lshl_b32 s93, s92, 10\n"
      "s_cmp_eq_u32 s92, s61\n"
      "s_cselect_b32 s93, 2146435072, s93\n"
      "buffer_load_dwordx4 v[160:163], v6, s[16:19], s93 offen sc1\n"
      "buffer_load_dwordx4 v[164:167], v7, s[16:19], s93 offen sc1\n"
      "buffer_load_dwordx4 v[168:171], v8, s[16:19], s93 offen sc1\n"
      "buffer_load_dwordx4 v[172:175], v9, s[16:19], s93 offen sc1\n"
      "s_add_u32 s92, s60, 2\n"
      "s_sub_u32 s93, s92, 14\n"
      "s_min_u32 s92, s92, s93\n"
      "s_lshl_b32 s93, s92, 10\n"
      "s_cmp_eq_u32 s92, s61\n"
      "s_cselect_b32 s93, 2146435072, s93\n"
      "buffer_load_dwordx4 v[176:179], v6, s[16:19], s93 offen sc1\n"
      "buffer_load_dwordx4 v[180:183], v7, s[16:19], s93 offen sc1\n"
      "buffer_load_dwordx4 v[184:187], v8, s[16:19], s93 offen sc1\n"
      "buffer_load_dwordx4 v[188:191], v9, s[16:19], s93 offen sc1\n"
      "s_waitcnt lgkmcnt(5)\n"
      "v_pk_add_f32 v[80:81], v[80:81], v[88:89]\n"
      "v_pk_add_f32 v[82:83], v[82:83], v[90:91]\n"
      "s_waitcnt lgkmcnt(4)\n"
      "v_pk_add_f32 v[84:85], v[84:85], v[92:93]\n"
      "v_pk_add_f32 v[86:87], v[86:87], v[94:95]\n"
      "s_waitcnt lgkmcnt(1)\n"
      "v_pk_add_f32 v[96:97], v[96:97], v[104:105]\n"
      "v_pk_add_f32 v[98:99], v[98:99], v[106:107]\n"
      "s_waitcnt lgkmcnt(0)\n"
      "v_pk_add_f32 v[100:101], v[100:101], v[108:109]\n"
      "v_pk_add_f32 v[102:103], v[102:103], v[110:111]\n"
      "v_pk_add_f32 v[80:81], v[80:81], v[96:97]\n"
      "v_pk_add_f32 v[82:83], v[82:83], v[98:99]\n"
      "v_pk_add_f32 v[84:85], v[84:85], v[100:101]\n"
      "v_pk_add_f32 v[86:87], v[86:87], v[102:103]\n"
      "v_cvt_pk_bf16_f32 v88, v80, v81\n"
      "v_cvt_pk_bf16_f32 v89, v82, v83\n"
      "v_cvt_pk_bf16_f32 v90, v84, v85\n"
      "v_cvt_pk_bf16_f32 v91, v86, v87\n"
      "s_nop 0\n"
      "v_permlane16_swap_b32 v88, v90\n"
      "v_permlane16_swap_b32 v89, v91\n"
      "buffer_store_dwordx4 v[88:91], v30, s[16:19], s68 offen sc1\n"
      "s_lshl_b32 s98, s63, 12\n"
      "s_add_u32 s98, s98, 6291456\n"
      "buffer_store_dwordx4 v[208:211], v30, s[16:19], s98 offen sc1\n"
      "buffer_store_dwordx4 v[212:215], v30, s[16:19], s98 offen offset:1024 sc1\n"
      "s_mov_b32 s83, 0\n"
      ".Lgemm2:\n"
      "s_waitcnt vmcnt(11)\n"
      "v_mfma_f32_16x16x32_bf16 v[48:51], a[0:3], v[144:147], 0\n"
      "s_add_u32 s92, s60, 3\n"
      "s_sub_u32 s93, s92, 14\n"
      "s_min_u32 s92, s92, s93\n"
      "v_mfma_f32_16x16x32_bf16 v[64:67], a[4:7], v[144:147], 0\n"
      "s_lshl_b32 s93, s92, 10\n"
      "s_cmp_eq_u32 s92, s61\n"
      "s_cselect_b32 s93, 2146435072, s93\n"
      "v_mfma_f32_16x16x32_bf16 v[80:83], a[8:11], v[144:147], 0\n"
      "buffer_load_dwordx4 v[192:195], v6, s[16:19], s93 offen sc1\n"
      "v_mfma_f32_16x16x32_bf16 v[96:99], a[12:15], v[144:147], 0\n"
      "buffer_load_dwordx4 v[196:199], v7, s[16:19], s93 offen sc1\n"
      "v_mfma_f32_16x16x32_bf16 v[112:115], a[16:19], v[144:147], 0\n"
      "buffer_load_dwordx4 v[200:203], v8, s[16:19], s93 offen sc1\n"
      "s_waitcnt vmcnt(13)\n"
      "v_mfma_f32_16x16x32_bf16 v[52:55], a[0:3], v[148:151], 0\n"
      "buffer_load_dwordx4 v[204:207], v9, s[16:19], s93 offen sc1\n"
      "v_mfma_f32_16x16x32_bf16 v[68:71], a[4:7], v[148:151], 0\n"
      "v_mfma_f32_16x16x32_bf16 v[84:87], a[8:11], v[148:151], 0\n"
      "v_mfma_f32_16x16x32_bf16 v[100:103], a[12:15], v[148:151], 0\n"
      "v_mfma_f32_16x16x32_bf16 v[116:119], a[16:19], v[148:151], 0\n"
      "s_waitcnt vmcnt(13)\n"
      "v_mfma_f32_16x16x32_bf16 v[56:59], a[0:3], v[152:155], 0\n"
      "v_mfma_f32_16x16x32_bf16 v[72:75], a[4:7], v[152:155], 0\n"
      "v_mfma_f32_16x16x32_bf16 v[88:91], a[8:11], v[152:155], 0\n"
      "v_mfma_f32_16x16x32_bf16 v[104:107], a[12:15], v[152:155], 0\n"
      "v_mfma_f32_16x16x32_bf16 v[120:123], a[16:19], v[152:155], 0\n"
      "s_waitcnt vmcnt(12)\n"
      "v_mfma_f32_16x16x32_bf16 v[60:63], a[0:3], v[156:159], 0\n"
      "v_mfma_f32_16x16x32_bf16 v[76:79], a[4:7], v[156:159], 0\n"
      "v_mfma_f32_16x16x32_bf16 v[92:95], a[8:11], v[156:159], 0\n"
      "v_mfma_f32_16x16x32_bf16 v[108:111], a[12:15], v[156:159], 0\n"
      "v_mfma_f32_16x16x32_bf16 v[124:127], a[16:19], v[156:159], 0\n"
      "s_waitcnt vmcnt(11)\n"
      "v_mfma_f32_16x16x32_bf16 v[48:51], a[20:23], v[160:163], v[48:51]\n"
      "s_add_u32 s92, s60, 4\n"
      "s_sub_u32 s93, s92, 14\n"
      "s_min_u32 s92, s92, s93\n"
      "v_mfma_f32_16x16x32_bf16 v[64:67], a[24:27], v[160:163], v[64:67]\n"
      "s_lshl_b32 s93, s92, 10\n"
      "s_cmp_eq_u32 s92, s61\n"
      "s_cselect_b32 s93, 2146435072, s93\n"
      "v_mfma_f32_16x16x32_bf16 v[80:83], a[28:31], v[160:163], v[80:83]\n"
      "buffer_load_dwordx4 v[208:211], v6, s[16:19], s93 offen sc1\n"
      "v_mfma_f32_16x16x32_bf16 v[96:99], a[32:35], v[160:163], v[96:99]\n"
      "buffer_load_dwordx4 v[212:215], v7, s[16:19], s93 offen sc1\n"
      "v_mfma_f32_16x16x32_bf16 v[112:115], a[36:39], v[160:163], v[112:115]\n"
      "buffer_load_dwordx4 v[216:219], v8, s[16:19], s93 offen sc1\n"
      "s_waitcnt vmcnt(13)\n"
      "v_mfma_f32_16x16x32_bf16 v[52:55], a[20:23], v[164:167], v[52:55]\n"
      "buffer_load_dwordx4 v[220:223], v9, s[16:19], s93 offen sc1\n"
      "v_mfma_f32_16x16x32_bf16 v[68:71], a[24:27], v[164:167], v[68:71]\n"
      "v_mfma_f32_16x16x32_bf16 v[84:87], a[28:31], v[164:167], v[84:87]\n"
      "v_mfma_f32_16x16x32_bf16 v[100:103], a[32:35], v[164:167], v[100:103]\n"
      "v_mfma_f32_16x16x32_bf16 v[116:119], a[36:39], v[164:167], v[116:119]\n"
      "s_waitcnt vmcnt(13)\n"
      "v_mfma_f32_16x16x32_bf16 v[56:59], a[20:23], v[168:171], v[56:59]\n"
      "v_mfma_f32_16x16x32_bf16 v[72:75], a[24:27], v[168:171], v[72:75]\n"
      "v_mfma_f32_16x16x32_bf16 v[88:91], a[28:31], v[168:171], v[88:91]\n"
      "v_mfma_f32_16x16x32_bf16 v[104:107], a[32:35], v[168:171], v[104:107]\n"
      "v_mfma_f32_16x16x32_bf16 v[120:123], a[36:39], v[168:171], v[120:123]\n"
      "s_waitcnt vmcnt(12)\n"
      "v_mfma_f32_16x16x32_bf16 v[60:63], a[20:23], v[172:175], v[60:63]\n"
      "v_mfma_f32_16x16x32_bf16 v[76:79], a[24:27], v[172:175], v[76:79]\n"
      "v_mfma_f32_16x16x32_bf16 v[92:95], a[28:31], v[172:175], v[92:95]\n"
      "v_mfma_f32_16x16x32_bf16 v[108:111], a[32:35], v[172:175], v[108:111]\n"
      "v_mfma_f32_16x16x32_bf16 v[124:127], a[36:39], v[172:175], v[124:127]\n"
      "s_waitcnt vmcnt(11)\n"
      "v_mfma_f32_16x16x32_bf16 v[48:51], a[40:43], v[176:179], v[48:51]\n"
      "s_add_u32 s92, s60, 5\n"
      "s_sub_u32 s93, s92, 14\n"
      "s_min_u32 s92, s92, s93\n"
      "v_mfma_f32_16x16x32_bf16 v[64:67], a[44:47], v[176:179], v[64:67]\n"
      "s_lshl_b32 s93, s92, 10\n"
      "s_cmp_eq_u32 s92, s61\n"
      "s_cselect_b32 s93, 2146435072, s93\n"
      "v_mfma_f32_16x16x32_bf16 v[80:83], a[48:51], v[176:179], v[80:83]\n"
      "buffer_load_dwordx4 v[128:131], v6, s[16:19], s93 offen sc1\n"
      "v_mfma_f32_16x16x32_bf16 v[96:99], a[52:55], v[176:179], v[96:99]\n"
      "buffer_load_dwordx4 v[132:135], v7, s[16:19], s93 offen sc1\n"
      "v_mfma_f32_16x16x32_bf16 v[112:115], a[56:59], v[176:179], v[112:115]\n"
      "buffer_load_dwordx4 v[136:139], v8, s[16:19], s93 offen sc1\n"
      "s_waitcnt vmcnt(13)\n"
      "v_mfma_f32_16x16x32_bf16 v[52:55], a[40:43], v[180:183], v[52:55]\n"
      "buffer_load_dwordx4 v[140:143], v9, s[16:19], s93 offen sc1\n"
      "v_mfma_f32_16x16x32_bf16 v[68:71], a[44:47], v[180:183], v[68:71]\n"
      "v_mfma_f32_16x16x32_bf16 v[84:87], a[48:51], v[180:183], v[84:87]\n"
      "v_mfma_f32_16x16x32_bf16 v[100:103], a[52:55], v[180:183], v[100:103]\n"
      "v_mfma_f32_16x16x32_bf16 v[116:119], a[56:59], v[180:183], v[116:119]\n"
      "s_waitcnt vmcnt(13)\n"
      "v_mfma_f32_16x16x32_bf16 v[56:59], a[40:43], v[184:187], v[56:59]\n"
      "v_mfma_f32_16x16x32_bf16 v[72:75], a[44:47], v[184:187], v[72:75]\n"
      "v_mfma_f32_16x16x32_bf16 v[88:91], a[48:51], v[184:187], v[88:91]\n"
      "v_mfma_f32_16x16x32_bf16 v[104:107], a[52:55], v[184:187], v[104:107]\n"
      "v_mfma_f32_16x16x32_bf16 v[120:123], a[56:59], v[184:187], v[120:123]\n"
      "s_waitcnt vmcnt(12)\n"
      "v_mfma_f32_16x16x32_bf16 v[60:63], a[40:43], v[188:191], v[60:63]\n"
      "v_mfma_f32_16x16x32_bf16 v[76:79], a[44:47], v[188:191], v[76:79]\n"
      "v_mfma_f32_16x16x32_bf16 v[92:95], a[48:51], v[188:191], v[92:95]\n"
      "v_mfma_f32_16x16x32_bf16 v[108:111], a[52:55], v[188:191], v[108:111]\n"
      "v_mfma_f32_16x16x32_bf16 v[124:127], a[56:59], v[188:191], v[124:127]\n"
      "s_barrier\n"
      "s_cmp_lg_u32 s43, 0\n"
      "s_cbranch_scc1 .Lw0_33\n"
      "s_lshl_b32 s98, s42, 7\n"
      "s_add_u32 s98, s98, 65536\n"
      "v_mov_b32 v14, s98\n"
      "v_mov_b32 v16, s40\n"
      "v_mov_b32 v17, s41\n"
      "global_store_dwordx2 v14, v[16:17], s[16:17] sc1\n"
      "s_nop 0\n"
      ".Lw0_33:\n"
      "s_waitcnt vmcnt(11)\n"
      "v_mfma_f32_16x16x32_bf16 v[48:51], a[60:63], v[192:195], v[48:51]\n"
      "s_add_u32 s92, s60, 6\n"
      "s_sub_u32 s93, s92, 14\n"
      "s_min_u32 s92, s92, s93\n"
      "v_mfma_f32_16x16x32_bf16 v[64:67], a[64:67], v[192:195], v[64:67]\n"
      "s_lshl_b32 s93, s92, 10\n"
      "s_cmp_eq_u32 s92, s61\n"
      "s_cselect_b32 s93, 2146435072, s93\n"
      "v_mfma_f32_16x16x32_bf16 v[80:83], a[68:71], v[192:195], v[80:83]\n"
      "buffer_load_dwordx4 a[0:3], v6, s[16:19], s93 offen sc1\n"
      "v_mfma_f32_16x16x32_bf16 v[96:99], a[72:75], v[192:195], v[96:99]\n"
      "buffer_load_dwordx4 a[4:7], v7, s[16:19], s93 offen sc1\n"
      "v_mfma_f32_16x16x32_bf16 v[112:115], a[76:79], v[192:195], v[112:115]\n"
      "buffer_load_dwordx4 a[8:11], v8, s[16:19], s93 offen sc1\n"
      "s_waitcnt vmcnt(13)\n"
      "v_mfma_f32_16x16x32_bf16 v[52:55], a[60:63], v[196:199], v[52:55]\n"
      "buffer_load_dwordx4 a[12:15], v9, s[16:19], s93 offen sc1\n"
      "v_mfma_f32_16x16x32_bf16 v[68:71], a[64:67], v[196:199], v[68:71]\n"
      "v_mfma_f32_16x16x32_bf16 v[84:87], a[68:71], v[196:199], v[84:87]\n"
      "v_mfma_f32_16x16x32_bf16 v[100:103], a[72:75], v[196:199], v[100:103]\n"
      "v_mfma_f32_16x16x32_bf16 v[116:119], a[76:79], v[196:199], v[116:119]\n"
      "s_waitcnt vmcnt(13)\n"
      "v_mfma_f32_16x16x32_bf16 v[56:59], a[60:63], v[200:203], v[56:59]\n"
      "v_mfma_f32_16x16x32_bf16 v[72:75], a[64:67], v[200:203], v[72:75]\n"
      "v_mfma_f32_16x16x32_bf16 v[88:91], a[68:71], v[200:203], v[88:91]\n"
      "v_mfma_f32_16x16x32_bf16 v[104:107], a[72:75], v[200:203], v[104:107]\n"
      "v_mfma_f32_16x16x32_bf16 v[120:123], a[76:79], v[200:203], v[120:123]\n"
      "s_waitcnt vmcnt(12)\n"
      "v_mfma_f32_16x16x32_bf16 v[60:63], a[60:63], v[204:207], v[60:63]\n"
      "v_mfma_f32_16x16x32_bf16 v[76:79], a[64:67], v[204:207], v[76:79]\n"
      "v_mfma_f32_16x16x32_bf16 v[92:95], a[68:71], v[204:207], v[92:95]\n"
      "v_mfma_f32_16x16x32_bf16 v[108:111], a[72:75], v[204:207], v[108:111]\n"
      "v_mfma_f32_16x16x32_bf16 v[124:127], a[76:79], v[204:207], v[124:127]\n"
      "s_waitcnt vmcnt(11)\n"
      "v_mfma_f32_16x16x32_bf16 v[48:51], a[80:83], v[208:211], v[48:51]\n"
      "s_add_u32 s92, s60, 7\n"
      "s_sub_u32 s93, s92, 14\n"
      "s_min_u32 s92, s92, s93\n"
      "v_mfma_f32_16x16x32_bf16 v[64:67], a[84:87], v[208:211], v[64:67]\n"
      "s_lshl_b32 s93, s92, 10\n"
      "s_cmp_eq_u32 s92, s61\n"
      "s_cselect_b32 s93, 2146435072, s93\n"
      "v_mfma_f32_16x16x32_bf16 v[80:83], a[88:91], v[208:211], v[80:83]\n"
      "buffer_load_dwordx4 v[144:147], v6, s[16:19], s93 offen sc1\n"
      "v_mfma_f32_16x16x32_bf16 v[96:99], a[92:95], v[208:211], v[96:99]\n"
      "buffer_load_dwordx4 v[148:151], v7, s[16:19], s93 offen sc1\n"
      "v_mfma_f32_16x16x32_bf16 v[112:115], a[96:99], v[208:211], v[112:115]\n"
      "buffer_load_dwordx4 v[152:155], v8, s[16:19], s93 offen sc1\n"
      "s_waitcnt vmcnt(13)\n"
      "v_mfma_f32_16x16x32_bf16 v[52:55], a[80:83], v[212:215], v[52:55]\n"
      "buffer_load_dwordx4 v[156:159], v9, s[16:19], s93 offen sc1\n"
      "v_mfma_f32_16x16x32_bf16 v[68:71], a[84:87], v[212:215], v[68:71]\n"
      "v_mfma_f32_16x16x32_bf16 v[84:87], a[88:91], v[212:215], v[84:87]\n"
      "v_mfma_f32_16x16x32_bf16 v[100:103], a[92:95], v[212:215], v[100:103]\n"
      "v_mfma_f32_16x16x32_bf16 v[116:119], a[96:99], v[212:215], v[116:119]\n"
      "s_waitcnt vmcnt(13)\n"
      "v_mfma_f32_16x16x32_bf16 v[56:59], a[80:83], v[216:219], v[56:59]\n"
      "v_mfma_f32_16x16x32_bf16 v[72:75], a[84:87], v[216:219], v[72:75]\n"
      "v_mfma_f32_16x16x32_bf16 v[88:91], a[88:91], v[216:219], v[88:91]\n"
      "v_mfma_f32_16x16x32_bf16 v[104:107], a[92:95], v[216:219], v[104:107]\n"
      "v_mfma_f32_16x16x32_bf16 v[120:123], a[96:99], v[216:219], v[120:123]\n"
      "s_waitcnt vmcnt(12)\n"
      "v_mfma_f32_16x16x32_bf16 v[60:63], a[80:83], v[220:223], v[60:63]\n"
      "v_mfma_f32_16x16x32_bf16 v[76:79], a[84:87], v[220:223], v[76:79]\n"
      "v_mfma_f32_16x16x32_bf16 v[92:95], a[88:91], v[220:223], v[92:95]\n"
      "v_mfma_f32_16x16x32_bf16 v[108:111], a[92:95], v[220:223], v[108:111]\n"
      "v_mfma_f32_16x16x32_bf16 v[124:127], a[96:99], v[220:223], v[124:127]\n"
      "s_waitcnt vmcnt(11)\n"
      "v_mfma_f32_16x16x32_bf16 v[48:51], a[100:103], v[128:131], v[48:51]\n"
      "s_add_u32 s92, s60, 8\n"
      "s_sub_u32 s93, s92, 14\n"
      "s_min_u32 s92, s92, s93\n"
      "v_mfma_f32_16x16x32_bf16 v[64:67], a[104:107], v[128:131], v[64:67]\n"
      "s_lshl_b32 s93, s92, 10\n"
      "s_cmp_eq_u32 s92, s61\n"
      "s_cselect_b32 s93, 2146435072, s93\n"
      "v_mfma_f32_16x16x32_bf16 v[80:83], a[108:111], v[128:131], v[80:83]\n"
      "buffer_load_dwordx4 a[20:23], v6, s[16:19], s93 offen sc1\n"
      "v_mfma_f32_16x16x32_bf16 v[96:99], a[112:115], v[128:131], v[96:99]\n"
      "buffer_load_dwordx4 a[24:27], v7, s[16:19], s93 offen sc1\n"
      "v_mfma_f32_16x16x32_bf16 v[112:115], a[116:119], v[128:131], v[112:115]\n"
      "buffer_load_dwordx4 a[28:31], v8, s[16:19], s93 offen sc1\n"
      "s_waitcnt vmcnt(13)\n"
      "v_mfma_f32_16x16x32_bf16 v[52:55], a[100:103], v[132:135], v[52:55]\n"
      "buffer_load_dwordx4 a[32:35], v9, s[16:19], s93 offen sc1\n"
      "v_mfma_f32_16x16x32_bf16 v[68:71], a[104:107], v[132:135], v[68:71]\n"
      "v_mfma_f32_16x16x32_bf16 v[84:87], a[108:111], v[132:135], v[84:87]\n"
      "v_mfma_f32_16x16x32_bf16 v[100:103], a[112:115], v[132:135], v[100:103]\n"
      "v_mfma_f32_16x16x32_bf16 v[116:119], a[116:119], v[132:135], v[116:119]\n"
      "s_waitcnt vmcnt(13)\n"
      "v_mfma_f32_16x16x32_bf16 v[56:59], a[100:103], v[136:139], v[56:59]\n"
      "v_mfma_f32_16x16x32_bf16 v[72:75], a[104:107], v[136:139], v[72:75]\n"
      "v_mfma_f32_16x16x32_bf16 v[88:91], a[108:111], v[136:139], v[88:91]\n"
      "v_mfma_f32_16x16x32_bf16 v[104:107], a[112:115], v[136:139], v[104:107]\n"
      "v_mfma_f32_16x16x32_bf16 v[120:123], a[116:119], v[136:139], v[120:123]\n"
      "s_waitcnt vmcnt(12)\n"
      "v_mfma_f32_16x16x32_bf16 v[60:63], a[100:103], v[140:143], v[60:63]\n"
      "v_mfma_f32_16x16x32_bf16 v[76:79], a[104:107], v[140:143], v[76:79]\n"
      "v_mfma_f32_16x16x32_bf16 v[92:95], a[108:111], v[140:143], v[92:95]\n"
      "v_mfma_f32_16x16x32_bf16 v[108:111], a[112:115], v[140:143], v[108:111]\n"
      "v_mfma_f32_16x16x32_bf16 v[124:127], a[116:119], v[140:143], v[124:127]\n"
      "s_waitcnt vmcnt(11)\n"
      "v_mfma_f32_16x16x32_bf16 v[48:51], a[120:123], a[0:3], v[48:51]\n"
      "s_add_u32 s92, s60, 9\n"
      "s_sub_u32 s93, s92, 14\n"
      "s_min_u32 s92, s92, s93\n"
      "v_mfma_f32_16x16x32_bf16 v[64:67], a[124:127], a[0:3], v[64:67]\n"
      "s_lshl_b32 s93, s92, 10\n"
      "s_cmp_eq_u32 s92, s61\n"
      "s_cselect_b32 s93, 2146435072, s93\n"
      "v_mfma_f32_16x16x32_bf16 v[80:83], a[128:131], a[0:3], v[80:83]\n"
      "buffer_load_dwordx4 v[160:163], v6, s[16:19], s93 offen sc1\n"
      "v_mfma_f32_16x16x32_bf16 v[96:99], a[132:135], a[0:3], v[96:99]\n"
      "buffer_load_dwordx4 v[164:167], v7, s[16:19], s93 offen sc1\n"
      "v_mfma_f32_16x16x32_bf16 v[112:115], a[136:139], a[0:3], v[112:115]\n"
      "buffer_load_dwordx4 v[168:171], v8, s[16:19], s93 offen sc1\n"
      "s_waitcnt vmcnt(13)\n"
      "v_mfma_f32_16x16x32_bf16 v[52:55], a[120:123], a[4:7], v[52:55]\n"
      "buffer_load_dwordx4 v[172:175], v9, s[16:19], s93 offen sc1\n"
      "v_mfma_f32_16x16x32_bf16 v[68:71], a[124:127], a[4:7], v[68:71]\n"
      "s_mul_i32 s68, s43, 15360\n"
      "s_add_u32 s68, s68, 0\n"
      "v_add_u32 v44, s68, v2\n"
      "v_mfma_f32_16x16x32_bf16 v[84:87], a[128:131], a[4:7], v[84:87]\n"
      "v_mfma_f32_16x16x32_bf16 v[100:103], a[132:135], a[4:7], v[100:103]\n"
      "v_lshrrev_b32 v45, 2, v4\n"
      "v_mfma_f32_16x16x32_bf16 v[116:119], a[136:139], a[4:7], v[116:119]\n"
      "s_waitcnt vmcnt(13)\n"
      "v_mfma_f32_16x16x32_bf16 v[56:59], a[120:123], a[8:11], v[56:59]\n"
      "v_lshl_add_u32 v45, v5, 2, v45\n"
      "v_mfma_f32_16x16x32_bf16 v[72:75], a[124:127], a[8:11], v[72:75]\n"
      "v_mfma_f32_16x16x32_bf16 v[88:91], a[128:131], a[8:11], v[88:91]\n"
      "v_mfma_f32_16x16x32_bf16 v[104:107], a[132:135], a[8:11], v[104:107]\n"
      "v_and_b32 v42, 3, v1\n"
      "v_mfma_f32_16x16x32_bf16 v[120:123], a[136:139], a[8:11], v[120:123]\n"
      "s_waitcnt vmcnt(12)\n"
      "v_mfma_f32_16x16x32_bf16 v[60:63], a[120:123], a[12:15], v[60:63]\n"
      "v_lshlrev_b32 v42, 3, v42\n"
      "v_mfma_f32_16x16x32_bf16 v[76:79], a[124:127], a[12:15], v[76:79]\n"
      "v_mfma_f32_16x16x32_bf16 v[92:95], a[128:131], a[12:15], v[92:95]\n"
      "v_lshl_add_u32 v45, v45, 7, v42\n"
      "v_mfma_f32_16x16x32_bf16 v[108:111], a[132:135], a[12:15], v[108:111]\n"
      "v_mfma_f32_16x16x32_bf16 v[124:127], a[136:139], a[12:15], v[124:127]\n"
      "s_waitcnt vmcnt(11)\n"
      "v_mfma_f32_16x16x32_bf16 v[48:51], a[140:143], v[144:147], v[48:51]\n"
      "s_add_u32 s92, s60, 10\n"
      "s_sub_u32 s93, s92, 14\n"
      "s_min_u32 s92, s92, s93\n"
      "v_mfma_f32_16x16x32_bf16 v[64:67], a[144:147], v[144:147], v[64:67]\n"
      "s_lshl_b32 s93, s92, 10\n"
      "s_cmp_eq_u32 s92, s61\n"
      "s_cselect_b32 s93, 2146435072, s93\n"
      "v_mfma_f32_16x16x32_bf16 v[80:83], a[148:151], v[144:147], v[80:83]\n"
      "buffer_load_dwordx4 a[40:43], v6, s[16:19], s93 offen sc1\n"
      "v_mfma_f32_16x16x32_bf16 v[96:99], a[152:155], v[144:147], v[96:99]\n"
      "buffer_load_dwordx4 a[44:47], v7, s[16:19], s93 offen sc1\n"
      "v_mfma_f32_16x16x32_bf16 v[112:115], a[156:159], v[144:147], v[112:115]\n"
      "buffer_load_dwordx4 a[48:51], v8, s[16:19], s93 offen sc1\n"
      "s_waitcnt vmcnt(13)\n"
      "v_mfma_f32_16x16x32_bf16 v[52:55], a[140:143], v[148:151], v[52:55]\n"
      "buffer_load_dwordx4 a[52:55], v9, s[16:19], s93 offen sc1\n"
      "v_mfma_f32_16x16x32_bf16 v[68:71], a[144:147], v[148:151], v[68:71]\n"
      "s_lshl_b32 s68, s43, 5\n"
      "s_add_u32 s68, s68, 81920\n"
      "v_add_u32 v45, s68, v45\n"
      "v_mfma_f32_16x16x32_bf16 v[84:87], a[148:151], v[148:151], v[84:87]\n"
      "v_mfma_f32_16x16x32_bf16 v[100:103], a[152:155], v[148:151], v[100:103]\n"
      "s_lshl_b32 s68, s46, 2\n"
      "s_add_u32 s68, s68, s43\n"
      "s_mul_i32 s68, s68, 20480\n"
      "v_mfma_f32_16x16x32_bf16 v[116:119], a[156:159], v[148:151], v[116:119]\n"
      "s_waitcnt vmcnt(13)\n"
      "v_mfma_f32_16x16x32_bf16 v[56:59], a[140:143], v[152:155], v[56:59]\n"
      "s_mul_i32 s69, s47, 160\n"
      "s_add_u32 s68, s68, s69\n"
      "v_mul_u32_u24 v46, 1280, v4\n"
      "v_add_u32 v46, s68, v46\n"
      "v_mfma_f32_16x16x32_bf16 v[72:75], a[144:147], v[152:155], v[72:75]\n"
      "v_mfma_f32_16x16x32_bf16 v[88:91], a[148:151], v[152:155], v[88:91]\n"
      "v_mfma_f32_16x16x32_bf16 v[104:107], a[152:155], v[152:155], v[104:107]\n"
      "v_lshl_add_u32 v47, v5, 3, v46\n"
      "v_mfma_f32_16x16x32_bf16 v[120:123], a[156:159], v[152:155], v[120:123]\n"
      "s_waitcnt vmcnt(12)\n"
      "v_mfma_f32_16x16x32_bf16 v[60:63], a[140:143], v[156:159], v[60:63]\n"
      "v_and_b32 v42, 1, v5\n"
      "v_mfma_f32_16x16x32_bf16 v[76:79], a[144:147], v[156:159], v[76:79]\n"
      "v_mfma_f32_16x16x32_bf16 v[92:95], a[148:151], v[156:159], v[92:95]\n"
      "v_lshl_add_u32 v46, v42, 5, v46\n"
      "v_mfma_f32_16x16x32_bf16 v[108:111], a[152:155], v[156:159], v[108:111]\n"
      "v_mfma_f32_16x16x32_bf16 v[124:127], a[156:159], v[156:159], v[124:127]\n"
      "global_load_dwordx2 v[12:13], v21, s[16:17] sc1\n"
      "s_waitcnt vmcnt(12)\n"
      "v_mfma_f32_16x16x32_bf16 v[48:51], a[160:163], a[20:23], v[48:51]\n"
      "s_add_u32 s92, s60, 11\n"
      "s_sub_u32 s93, s92, 14\n"
      "s_min_u32 s92, s92, s93\n"
      "v_mfma_f32_16x16x32_bf16 v[64:67], a[164:167], a[20:23], v[64:67]\n"
      "s_lshl_b32 s93, s92, 10\n"
      "s_cmp_eq_u32 s92, s61\n"
      "s_cselect_b32 s93, 2146435072, s93\n"
      "v_mfma_f32_16x16x32_bf16 v[80:83], a[168:171], a[20:23], v[80:83]\n"
      "buffer_load_dwordx4 v[176:179], v6, s[16:19], s93 offen sc1\n"
      "v_mfma_f32_16x16x32_bf16 v[96:99], a[172:175], a[20:23], v[96:99]\n"
      "buffer_load_dwordx4 v[180:183], v7, s[16:19], s93 offen sc1\n"
      "v_mfma_f32_16x16x32_bf16 v[112:115], a[176:179], a[20:23], v[112:115]\n"
      "buffer_load_dwordx4 v[184:187], v8, s[16:19], s93 offen sc1\n"
      "s_waitcnt vmcnt(14)\n"
      "v_mfma_f32_16x16x32_bf16 v[52:55], a[160:163], a[24:27], v[52:55]\n"
      "buffer_load_dwordx4 v[188:191], v9, s[16:19], s93 offen sc1\n"
      "v_mfma_f32_16x16x32_bf16 v[68:71], a[164:167], a[24:27], v[68:71]\n"
      "v_lshrrev_b32 v42, 1, v5\n"
      "v_mfma_f32_16x16x32_bf16 v[84:87], a[168:171], a[24:27], v[84:87]\n"
      "v_mfma_f32_16x16x32_bf16 v[100:103], a[172:175], a[24:27], v[100:103]\n"
      "v_lshl_add_u32 v46, v42, 4, v46\n"
      "v_mfma_f32_16x16x32_bf16 v[116:119], a[176:179], a[24:27], v[116:119]\n"
      "s_waitcnt vmcnt(14)\n"
      "v_mfma_f32_16x16x32_bf16 v[56:59], a[160:163], a[28:31], v[56:59]\n"
      "v_lshrrev_b32 v40, 3, v1\n"
      "v_mfma_f32_16x16x32_bf16 v[72:75], a[164:167], a[28:31], v[72:75]\n"
      "v_mfma_f32_16x16x32_bf16 v[88:91], a[168:171], a[28:31], v[88:91]\n"
      "v_mfma_f32_16x16x32_bf16 v[104:107], a[172:175], a[28:31], v[104:107]\n"
      "v_and_b32 v41, 7, v1\n"
      "v_mfma_f32_16x16x32_bf16 v[120:123], a[176:179], a[28:31], v[120:123]\n"
      "s_waitcnt vmcnt(13)\n"
      "v_mfma_f32_16x16x32_bf16 v[60:63], a[160:163], a[32:35], v[60:63]\n"
      "v_lshlrev_b32 v40, 12, v40\n"
      "v_mfma_f32_16x16x32_bf16 v[76:79], a[164:167], a[32:35], v[76:79]\n"
      "v_mfma_f32_16x16x32_bf16 v[92:95], a[168:171], a[32:35], v[92:95]\n"
      "v_lshl_add_u32 v40, v41, 4, v40\n"
      "v_mfma_f32_16x16x32_bf16 v[108:111], a[172:175], a[32:35], v[108:111]\n"
      "v_mfma_f32_16x16x32_bf16 v[124:127], a[176:179], a[32:35], v[124:127]\n"
      "s_waitcnt vmcnt(12)\n"
      "v_mfma_f32_16x16x32_bf16 v[48:51], a[180:183], v[160:163], v[48:51]\n"
      "s_add_u32 s92, s60, 12\n"
      "s_sub_u32 s93, s92, 14\n"
      "s_min_u32 s92, s92, s93\n"
      "v_mfma_f32_16x16x32_bf16 v[64:67], a[184:187], v[160:163], v[64:67]\n"
      "s_lshl_b32 s93, s92, 10\n"
      "s_cmp_eq_u32 s92, s61\n"
      "s_cselect_b32 s93, 2146435072, s93\n"
      "v_mfma_f32_16x16x32_bf16 v[80:83], a[188:191], v[160:163], v[80:83]\n"
      "buffer_load_dwordx4 a[60:63], v6, s[16:19], s93 offen sc1\n"
      "v_mfma_f32_16x16x32_bf16 v[96:99], a[192:195], v[160:163], v[96:99]\n"
      "buffer_load_dwordx4 a[64:67], v7, s[16:19], s93 offen sc1\n"
      "v_mfma_f32_16x16x32_bf16 v[112:115], a[196:199], v[160:163], v[112:115]\n"
      "buffer_load_dwordx4 a[68:71], v8, s[16:19], s93 offen sc1\n"
      "s_waitcnt vmcnt(14)\n"
      "v_mfma_f32_16x16x32_bf16 v[52:55], a[180:183], v[164:167], v[52:55]\n"
      "buffer_load_dwordx4 a[72:75], v9, s[16:19], s93 offen sc1\n"
      "v_mfma_f32_16x16x32_bf16 v[68:71], a[184:187], v[164:167], v[68:71]\n"
      "v_mfma_f32_16x16x32_bf16 v[84:87], a[188:191], v[164:167], v[84:87]\n"
      "v_mfma_f32_16x16x32_bf16 v[100:103], a[192:195], v[164:167], v[100:103]\n"
      "v_mfma_f32_16x16x32_bf16 v[116:119], a[196:199], v[164:167], v[116:119]\n"
      "s_waitcnt vmcnt(14)\n"
      "v_mfma_f32_16x16x32_bf16 v[56:59], a[180:183], v[168:171], v[56:59]\n"
      "v_mfma_f32_16x16x32_bf16 v[72:75], a[184:187], v[168:171], v[72:75]\n"
      "v_mfma_f32_16x16x32_bf16 v[88:91], a[188:191], v[168:171], v[88:91]\n"
      "v_mfma_f32_16x16x32_bf16 v[104:107], a[192:195], v[168:171], v[104:107]\n"
      "v_mfma_f32_16x16x32_bf16 v[120:123], a[196:199], v[168:171], v[120:123]\n"
      "s_waitcnt vmcnt(13)\n"
      "v_mfma_f32_16x16x32_bf16 v[60:63], a[180:183], v[172:175], v[60:63]\n"
      "v_mfma_f32_16x16x32_bf16 v[76:79], a[184:187], v[172:175], v[76:79]\n"
      "v_mfma_f32_16x16x32_bf16 v[92:95], a[188:191], v[172:175], v[92:95]\n"
      "v_mfma_f32_16x16x32_bf16 v[108:111], a[192:195], v[172:175], v[108:111]\n"
      "v_mfma_f32_16x16x32_bf16 v[124:127], a[196:199], v[172:175], v[124:127]\n"
      "s_waitcnt vmcnt(12)\n"
      "v_mfma_f32_16x16x32_bf16 v[48:51], a[200:203], a[40:43], v[48:51]\n"
      "s_add_u32 s92, s60, 13\n"
      "s_sub_u32 s93, s92, 14\n"
      "s_min_u32 s92, s92, s93\n"
      "v_mfma_f32_16x16x32_bf16 v[64:67], a[204:207], a[40:43], v[64:67]\n"
      "s_lshl_b32 s93, s92, 10\n"
      "s_cmp_eq_u32 s92, s61\n"
      "s_cselect_b32 s93, 2146435072, s93\n"
      "v_mfma_f32_16x16x32_bf16 v[80:83], a[208:211], a[40:43], v[80:83]\n"
      "buffer_load_dwordx4 v[192:195], v6, s[16:19], s93 offen sc1\n"
      "v_mfma_f32_16x16x32_bf16 v[96:99], a[212:215], a[40:43], v[96:99]\n"
      "buffer_load_dwordx4 v[196:199], v7, s[16:19], s93 offen sc1\n"
      "v_mfma_f32_16x16x32_bf16 v[112:115], a[216:219], a[40:43], v[112:115]\n"
      "buffer_load_dwordx4 v[200:203], v8, s[16:19], s93 offen sc1\n"
      "s_waitcnt vmcnt(14)\n"
      "v_mfma_f32_16x16x32_bf16 v[52:55], a[200:203], a[44:47], v[52:55]\n"
      "buffer_load_dwordx4 v[204:207], v9, s[16:19], s93 offen sc1\n"
      "v_mfma_f32_16x16x32_bf16 v[68:71], a[204:207], a[44:47], v[68:71]\n"
      "v_mfma_f32_16x16x32_bf16 v[84:87], a[208:211], a[44:47], v[84:87]\n"
      "v_mfma_f32_16x16x32_bf16 v[100:103], a[212:215], a[44:47], v[100:103]\n"
      "ds_read_b64 v[24:25], v34 offset:0\n"
      "v_mfma_f32_16x16x32_bf16 v[116:119], a[216:219], a[44:47], v[116:119]\n"
      "s_waitcnt vmcnt(14)\n"
      "v_mfma_f32_16x16x32_bf16 v[56:59], a[200:203], a[48:51], v[56:59]\n"
      "ds_read_b64 v[26:27], v34 offset:32\n"
      "v_mfma_f32_16x16x32_bf16 v[72:75], a[204:207], a[48:51], v[72:75]\n"
      "v_mfma_f32_16x16x32_bf16 v[88:91], a[208:211], a[48:51], v[88:91]\n"
      "v_mfma_f32_16x16x32_bf16 v[104:107], a[212:215], a[48:51], v[104:107]\n"
      "ds_read_b64 v[28:29], v34 offset:64\n"
      "v_mfma_f32_16x16x32_bf16 v[120:123], a[216:219], a[48:51], v[120:123]\n"
      "s_waitcnt vmcnt(13)\n"
      "v_mfma_f32_16x16x32_bf16 v[60:63], a[200:203], a[52:55], v[60:63]\n"
      "ds_read_b64 v[30:31], v34 offset:96\n"
      "v_mfma_f32_16x16x32_bf16 v[76:79], a[204:207], a[52:55], v[76:79]\n"
      "v_mfma_f32_16x16x32_bf16 v[92:95], a[208:211], a[52:55], v[92:95]\n"
      "ds_read_b64 v[32:33], v34 offset:128\n"
      "v_mfma_f32_16x16x32_bf16 v[108:111], a[212:215], a[52:55], v[108:111]\n"
      "v_mfma_f32_16x16x32_bf16 v[124:127], a[216:219], a[52:55], v[124:127]\n"
      "s_waitcnt vmcnt(12)\n"
      "v_cmp_eq_u64 vcc, s[40:41], v[12:13]\n"
      "s_nop 0\n"
      "s_cmp_eq_u64 vcc, -1\n"
      "s_cbranch_scc1 .Lok_34\n"
      "s_mov_b32 s82, 0x20000\n"
      ".Lsp_35:\n"
      "s_sleep 4\n"
      "global_load_dwordx2 v[12:13], v21, s[16:17] sc1\n"
      "s_waitcnt vmcnt(0)\n"
      "v_cmp_eq_u64 vcc, s[40:41], v[12:13]\n"
      "s_nop 0\n"
      "s_cmp_eq_u64 vcc, -1\n"
      "s_cbranch_scc1 .Lok_34\n"
      "s_sub_u32 s82, s82, 1\n"
      "s_cmp_lg_u32 s82, 0\n"
      "s_cbranch_scc1 .Lsp_35\n"
      ".Lok_34:\n"
      "s_mul_i32 s68, s47, 80\n"
      "s_lshl_b32 s69, s43, 3\n"
      "s_add_u32 s68, s68, s69\n"
      "s_lshl_b32 s68, s68, 12\n"
      "s_lshl_b32 s69, s46, 7\n"
      "s_add_u32 s68, s68, s69\n"
      "s_add_u32 s68, s68, 6291456\n"
      "s_lshl_b32 s69, s43, 10\n"
      "s_add_u32 s69, s69, 81920\n"
      "s_add_u32 m0, s69, 0\n"
      "s_nop 0\n"
      "buffer_load_dwordx4 v40, s[16:19], s68 offen sc1 lds\n"
      "s_add_u32 s68, s68, 131072\n"
      "s_add_u32 m0, s69, 4096\n"
      "s_nop 0\n"
      "buffer_load_dwordx4 v40, s[16:19], s68 offen sc1 lds\n"
      "s_add_u32 s68, s68, 131072\n"
      "s_cmp_ge_u32 s43, 2\n"
      "s_cselect_b32 s68, 2146435072, s68\n"
      "s_add_u32 m0, s69, 8192\n"
      "s_nop 0\n"
      "buffer_load_dwordx4 v40, s[16:19], s68 offen sc1 lds\n"
      "s_add_u32 s68, s68, 131072\n"
      "s_waitcnt vmcnt(14)\n"
      "v_mfma_f32_16x16x32_bf16 v[48:51], a[220:223], v[176:179], v[48:51]\n"
      "v_mfma_f32_16x16x32_bf16 v[64:67], a[224:227], v[176:179], v[64:67]\n"
      "v_mfma_f32_16x16x32_bf16 v[80:83], a[228:231], v[176:179], v[80:83]\n"
      "v_mfma_f32_16x16x32_bf16 v[96:99], a[232:235], v[176:179], v[96:99]\n"
      "v_mfma_f32_16x16x32_bf16 v[112:115], a[236:239], v[176:179], v[112:115]\n"
      "s_waitcnt vmcnt(13)\n"
      "v_mfma_f32_16x16x32_bf16 v[52:55], a[220:223], v[180:183], v[52:55]\n"
      "v_mfma_f32_16x16x32_bf16 v[68:71], a[224:227], v[180:183], v[68:71]\n"
      "v_add_f32 v22, v22, v23\n"
      "v_add_f32 v22, v22, v35\n"
      "s_or_b32 s70, s42, s43\n"
      "s_cmp_lg_u32 s70, 0\n"
      "s_cbranch_scc1 .Le0_36\n"
      "v_lshlrev_b32 v38, 7, v1\n"
      "v_add_u32 v38, 65536, v38\n"
      "global_load_dwordx2 v[12:13], v38, s[16:17] sc1\n"
      "v_add_u32 v38, 8192, v38\n"
      "global_load_dwordx2 v[14:15], v38, s[16:17] sc1\n"
      "v_add_u32 v38, 8192, v38\n"
      "global_load_dwordx2 v[16:17], v38, s[16:17] sc1\n"
      "v_add_u32 v38, 8192, v38\n"
      "global_load_dwordx2 v[18:19], v38, s[16:17] sc1\n"
      ".Le0_36:\n"
      "v_mfma_f32_16x16x32_bf16 v[84:87], a[228:231], v[180:183], v[84:87]\n"
      "v_mov_b32 v23, v22\n"
      "v_mfma_f32_16x16x32_bf16 v[100:103], a[232:235], v[180:183], v[100:103]\n"
      "s_nop 0\n"
      "v_permlane16_swap_b32 v22, v23\n"
      "v_add_f32 v22, v22, v23\n"
      "v_mfma_f32_16x16x32_bf16 v[116:119], a[236:239], v[180:183], v[116:119]\n"
      "v_mov_b32 v23, v22\n"
      "s_waitcnt vmcnt(12)\n"
      "v_mfma_f32_16x16x32_bf16 v[56:59], a[220:223], v[184:187], v[56:59]\n"
      "v_permlane32_swap_b32 v22, v23\n"
      "v_mfma_f32_16x16x32_bf16 v[72:75], a[224:227], v[184:187], v[72:75]\n"
      "v_add_f32 v22, v22, v23\n"
      "v_cvt_pk_bf16_f32 v22, v22, v22\n"
      "v_mfma_f32_16x16x32_bf16 v[88:91], a[228:231], v[184:187], v[88:91]\n"
      "v_lshlrev_b32 v22, 16, v22\n"
      "v_mfma_f32_16x16x32_bf16 v[104:107], a[232:235], v[184:187], v[104:107]\n"
      "v_mul_f32 v22, 0xbfb8aa3b, v22\n"
      "v_mfma_f32_16x16x32_bf16 v[120:123], a[236:239], v[184:187], v[120:123]\n"
      "v_exp_f32 v22, v22\n"
      "s_nop 0\n"
      "v_add_f32 v22, 1.0, v22\n"
      "s_waitcnt vmcnt(11)\n"
      "v_mfma_f32_16x16x32_bf16 v[60:63], a[220:223], v[188:191], v[60:63]\n"
      "v_rcp_f32 v22, v22\n"
      "v_mfma_f32_16x16x32_bf16 v[76:79], a[224:227], v[188:191], v[76:79]\n"
      "v_cvt_pk_bf16_f32 v22, v22, v22\n"
      "v_mfma_f32_16x16x32_bf16 v[92:95], a[228:231], v[188:191], v[92:95]\n"
      "v_lshlrev_b32 v22, 16, v22\n"
      "v_sub_f32 v23, 1.0, v22\n"
      "v_mfma_f32_16x16x32_bf16 v[108:111], a[232:235], v[188:191], v[108:111]\n"
      "v_cvt_pk_bf16_f32 v23, v23, v23\n"
      "v_mfma_f32_16x16x32_bf16 v[124:127], a[236:239], v[188:191], v[124:127]\n"
      "v_lshlrev_b32 v23, 16, v23\n"
      "s_waitcnt vmcnt(10)\n"
      "v_mfma_f32_16x16x32_bf16 v[48:51], a[240:243], a[60:63], v[48:51]\n"
      "s_waitcnt lgkmcnt(4)\n"
      "v_lshlrev_b32 v128, 16, v24\n"
      "v_and_b32 v129, 0xffff0000, v24\n"
      "v_mfma_f32_16x16x32_bf16 v[64:67], a[244:247], a[60:63], v[64:67]\n"
      "v_lshlrev_b32 v130, 16, v25\n"
      "v_and_b32 v131, 0xffff0000, v25\n"
      "v_mfma_f32_16x16x32_bf16 v[80:83], a[248:251], a[60:63], v[80:83]\n"
      "s_waitcnt lgkmcnt(3)\n"
      "v_lshlrev_b32 v132, 16, v26\n"
      "v_and_b32 v133, 0xffff0000, v26\n"
      "v_mfma_f32_16x16x32_bf16 v[96:99], a[252:255], a[60:63], v[96:99]\n"
      "v_lshlrev_b32 v134, 16, v27\n"
      "v_and_b32 v135, 0xffff0000, v27\n"
      "v_mfma_f32_16x16x32_bf16 v[112:115], v[232:235], a[60:63], v[112:115]\n"
      "s_waitcnt lgkmcnt(2)\n"
      "v_lshlrev_b32 v136, 16, v28\n"
      "v_and_b32 v137, 0xffff0000, v28\n"
      "s_waitcnt vmcnt(6)\n"
      "v_mfma_f32_16x16x32_bf16 v[48:51], v[236:239], v[192:195], v[48:51]\n"
      "v_lshlrev_b32 v138, 16, v29\n"
      "v_and_b32 v139, 0xffff0000, v29\n"
      "v_mfma_f32_16x16x32_bf16 v[64:67], v[240:243], v[192:195], v[64:67]\n"
      "s_waitcnt lgkmcnt(1)\n"
      "v_lshlrev_b32 v140, 16, v30\n"
      "v_and_b32 v141, 0xffff0000, v30\n"
      "v_mfma_f32_16x16x32_bf16 v[80:83], v[244:247], v[192:195], v[80:83]\n"
      "v_lshlrev_b32 v142, 16, v31\n"
      "v_and_b32 v143, 0xffff0000, v31\n"
      "v_mfma_f32_16x16x32_bf16 v[96:99], v[248:251], v[192:195], v[96:99]\n"
      "s_waitcnt lgkmcnt(0)\n"
      "v_lshlrev_b32 v144, 16, v32\n"
      "v_and_b32 v145, 0xffff0000, v32\n"
      "v_mfma_f32_16x16x32_bf16 v[112:115], v[252:255], v[192:195], v[112:115]\n"
      "v_lshlrev_b32 v146, 16, v33\n"
      "v_and_b32 v147, 0xffff0000, v33\n"
      "v_mfma_f32_16x16x32_bf16 v[52:55], a[240:243], a[64:67], v[52:55]\n"
      "s_add_u32 s68, s43, 1\n"
      "s_and_b32 s68, s68, 3\n"
      "v_mfma_f32_16x16x32_bf16 v[68:71], a[244:247], a[64:67], v[68:71]\n"
      "s_mul_i32 s68, s68, 15360\n"
      "s_add_u32 s68, s68, 0\n"
      "v_mfma_f32_16x16x32_bf16 v[84:87], a[248:251], a[64:67], v[84:87]\n"
      "v_add_u32 v41, s68, v2\n"
      "v_mfma_f32_16x16x32_bf16 v[100:103], a[252:255], a[64:67], v[100:103]\n"
      "v_nop\n"
      "v_mfma_f32_16x16x32_bf16 v[116:119], v[232:235], a[64:67], v[116:119]\n"
      "ds_write_b128 v41, v[48:51] offset:0\n"
      "s_waitcnt vmcnt(5)\n"
      "v_mfma_f32_16x16x32_bf16 v[52:55], v[236:239], v[196:199], v[52:55]\n"
      "ds_write_b128 v41, v[64:67] offset:1024\n"
      "v_mfma_f32_16x16x32_bf16 v[68:71], v[240:243], v[196:199], v[68:71]\n"
      "ds_write_b128 v41, v[80:83] offset:2048\n"
      "v_mfma_f32_16x16x32_bf16 v[84:87], v[244:247], v[196:199], v[84:87]\n"
      "ds_write_b128 v41, v[96:99] offset:3072\n"
      "v_mfma_f32_16x16x32_bf16 v[100:103], v[248:251], v[196:199], v[100:103]\n"
      "ds_write_b128 v41, v[112:115] offset:4096\n"
      "v_mfma_f32_16x16x32_bf16 v[116:119], v[252:255], v[196:199], v[116:119]\n"
      "v_mfma_f32_16x16x32_bf16 v[56:59], a[240:243], a[68:71], v[56:59]\n"
      "s_add_u32 s68, s43, 2\n"
      "s_and_b32 s68, s68, 3\n"
      "v_mfma_f32_16x16x32_bf16 v[72:75], a[244:247], a[68:71], v[72:75]\n"
      "s_mul_i32 s68, s68, 15360\n"
      "s_add_u32 s68, s68, 5120\n"
      "v_mfma_f32_16x16x32_bf16 v[88:91], a[248:251], a[68:71], v[88:91]\n"
      "v_add_u32 v41, s68, v2\n"
      "v_mfma_f32_16x16x32_bf16 v[104:107], a[252:255], a[68:71], v[104:107]\n"
      "v_nop\n"
      "v_mfma_f32_16x16x32_bf16 v[120:123], v[232:235], a[68:71], v[120:123]\n"
      "ds_write_b128 v41, v[52:55] offset:0\n"
      "s_waitcnt vmcnt(4)\n"
      "v_mfma_f32_16x16x32_bf16 v[56:59], v[236:239], v[200:203], v[56:59]\n"
      "ds_write_b128 v41, v[68:71] offset:1024\n"
      "v_mfma_f32_16x16x32_bf16 v[72:75], v[240:243], v[200:203], v[72:75]\n"
      "ds_write_b128 v41, v[84:87] offset:2048\n"
      "v_mfma_f32_16x16x32_bf16 v[88:91], v[244:247], v[200:203], v[88:91]\n"
      "ds_write_b128 v41, v[100:103] offset:3072\n"
      "v_mfma_f32_16x16x32_bf16 v[104:107], v[248:251], v[200:203], v[104:107]\n"
      "ds_write_b128 v41, v[116:119] offset:4096\n"
      "v_mfma_f32_16x16x32_bf16 v[120:123], v[252:255], v[200:203], v[120:123]\n"
      "v_mfma_f32_16x16x32_bf16 v[60:63], a[240:243], a[72:75], v[60:63]\n"
      "s_add_u32 s68, s43, 3\n"
      "s_and_b32 s68, s68, 3\n"
      "v_mfma_f32_16x16x32_bf16 v[76:79], a[244:247], a[72:75], v[76:79]\n"
      "s_mul_i32 s68, s68, 15360\n"
      "s_add_u32 s68, s68, 10240\n"
      "v_mfma_f32_16x16x32_bf16 v[92:95], a[248:251], a[72:75], v[92:95]\n"
      "v_add_u32 v41, s68, v2\n"
      "v_mfma_f32_16x16x32_bf16 v[108:111], a[252:255], a[72:75], v[108:111]\n"
      "v_nop\n"
      "v_mfma_f32_16x16x32_bf16 v[124:127], v[232:235], a[72:75], v[124:127]\n"
      "ds_write_b128 v41, v[56:59] offset:0\n"
      "s_waitcnt vmcnt(3)\n"
      "v_mfma_f32_16x16x32_bf16 v[60:63], v[236:239], v[204:207], v[60:63]\n"
      "ds_write_b128 v41, v[72:75] offset:1024\n"
      "v_mfma_f32_16x16x32_bf16 v[76:79], v[240:243], v[204:207], v[76:79]\n"
      "ds_write_b128 v41, v[88:91] offset:2048\n"
      "v_mfma_f32_16x16x32_bf16 v[92:95], v[244:247], v[204:207], v[92:95]\n"
      "ds_write_b128 v41, v[104:107] offset:3072\n"
      "v_mfma_f32_16x16x32_bf16 v[108:111], v[248:251], v[204:207], v[108:111]\n"
      "ds_write_b128 v41, v[120:123] offset:4096\n"
      "v_mfma_f32_16x16x32_bf16 v[124:127], v[252:255], v[204:207], v[124:127]\n"
      "s_waitcnt vmcnt(0) lgkmcnt(0)\n"
      "s_barrier\n"
      "ds_read_b64_tr_b16 v[52:53], v45\n"
      "ds_read_b64_tr_b16 v[68:69], v45 offset:2048\n"
      "ds_read_b64_tr_b16 v[84:85], v45 offset:4096\n"
      "ds_read_b64_tr_b16 v[100:101], v45 offset:6144\n"
      "ds_read_b64_tr_b16 v[116:117], v45 offset:8192\n"
      "ds_read_b128 v[148:151], v44 offset:0\n"
      "ds_read_b128 v[152:155], v44 offset:1024\n"
      "ds_read_b128 v[156:159], v44 offset:2048\n"
      "ds_read_b128 v[160:163], v44 offset:3072\n"
      "ds_read_b128 v[164:167], v44 offset:4096\n"
      "ds_read_b128 v[168:171], v44 offset:5120\n"
      "ds_read_b128 v[172:175], v44 offset:6144\n"
      "ds_read_b128 v[176:179], v44 offset:7168\n"
      "ds_read_b128 v[180:183], v44 offset:8192\n"
      "ds_read_b128 v[184:187], v44 offset:9216\n"
      "ds_read_b128 v[188:191], v44 offset:10240\n"
      "ds_read_b128 v[192:195], v44 offset:11264\n"
      "ds_read_b128 v[196:199], v44 offset:12288\n"
      "ds_read_b128 v[200:203], v44 offset:13312\n"
      "ds_read_b128 v[204:207], v44 offset:14336\n"
      "v_pk_add_f32 v[60:61], v[60:61], v[128:129]\n"
      "v_pk_add_f32 v[62:63], v[62:63], v[130:131]\n"
      "v_pk_add_f32 v[76:77], v[76:77], v[132:133]\n"
      "v_pk_add_f32 v[78:79], v[78:79], v[134:135]\n"
      "v_pk_add_f32 v[92:93], v[92:93], v[136:137]\n"
      "v_pk_add_f32 v[94:95], v[94:95], v[138:139]\n"
      "v_pk_add_f32 v[108:109], v[108:109], v[140:141]\n"
      "v_pk_add_f32 v[110:111], v[110:111], v[142:143]\n"
      "v_pk_add_f32 v[124:125], v[124:125], v[144:145]\n"
      "v_pk_add_f32 v[126:127], v[126:127], v[146:147]\n"
      "s_waitcnt lgkmcnt(15)\n"
      "v_lshlrev_b32 v48, 16, v52\n"
      "v_and_b32 v49, 0xffff0000, v52\n"
      "v_lshlrev_b32 v50, 16, v53\n"
      "v_and_b32 v51, 0xffff0000, v53\n"
      "v_pk_mul_f32 v[48:49], v[48:49], v[22:23] op_sel_hi:[1,0]\n"
      "v_pk_mul_f32 v[50:51], v[50:51], v[22:23] op_sel_hi:[1,0]\n"
      "v_lshlrev_b32 v64, 16, v68\n"
      "v_and_b32 v65, 0xffff0000, v68\n"
      "v_lshlrev_b32 v66, 16, v69\n"
      "v_and_b32 v67, 0xffff0000, v69\n"
      "v_pk_mul_f32 v[64:65], v[64:65], v[22:23] op_sel_hi:[1,0]\n"
      "v_pk_mul_f32 v[66:67], v[66:67], v[22:23] op_sel_hi:[1,0]\n"
      "v_lshlrev_b32 v80, 16, v84\n"
      "v_and_b32 v81, 0xffff0000, v84\n"
      "v_lshlrev_b32 v82, 16, v85\n"
      "v_and_b32 v83, 0xffff0000, v85\n"
      "v_pk_mul_f32 v[80:81], v[80:81], v[22:23] op_sel_hi:[1,0]\n"
      "v_pk_mul_f32 v[82:83], v[82:83], v[22:23] op_sel_hi:[1,0]\n"
      "v_lshlrev_b32 v96, 16, v100\n"
      "v_and_b32 v97, 0xffff0000, v100\n"
      "v_lshlrev_b32 v98, 16, v101\n"
      "v_and_b32 v99, 0xffff0000, v101\n"
      "v_pk_mul_f32 v[96:97], v[96:97], v[22:23] op_sel_hi:[1,0]\n"
      "v_pk_mul_f32 v[98:99], v[98:99], v[22:23] op_sel_hi:[1,0]\n"
      "v_lshlrev_b32 v112, 16, v116\n"
      "v_and_b32 v113, 0xffff0000, v116\n"
      "v_lshlrev_b32 v114, 16, v117\n"
      "v_and_b32 v115, 0xffff0000, v117\n"
      "v_pk_mul_f32 v[112:113], v[112:113], v[22:23] op_sel_hi:[1,0]\n"
      "v_pk_mul_f32 v[114:115], v[114:115], v[22:23] op_sel_hi:[1,0]\n"
      "s_waitcnt lgkmcnt(14)\n"
      "v_pk_add_f32 v[60:61], v[60:61], v[148:149]\n"
      "v_pk_add_f32 v[62:63], v[62:63], v[150:151]\n"
      "s_waitcnt lgkmcnt(13)\n"
      "v_pk_add_f32 v[76:77], v[76:77], v[152:153]\n"
      "v_pk_add_f32 v[78:79], v[78:79], v[154:155]\n"
      "s_waitcnt lgkmcnt(12)\n"
      "v_pk_add_f32 v[92:93], v[92:93], v[156:157]\n"
      "v_pk_add_f32 v[94:95], v[94:95], v[158:159]\n"
      "s_waitcnt lgkmcnt(11)\n"
      "v_pk_add_f32 v[108:109], v[108:109], v[160:161]\n"
      "v_pk_add_f32 v[110:111], v[110:111], v[162:163]\n"
      "s_waitcnt lgkmcnt(10)\n"
      "v_pk_add_f32 v[124:125], v[124:125], v[164:165]\n"
      "v_pk_add_f32 v[126:127], v[126:127], v[166:167]\n"
      "s_waitcnt lgkmcnt(9)\n"
      "v_pk_add_f32 v[60:61], v[60:61], v[168:169]\n"
      "v_pk_add_f32 v[62:63], v[62:63], v[170:171]\n"
      "s_waitcnt lgkmcnt(8)\n"
      "v_pk_add_f32 v[76:77], v[76:77], v[172:173]\n"
      "v_pk_add_f32 v[78:79], v[78:79], v[174:175]\n"
      "s_waitcnt lgkmcnt(7)\n"
      "v_pk_add_f32 v[92:93], v[92:93], v[176:177]\n"
      "v_pk_add_f32 v[94:95], v[94:95], v[178:179]\n"
      "s_waitcnt lgkmcnt(6)\n"
      "v_pk_add_f32 v[108:109], v[108:109], v[180:181]\n"
      "v_pk_add_f32 v[110:111], v[110:111], v[182:183]\n"
      "s_waitcnt lgkmcnt(5)\n"
      "v_pk_add_f32 v[124:125], v[124:125], v[184:185]\n"
      "v_pk_add_f32 v[126:127], v[126:127], v[186:187]\n"
      "s_waitcnt lgkmcnt(4)\n"
      "v_pk_add_f32 v[60:61], v[60:61], v[188:189]\n"
      "v_pk_add_f32 v[62:63], v[62:63], v[190:191]\n"
      "s_waitcnt lgkmcnt(3)\n"
      "v_pk_add_f32 v[76:77], v[76:77], v[192:193]\n"
      "v_pk_add_f32 v[78:79], v[78:79], v[194:195]\n"
      "s_waitcnt lgkmcnt(2)\n"
      "v_pk_add_f32 v[92:93], v[92:93], v[196:197]\n"
      "v_pk_add_f32 v[94:95], v[94:95], v[198:199]\n"
      "s_waitcnt lgkmcnt(1)\n"
      "v_pk_add_f32 v[108:109], v[108:109], v[200:201]\n"
      "v_pk_add_f32 v[110:111], v[110:111], v[202:203]\n"
      "s_waitcnt lgkmcnt(0)\n"
      "v_pk_add_f32 v[124:125], v[124:125], v[204:205]\n"
      "v_pk_add_f32 v[126:127], v[126:127], v[206:207]\n"
      "v_pk_fma_f32 v[60:61], v[60:61], v[22:23], v[48:49] op_sel:[0,1,0] op_sel_hi:[1,1,1]\n"
      "v_pk_fma_f32 v[62:63], v[62:63], v[22:23], v[50:51] op_sel:[0,1,0] op_sel_hi:[1,1,1]\n"
      "v_cvt_pk_bf16_f32 v128, v60, v61\n"
      "v_cvt_pk_bf16_f32 v129, v62, v63\n"
      "v_pk_fma_f32 v[76:77], v[76:77], v[22:23], v[64:65] op_sel:[0,1,0] op_sel_hi:[1,1,1]\n"
      "v_pk_fma_f32 v[78:79], v[78:79], v[22:23], v[66:67] op_sel:[0,1,0] op_sel_hi:[1,1,1]\n"
      "v_cvt_pk_bf16_f32 v130, v76, v77\n"
      "v_cvt_pk_bf16_f32 v131, v78, v79\n"
      "v_pk_fma_f32 v[92:93], v[92:93], v[22:23], v[80:81] op_sel:[0,1,0] op_sel_hi:[1,1,1]\n"
      "v_pk_fma_f32 v[94:95], v[94:95], v[22:23], v[82:83] op_sel:[0,1,0] op_sel_hi:[1,1,1]\n"
      "v_cvt_pk_bf16_f32 v132, v92, v93\n"
      "v_cvt_pk_bf16_f32 v133, v94, v95\n"
      "v_permlane16_swap_b32 v128, v130\n"
      "v_permlane16_swap_b32 v129, v131\n"
      "global_store_dwordx4 v46, v[128:131], s[38:39] offset:0\n"
      "v_pk_fma_f32 v[108:109], v[108:109], v[22:23], v[96:97] op_sel:[0,1,0] op_sel_hi:[1,1,1]\n"
      "v_pk_fma_f32 v[110:111], v[110:111], v[22:23], v[98:99] op_sel:[0,1,0] op_sel_hi:[1,1,1]\n"
      "v_cvt_pk_bf16_f32 v134, v108, v109\n"
      "v_cvt_pk_bf16_f32 v135, v110, v111\n"
      "v_pk_fma_f32 v[124:125], v[124:125], v[22:23], v[112:113] op_sel:[0,1,0] op_sel_hi:[1,1,1]\n"
      "v_pk_fma_f32 v[126:127], v[126:127], v[22:23], v[114:115] op_sel:[0,1,0] op_sel_hi:[1,1,1]\n"
      "v_cvt_pk_bf16_f32 v136, v124, v125\n"
      "v_cvt_pk_bf16_f32 v137, v126, v127\n"
      "global_store_dwordx2 v47, v[136:137], s[38:39] offset:128\n"
      "v_permlane16_swap_b32 v132, v134\n"
      "v_permlane16_swap_b32 v133, v135\n"
      "global_store_dwordx4 v46, v[132:135], s[38:39] offset:64\n"
      "s_or_b32 s68, s42, s43\n"
      "s_cmp_lg_u32 s68, 0\n"
      "s_cbranch_scc1 .Lend_37\n"
      "v_cmp_eq_u64 vcc, s[40:41], v[12:13]\n"
      "v_cmp_eq_u64 s[74:75], s[40:41], v[14:15]\n"
      "v_cmp_eq_u64 s[76:77], s[40:41], v[16:17]\n"
      "s_and_b64 s[74:75], s[74:75], vcc\n"
      "v_cmp_eq_u64 vcc, s[40:41], v[18:19]\n"
      "s_and_b64 s[74:75], s[74:75], s[76:77]\n"
      "s_and_b64 vcc, vcc, s[74:75]\n"
      "s_cmp_eq_u64 vcc, -1\n"
      "s_cbranch_scc1 .Lgo_38\n"
      "v_lshlrev_b32 v38, 7, v1\n"
      "v_add_u32 v38, 65536, v38\n"
      "global_load_dwordx2 v[12:13], v38, s[16:17] sc1\n"
      "s_waitcnt vmcnt(0)\n"
      "v_cmp_eq_u64 vcc, s[40:41], v[12:13]\n"
      "s_nop 0\n"
      "s_cmp_eq_u64 vcc, -1\n"
      "s_cbranch_scc1 .Lok_39\n"
      "s_mov_b32 s82, 0x20000\n"
      ".Lsp_40:\n"
      "s_sleep 4\n"
      "global_load_dwordx2 v[12:13], v38, s[16:17] sc1\n"
      "s_waitcnt vmcnt(0)\n"
      "v_cmp_eq_u64 vcc, s[40:41], v[12:13]\n"
      "s_nop 0\n"
      "s_cmp_eq_u64 vcc, -1\n"
      "s_cbranch_scc1 .Lok_39\n"
      "s_sub_u32 s82, s82, 1\n"
      "s_cmp_lg_u32 s82, 0\n"
      "s_cbranch_scc1 .Lsp_40\n"
      ".Lok_39:\n"
      "v_lshlrev_b32 v38, 7, v1\n"
      "v_add_u32 v38, 73728, v38\n"
      "global_load_dwordx2 v[12:13], v38, s[16:17] sc1\n"
      "s_waitcnt vmcnt(0)\n"
      "v_cmp_eq_u64 vcc, s[40:41], v[12:13]\n"
      "s_nop 0\n"
      "s_cmp_eq_u64 vcc, -1\n"
      "s_cbranch_scc1 .Lok_41\n"
      "s_mov_b32 s82, 0x20000\n"
      ".Lsp_42:\n"
      "s_sleep 4\n"
      "global_load_dwordx2 v[12:13], v38, s[16:17] sc1\n"
      "s_waitcnt vmcnt(0)\n"
      "v_cmp_eq_u64 vcc, s[40:41], v[12:13]\n"
      "s_nop 0\n"
      "s_cmp_eq_u64 vcc, -1\n"
      "s_cbranch_scc1 .Lok_41\n"
      "s_sub_u32 s82, s82, 1\n"
      "s_cmp_lg_u32 s82, 0\n"
      "s_cbranch_scc1 .Lsp_42\n"
      ".Lok_41:\n"
      "v_lshlrev_b32 v38, 7, v1\n"
      "v_add_u32 v38, 81920, v38\n"
      "global_load_dwordx2 v[12:13], v38, s[16:17] sc1\n"
      "s_waitcnt vmcnt(0)\n"
      "v_cmp_eq_u64 vcc, s[40:41], v[12:13]\n"
      "s_nop 0\n"
      "s_cmp_eq_u64 vcc, -1\n"
      "s_cbranch_scc1 .Lok_43\n"
      "s_mov_b32 s82, 0x20000\n"
      ".Lsp_44:\n"
      "s_sleep 4\n"
      "global_load_dwordx2 v[12:13], v38, s[16:17] sc1\n"
      "s_waitcnt vmcnt(0)\n"
      "v_cmp_eq_u64 vcc, s[40:41], v[12:13]\n"
      "s_nop 0\n"
      "s_cmp_eq_u64 vcc, -1\n"
      "s_cbranch_scc1 .Lok_43\n"
      "s_sub_u32 s82, s82, 1\n"
      "s_cmp_lg_u32 s82, 0\n"
      "s_cbranch_scc1 .Lsp_44\n"
      ".Lok_43:\n"
      "v_lshlrev_b32 v38, 7, v1\n"
      "v_add_u32 v38, 90112, v38\n"
      "global_load_dwordx2 v[12:13], v38, s[16:17] sc1\n"
      "s_waitcnt vmcnt(0)\n"
      "v_cmp_eq_u64 vcc, s[40:41], v[12:13]\n"
      "s_nop 0\n"
      "s_cmp_eq_u64 vcc, -1\n"
      "s_cbranch_scc1 .Lok_45\n"
      "s_mov_b32 s82, 0x20000\n"
      ".Lsp_46:\n"
      "s_sleep 4\n"
      "global_load_dwordx2 v[12:13], v38, s[16:17] sc1\n"
      "s_waitcnt vmcnt(0)\n"
      "v_cmp_eq_u64 vcc, s[40:41], v[12:13]\n"
      "s_nop 0\n"
      "s_cmp_eq_u64 vcc, -1\n"
      "s_cbranch_scc1 .Lok_45\n"
      "s_sub_u32 s82, s82, 1\n"
      "s_cmp_lg_u32 s82, 0\n"
      "s_cbranch_scc1 .Lsp_46\n"
      ".Lok_45:\n"
      ".Lgo_38:\n"
      "s_lshl_b32 s98, s42, 7\n"
      "s_add_u32 s98, s98, 98304\n"
      "v_mov_b32 v14, s98\n"
      "v_mov_b32 v16, s40\n"
      "v_mov_b32 v17, s41\n"
      "global_store_dwordx2 v14, v[16:17], s[16:17] sc1\n"
      ".Lend_37:\n"
      "s_endpgm\n"
      ".Lrole_s:\n"
      "s_lshr_b32 s68, s45, 2\n"
      "s_and_b32 s46, s44, 6\n"
      "s_lshl_b32 s46, s46, 2\n"
      "s_add_u32 s46, s46, s68\n"
      "s_and_b32 s47, s44, 1\n"
      "s_lshl_b32 s47, s47, 2\n"
      "s_and_b32 s68, s45, 3\n"
      "s_add_u32 s47, s47, s68\n"
      "s_mul_i32 s63, s44, 80\n"
      "s_add_u32 s63, s63, s51\n"
      "v_add_u32 v3, 4096, v2\n"
      "s_sub_u32 s68, s45, 28\n"
      "s_mul_i32 s68, s68, 6\n"
      "s_add_u32 s63, s63, s68\n"
      "s_add_u32 s63, s63, 56\n"
      "s_waitcnt lgkmcnt(0)\n"
      "s_lshl_b32 s98, s63, 8\n"
      "s_lshl_b32 s99, s50, 7\n"
      "s_add_u32 s98, s98, s99\n"
      "v_lshl_add_u32 v46, v1, 1, s98\n"
      "s_lshl_b32 s99, s63, 1\n"
      "v_mov_b32 v47, s99\n"
      "global_load_ushort v6, v46, s[20:21] nt\n"
      "global_load_ushort v7, v46, s[22:23] nt\n"
      "global_load_ushort v8, v46, s[24:25] nt\n"
      "global_load_ushort v9, v46, s[26:27] nt\n"
      "global_load_ushort v10, v47, s[28:29]\n"
      "global_load_ushort v11, v47, s[30:31]\n"
      "s_add_u32 s71, s63, 2\n"
      "s_lshl_b32 s98, s71, 8\n"
      "s_lshl_b32 s99, s50, 7\n"
      "s_add_u32 s98, s98, s99\n"
      "v_lshl_add_u32 v46, v1, 1, s98\n"
      "s_lshl_b32 s99, s71, 1\n"
      "v_mov_b32 v47, s99\n"
      "global_load_ushort v240, v46, s[20:21] nt\n"
      "global_load_ushort v241, v46, s[22:23] nt\n"
      "global_load_ushort v242, v46, s[24:25] nt\n"
      "global_load_ushort v243, v46, s[26:27] nt\n"
      "global_load_ushort v244, v47, s[28:29]\n"
      "global_load_ushort v245, v47, s[30:31]\n"
      "s_add_u32 s71, s63, 4\n"
      "s_lshl_b32 s98, s71, 8\n"
      "s_lshl_b32 s99, s50, 7\n"
      "s_add_u32 s98, s98, s99\n"
      "v_lshl_add_u32 v46, v1, 1, s98\n"
      "s_lshl_b32 s99, s71, 1\n"
      "v_mov_b32 v47, s99\n"
      "global_load_ushort v246, v46, s[20:21] nt\n"
      "global_load_ushort v247, v46, s[22:23] nt\n"
      "global_load_ushort v248, v46, s[24:25] nt\n"
      "global_load_ushort v249, v46, s[26:27] nt\n"
      "global_load_ushort v250, v47, s[28:29]\n"
      "global_load_ushort v251, v47, s[30:31]\n"
      "s_load_dwordx2 s[40:41], s[16:17], 98304\n"
      "s_and_b32 s98, s45, 3\n"
      "s_lshl_b32 s98, s98, 2\n"
      "s_add_u32 s59, s98, s43\n"
      "s_and_b32 s98, s44, 1\n"
      "s_mul_i32 s49, s98, 7\n"
      "s_bfe_u32 s99, s44, 0x10001\n"
      "s_and_b32 s58, s98, s99\n"
      "s_mul_i32 s56, s59, 13824\n"
      "s_lshl_b32 s58, s58, 7\n"
      "s_sub_u32 s56, s56, s58\n"
      "v_mul_u32_u24 v38, 3456, v5\n"
      "v_lshl_add_u32 v38, v4, 4, v38\n"
      "s_lshr_b32 s98, s59, 2\n"
      "s_lshl_b32 s99, s98, 1\n"
      "s_add_u32 s99, s99, s49\n"
      "s_bfe_u32 s52, s59, 0x10001\n"
      "s_add_u32 s99, s99, s52\n"
      "s_add_u32 s99, s99, 8\n"
      "s_sub_u32 s52, s99, 10\n"
      "s_min_u32 s99, s99, s52\n"
      "s_sub_u32 s52, s99, 10\n"
      "s_min_u32 s99, s99, s52\n"
      "s_and_b32 s52, s44, 6\n"
      "s_lshl_b32 s52, s52, 1\n"
      "s_add_u32 s98, s98, s52\n"
      "s_lshl_b32 s98, s98, 1\n"
      "s_and_b32 s52, s59, 1\n"
      "s_add_u32 s98, s98, s52\n"
      "s_mul_i32 s55, s98, 81920\n"
      "s_lshl_b32 s99, s99, 7\n"
      "s_add_u32 s55, s55, s99\n"
      "v_mov_b32 v36, v56\n"
      "s_add_u32 s98, s49, 8\n"
      "s_sub_u32 s99, s98, 10\n"
      "s_min_u32 s98, s98, s99\n"
      "s_sub_u32 s99, s98, 10\n"
      "s_min_u32 s98, s98, s99\n"
      "s_mul_i32 s98, s98, 221184\n"
      "s_add_u32 s99, s49, 0\n"
      "s_lshl_b32 s99, s99, 8\n"
      "s_add_u32 s98, s98, s99\n"
      "s_add_u32 s98, s98, s56\n"
      "buffer_load_dwordx4 a[0:3], v38, s[8:11], s98 offen\n"
      "buffer_load_dwordx4 a[64:67], v36, s[4:7], s55 offen\n"
      "s_add_u32 s55, s55, 10240\n"
      "s_add_u32 s98, s49, 9\n"
      "s_sub_u32 s99, s98, 10\n"
      "s_min_u32 s98, s98, s99\n"
      "s_sub_u32 s99, s98, 10\n"
      "s_min_u32 s98, s98, s99\n"
      "s_mul_i32 s98, s98, 221184\n"
      "s_add_u32 s99, s49, 0\n"
      "s_lshl_b32 s99, s99, 8\n"
      "s_add_u32 s98, s98, s99\n"
      "s_add_u32 s98, s98, s56\n"
      "buffer_load_dwordx4 a[4:7], v38, s[8:11], s98 offen\n"
      "buffer_load_dwordx4 a[68:71], v36, s[4:7], s55 offen\n"
      "s_add_u32 s55, s55, 10240\n"
      "s_add_u32 s98, s49, 9\n"
      "s_sub_u32 s99, s98, 10\n"
      "s_min_u32 s98, s98, s99\n"
      "s_sub_u32 s99, s98, 10\n"
      "s_min_u32 s98, s98, s99\n"
      "s_mul_i32 s98, s98, 221184\n"
      "s_add_u32 s99, s49, 1\n"
      "s_lshl_b32 s99, s99, 8\n"
      "s_add_u32 s98, s98, s99\n"
      "s_add_u32 s98, s98, s56\n"
      "buffer_load_dwordx4 a[8:11], v38, s[8:11], s98 offen\n"
      "buffer_load_dwordx4 a[72:75], v36, s[4:7], s55 offen\n"
      "s_add_u32 s55, s55, 10240\n"
      "s_add_u32 s98, s49, 10\n"
      "s_sub_u32 s99, s98, 10\n"
      "s_min_u32 s98, s98, s99\n"
      "s_sub_u32 s99, s98, 10\n"
      "s_min_u32 s98, s98, s99\n"
      "s_mul_i32 s98, s98, 221184\n"
      "s_add_u32 s99, s49, 1\n"
      "s_lshl_b32 s99, s99, 8\n"
      "s_add_u32 s98, s98, s99\n"
      "s_add_u32 s98, s98, s56\n"
      "buffer_load_dwordx4 a[12:15], v38, s[8:11], s98 offen\n"
      "buffer_load_dwordx4 a[76:79], v36, s[4:7], s55 offen\n"
      "s_add_u32 s55, s55, 10240\n"
      "s_add_u32 s98, s49, 10\n"
      "s_sub_u32 s99, s98, 10\n"
      "s_min_u32 s98, s98, s99\n"
      "s_sub_u32 s99, s98, 10\n"
      "s_min_u32 s98, s98, s99\n"
      "s_mul_i32 s98, s98, 221184\n"
      "s_add_u32 s99, s49, 2\n"
      "s_lshl_b32 s99, s99, 8\n"
      "s_add_u32 s98, s98, s99\n"
      "s_add_u32 s98, s98, s56\n"
      "buffer_load_dwordx4 a[16:19], v38, s[8:11], s98 offen\n"
      "s_mul_i32 s68, s43, 17408\n"
      "s_add_u32 s68, s68, 65536\n"
      "v_mul_u32_u24 v40, 272, v1\n"
      "v_add_u32 v22, s68, v40\n"
      "v_add_u32 v28, s68, v2\n"
      "v_lshrrev_b32 v40, 1, v5\n"
      "v_and_b32 v41, 1, v5\n"
      "v_lshl_or_b32 v40, v40, 1, v41\n"
      "v_bfe_u32 v41, v1, 3, 1\n"
      "v_lshl_or_b32 v40, v40, 1, v41\n"
      "v_mul_u32_u24 v40, 544, v40\n"
      "v_bfe_u32 v41, v1, 2, 1\n"
      "v_lshl_add_u32 v40, v41, 7, v40\n"
      "v_and_b32 v41, 3, v1\n"
      "v_lshl_add_u32 v40, v41, 3, v40\n"
      "v_add_u32 v23, s68, v40\n"
      "v_lshlrev_b32 v40, 4, v4\n"
      "v_lshl_add_u32 v41, v5, 8, v40\n"
      "v_sub_u32 v41, 3568, v41\n"
      "v_lshl_add_u32 v42, v5, 9, v40\n"
      "v_sub_u32 v42, 3824, v42\n"
      "v_cmp_gt_u32 vcc, 2, v5\n"
      "s_nop 1\n"
      "v_cndmask_b32 v41, v42, v41, vcc\n"
      "v_lshl_add_u32 v42, v5, 10, v40\n"
      "v_sub_u32 v42, 1264, v42\n"
      "s_cmp_lg_u32 s50, 0\n"
      "s_cselect_b64 vcc, -1, 0\n"
      "v_cndmask_b32 v24, v42, v41, vcc\n"
      "v_lshrrev_b32 v41, 1, v5\n"
      "v_lshl_add_u32 v41, v41, 8, v40\n"
      "v_and_b32 v42, 1, v5\n"
      "v_lshl_add_u32 v41, v42, 3, v41\n"
      "v_sub_u32 v25, 4088, v41\n"
      "v_lshrrev_b32 v43, 1, v1\n"
      "v_min_u32 v43, 1, v43\n"
      "s_mul_i32 s65, s51, 4608\n"
      "s_add_u32 s65, s65, 135168\n"
      "s_mov_b32 s66, 0\n"
      "v_lshl_add_u32 v37, v43, 27, v3\n"
      "s_waitcnt lgkmcnt(0)\n"
      "s_add_u32 s40, s40, 1\n"
      "s_addc_u32 s41, s41, 0\n"
      "s_mul_i32 s71, s44, 4\n"
      "s_add_u32 s71, s71, s45\n"
      "s_sub_u32 s71, s71, 28\n"
      "s_mul_i32 s71, s71, 27648\n"
      "s_lshl_b32 s72, s43, 10\n"
      "s_add_u32 s71, s71, s72\n"
      "s_add_u32 s53, s71, 16908288\n"
      "s_add_u32 s72, s72, 135168\n"
      "v_add_u32 v31, s72, v2\n"
      "s_waitcnt vmcnt(22)\n"
      "v_lshlrev_b32 v98, 16, v10\n"
      "v_mul_f32 v99, 0x3fb8aa00, v98\n"
      "v_mul_f32 v100, 0x36eca570, v98\n"
      "v_rndne_f32 v101, v99\n"
      "v_sub_f32 v99, v99, v101\n"
      "v_add_f32 v99, v99, v100\n"
      "v_exp_f32 v99, v99\n"
      "v_cvt_i32_f32 v101, v101\n"
      "v_ldexp_f32 v99, v99, v101\n"
      "v_cvt_pk_bf16_f32 v99, v99, v99\n"
      "v_lshlrev_b32 v102, 16, v99\n"
      "v_lshlrev_b32 v98, 16, v6\n"
      "v_mul_f32 v99, 0x3fb8aa00, v98\n"
      "v_mul_f32 v100, 0x36eca570, v98\n"
      "v_rndne_f32 v101, v99\n"
      "v_sub_f32 v99, v99, v101\n"
      "v_add_f32 v99, v99, v100\n"
      "v_exp_f32 v99, v99\n"
      "v_cvt_i32_f32 v101, v101\n"
      "v_ldexp_f32 v99, v99, v101\n"
      "v_cvt_pk_bf16_f32 v99, v99, v99\n"
      "v_lshlrev_b32 v103, 16, v99\n"
      "v_mul_f32 v104, v103, v102\n"
      "v_mul_f32 v104, 0xbfb8aa3b, v104\n"
      "v_lshlrev_b32 v105, 16, v7\n"
      "v_mul_f32 v105, v105, v102\n"
      "v_mul_f32 v105, 0x3e22f983, v105\n"
      "v_mul_f32 v98, 0x41000000, v104\n"
      "v_mul_f32 v99, 0x41000000, v105\n"
      "v_fract_f32 v100, v99\n"
      "v_exp_f32 v98, v98\n"
      "v_cos_f32 v124, v100\n"
      "v_sin_f32 v125, v100\n"
      "v_mul_f32 v124, v98, v124\n"
      "v_mul_f32 v125, v98, v125\n"
      "v_mov_b32 v122, 1.0\n"
      "v_mov_b32 v123, 0\n"
      "v_mov_b64 v[112:113], v[124:125]\n"
      "v_pk_mul_f32 v[116:117], v[124:125], v[112:113] op_sel:[0,0] op_sel_hi:[0,1]\n"
      "v_pk_fma_f32 v[126:127], v[124:125], v[112:113], v[116:117] op_sel:[1,1,0] op_sel_hi:[1,0,1] neg_lo:[1,0,0]\n"
      "v_pk_mul_f32 v[116:117], v[126:127], v[112:113] op_sel:[0,0] op_sel_hi:[0,1]\n"
      "v_pk_fma_f32 v[128:129], v[126:127], v[112:113], v[116:117] op_sel:[1,1,0] op_sel_hi:[1,0,1] neg_lo:[1,0,0]\n"
      "v_pk_mul_f32 v[116:117], v[128:129], v[112:113] op_sel:[0,0] op_sel_hi:[0,1]\n"
      "v_pk_fma_f32 v[130:131], v[128:129], v[112:113], v[116:117] op_sel:[1,1,0] op_sel_hi:[1,0,1] neg_lo:[1,0,0]\n"
      "v_pk_mul_f32 v[116:117], v[130:131], v[112:113] op_sel:[0,0] op_sel_hi:[0,1]\n"
      "v_pk_fma_f32 v[132:133], v[130:131], v[112:113], v[116:117] op_sel:[1,1,0] op_sel_hi:[1,0,1] neg_lo:[1,0,0]\n"
      "v_pk_mul_f32 v[116:117], v[132:133], v[112:113] op_sel:[0,0] op_sel_hi:[0,1]\n"
      "v_pk_fma_f32 v[134:135], v[132:133], v[112:113], v[116:117] op_sel:[1,1,0] op_sel_hi:[1,0,1] neg_lo:[1,0,0]\n"
      "v_pk_mul_f32 v[116:117], v[134:135], v[112:113] op_sel:[0,0] op_sel_hi:[0,1]\n"
      "v_pk_fma_f32 v[136:137], v[134:135], v[112:113], v[116:117] op_sel:[1,1,0] op_sel_hi:[1,0,1] neg_lo:[1,0,0]\n"
      "v_pk_mul_f32 v[116:117], v[136:137], v[112:113] op_sel:[0,0] op_sel_hi:[0,1]\n"
      "v_pk_fma_f32 v[138:139], v[136:137], v[112:113], v[116:117] op_sel:[1,1,0] op_sel_hi:[1,0,1] neg_lo:[1,0,0]\n"
      "v_cvt_pk_bf16_f32 v40, v122, v124\n"
      "v_cvt_pk_bf16_f32 v41, v126, v128\n"
      "v_cvt_pk_bf16_f32 v42, v130, v132\n"
      "v_cvt_pk_bf16_f32 v43, v134, v136\n"
      "v_cvt_pk_bf16_f32 v44, -v123, -v125\n"
      "v_cvt_pk_bf16_f32 v45, -v127, -v129\n"
      "v_cvt_pk_bf16_f32 v46, -v131, -v133\n"
      "v_cvt_pk_bf16_f32 v47, -v135, -v137\n"
      "v_pk_mul_f32 v[116:117], v[138:139], v[112:113] op_sel:[0,0] op_sel_hi:[0,1]\n"
      "v_pk_fma_f32 v[140:141], v[138:139], v[112:113], v[116:117] op_sel:[1,1,0] op_sel_hi:[1,0,1] neg_lo:[1,0,0]\n"
      "v_pk_mul_f32 v[116:117], v[140:141], v[112:113] op_sel:[0,0] op_sel_hi:[0,1]\n"
      "v_pk_fma_f32 v[142:143], v[140:141], v[112:113], v[116:117] op_sel:[1,1,0] op_sel_hi:[1,0,1] neg_lo:[1,0,0]\n"
      "v_pk_mul_f32 v[116:117], v[142:143], v[112:113] op_sel:[0,0] op_sel_hi:[0,1]\n"
      "v_pk_fma_f32 v[144:145], v[142:143], v[112:113], v[116:117] op_sel:[1,1,0] op_sel_hi:[1,0,1] neg_lo:[1,0,0]\n"
      "v_pk_mul_f32 v[116:117], v[144:145], v[112:113] op_sel:[0,0] op_sel_hi:[0,1]\n"
      "v_pk_fma_f32 v[146:147], v[144:145], v[112:113], v[116:117] op_sel:[1,1,0] op_sel_hi:[1,0,1] neg_lo:[1,0,0]\n"
      "v_pk_mul_f32 v[116:117], v[146:147], v[112:113] op_sel:[0,0] op_sel_hi:[0,1]\n"
      "v_pk_fma_f32 v[148:149], v[146:147], v[112:113], v[116:117] op_sel:[1,1,0] op_sel_hi:[1,0,1] neg_lo:[1,0,0]\n"
      "v_pk_mul_f32 v[116:117], v[148:149], v[112:113] op_sel:[0,0] op_sel_hi:[0,1]\n"
      "v_pk_fma_f32 v[150:151], v[148:149], v[112:113], v[116:117] op_sel:[1,1,0] op_sel_hi:[1,0,1] neg_lo:[1,0,0]\n"
      "v_pk_mul_f32 v[116:117], v[150:151], v[112:113] op_sel:[0,0] op_sel_hi:[0,1]\n"
      "v_pk_fma_f32 v[152:153], v[150:151], v[112:113], v[116:117] op_sel:[1,1,0] op_sel_hi:[1,0,1] neg_lo:[1,0,0]\n"
      "v_fract_f32 v100, v105\n"
      "v_exp_f32 v98, v104\n"
      "v_cos_f32 v106, v100\n"
      "v_sin_f32 v107, v100\n"
      "v_mul_f32 v106, v98, v106\n"
      "v_mul_f32 v107, v98, v107\n"
      "v_cvt_pk_bf16_f32 v48, v138, v140\n"
      "v_cvt_pk_bf16_f32 v49, v142, v144\n"
      "v_cvt_pk_bf16_f32 v50, v146, v148\n"
      "v_cvt_pk_bf16_f32 v51, v150, v152\n"
      "v_cvt_pk_bf16_f32 v52, -v139, -v141\n"
      "v_cvt_pk_bf16_f32 v53, -v143, -v145\n"
      "v_cvt_pk_bf16_f32 v54, -v147, -v149\n"
      "v_cvt_pk_bf16_f32 v55, -v151, -v153\n"
      "v_mul_f32 v98, 0x43000000, v104\n"
      "v_mul_f32 v99, 0x43000000, v105\n"
      "v_fract_f32 v100, v99\n"
      "v_exp_f32 v98, v98\n"
      "v_cos_f32 v114, v100\n"
      "v_sin_f32 v115, v100\n"
      "v_mul_f32 v114, v98, v114\n"
      "v_mul_f32 v115, v98, v115\n"
      "v_add_f32 v102, v102, v102\n"
      "v_lshlrev_b32 v122, 16, v8\n"
      "v_lshlrev_b32 v123, 16, v9\n"
      "v_mul_f32 v122, v122, v102\n"
      "v_mul_f32 v123, v123, v102\n"
      "s_waitcnt vmcnt(21)\n"
      "v_lshlrev_b32 v97, 16, v11\n"
      "v_pk_mul_f32 v[116:117], v[122:123], v[114:115] op_sel:[0,0] op_sel_hi:[0,1]\n"
      "v_pk_fma_f32 v[138:139], v[122:123], v[114:115], v[116:117] op_sel:[1,1,0] op_sel_hi:[1,0,1] neg_lo:[1,0,0]\n"
      "v_pk_mul_f32 v[116:117], v[122:123], v[106:107] op_sel:[0,0] op_sel_hi:[0,1]\n"
      "v_pk_fma_f32 v[124:125], v[122:123], v[106:107], v[116:117] op_sel:[1,1,0] op_sel_hi:[1,0,1] neg_lo:[1,0,0]\n"
      "v_pk_mul_f32 v[116:117], v[124:125], v[106:107] op_sel:[0,0] op_sel_hi:[0,1]\n"
      "v_pk_fma_f32 v[126:127], v[124:125], v[106:107], v[116:117] op_sel:[1,1,0] op_sel_hi:[1,0,1] neg_lo:[1,0,0]\n"
      "v_pk_mul_f32 v[116:117], v[126:127], v[106:107] op_sel:[0,0] op_sel_hi:[0,1]\n"
      "v_pk_fma_f32 v[128:129], v[126:127], v[106:107], v[116:117] op_sel:[1,1,0] op_sel_hi:[1,0,1] neg_lo:[1,0,0]\n"
      "v_pk_mul_f32 v[116:117], v[128:129], v[106:107] op_sel:[0,0] op_sel_hi:[0,1]\n"
      "v_pk_fma_f32 v[130:131], v[128:129], v[106:107], v[116:117] op_sel:[1,1,0] op_sel_hi:[1,0,1] neg_lo:[1,0,0]\n"
      "v_pk_mul_f32 v[116:117], v[130:131], v[106:107] op_sel:[0,0] op_sel_hi:[0,1]\n"
      "v_pk_fma_f32 v[132:133], v[130:131], v[106:107], v[116:117] op_sel:[1,1,0] op_sel_hi:[1,0,1] neg_lo:[1,0,0]\n"
      "v_pk_mul_f32 v[116:117], v[132:133], v[106:107] op_sel:[0,0] op_sel_hi:[0,1]\n"
      "v_pk_fma_f32 v[134:135], v[132:133], v[106:107], v[116:117] op_sel:[1,1,0] op_sel_hi:[1,0,1] neg_lo:[1,0,0]\n"
      "v_pk_mul_f32 v[116:117], v[134:135], v[106:107] op_sel:[0,0] op_sel_hi:[0,1]\n"
      "v_pk_fma_f32 v[136:137], v[134:135], v[106:107], v[116:117] op_sel:[1,1,0] op_sel_hi:[1,0,1] neg_lo:[1,0,0]\n"
      "v_cvt_pk_bf16_f32 v56, v122, v124\n"
      "v_cvt_pk_bf16_f32 v57, v126, v128\n"
      "v_cvt_pk_bf16_f32 v58, v130, v132\n"
      "v_cvt_pk_bf16_f32 v59, v134, v136\n"
      "v_cvt_pk_bf16_f32 v60, v123, v125\n"
      "v_cvt_pk_bf16_f32 v61, v127, v129\n"
      "v_cvt_pk_bf16_f32 v62, v131, v133\n"
      "v_cvt_pk_bf16_f32 v63, v135, v137\n"
      "v_pk_mul_f32 v[116:117], v[138:139], v[114:115] op_sel:[0,0] op_sel_hi:[0,1]\n"
      "v_pk_fma_f32 v[122:123], v[138:139], v[114:115], v[116:117] op_sel:[1,1,0] op_sel_hi:[1,0,1] neg_lo:[1,0,0]\n"
      "v_pk_mul_f32 v[116:117], v[138:139], v[106:107] op_sel:[0,0] op_sel_hi:[0,1]\n"
      "v_pk_fma_f32 v[140:141], v[138:139], v[106:107], v[116:117] op_sel:[1,1,0] op_sel_hi:[1,0,1] neg_lo:[1,0,0]\n"
      "v_pk_mul_f32 v[116:117], v[140:141], v[106:107] op_sel:[0,0] op_sel_hi:[0,1]\n"
      "v_pk_fma_f32 v[142:143], v[140:141], v[106:107], v[116:117] op_sel:[1,1,0] op_sel_hi:[1,0,1] neg_lo:[1,0,0]\n"
      "v_pk_mul_f32 v[116:117], v[142:143], v[106:107] op_sel:[0,0] op_sel_hi:[0,1]\n"
      "v_pk_fma_f32 v[144:145], v[142:143], v[106:107], v[116:117] op_sel:[1,1,0] op_sel_hi:[1,0,1] neg_lo:[1,0,0]\n"
      "v_pk_mul_f32 v[116:117], v[144:145], v[106:107] op_sel:[0,0] op_sel_hi:[0,1]\n"
      "v_pk_fma_f32 v[146:147], v[144:145], v[106:107], v[116:117] op_sel:[1,1,0] op_sel_hi:[1,0,1] neg_lo:[1,0,0]\n"
      "v_pk_mul_f32 v[116:117], v[146:147], v[106:107] op_sel:[0,0] op_sel_hi:[0,1]\n"
      "v_pk_fma_f32 v[148:149], v[146:147], v[106:107], v[116:117] op_sel:[1,1,0] op_sel_hi:[1,0,1] neg_lo:[1,0,0]\n"
      "v_pk_mul_f32 v[116:117], v[148:149], v[106:107] op_sel:[0,0] op_sel_hi:[0,1]\n"
      "v_pk_fma_f32 v[150:151], v[148:149], v[106:107], v[116:117] op_sel:[1,1,0] op_sel_hi:[1,0,1] neg_lo:[1,0,0]\n"
      "v_pk_mul_f32 v[116:117], v[150:151], v[106:107] op_sel:[0,0] op_sel_hi:[0,1]\n"
      "v_pk_fma_f32 v[152:153], v[150:151], v[106:107], v[116:117] op_sel:[1,1,0] op_sel_hi:[1,0,1] neg_lo:[1,0,0]\n"
      "v_pk_mul_f32 v[116:117], v[106:107], v[106:107] op_sel:[0,0] op_sel_hi:[0,1]\n"
      "v_pk_fma_f32 v[108:109], v[106:107], v[106:107], v[116:117] op_sel:[1,1,0] op_sel_hi:[1,0,1] neg_lo:[1,0,0]\n"
      "v_cvt_pk_bf16_f32 v64, v138, v140\n"
      "v_cvt_pk_bf16_f32 v65, v142, v144\n"
      "v_cvt_pk_bf16_f32 v66, v146, v148\n"
      "v_cvt_pk_bf16_f32 v67, v150, v152\n"
      "v_cvt_pk_bf16_f32 v68, v139, v141\n"
      "v_cvt_pk_bf16_f32 v69, v143, v145\n"
      "v_cvt_pk_bf16_f32 v70, v147, v149\n"
      "v_cvt_pk_bf16_f32 v71, v151, v153\n"
      "v_pk_mul_f32 v[116:117], v[122:123], v[114:115] op_sel:[0,0] op_sel_hi:[0,1]\n"
      "v_pk_fma_f32 v[130:131], v[122:123], v[114:115], v[116:117] op_sel:[1,1,0] op_sel_hi:[1,0,1] neg_lo:[1,0,0]\n"
      "v_pk_mul_f32 v[116:117], v[130:131], v[114:115] op_sel:[0,0] op_sel_hi:[0,1]\n"
      "v_pk_fma_f32 v[138:139], v[130:131], v[114:115], v[116:117] op_sel:[1,1,0] op_sel_hi:[1,0,1] neg_lo:[1,0,0]\n"
      "v_pk_mul_f32 v[116:117], v[122:123], v[108:109] op_sel:[0,0] op_sel_hi:[0,1]\n"
      "v_pk_fma_f32 v[124:125], v[122:123], v[108:109], v[116:117] op_sel:[1,1,0] op_sel_hi:[1,0,1] neg_lo:[1,0,0]\n"
      "v_pk_mul_f32 v[116:117], v[130:131], v[108:109] op_sel:[0,0] op_sel_hi:[0,1]\n"
      "v_pk_fma_f32 v[132:133], v[130:131], v[108:109], v[116:117] op_sel:[1,1,0] op_sel_hi:[1,0,1] neg_lo:[1,0,0]\n"
      "v_pk_mul_f32 v[116:117], v[124:125], v[108:109] op_sel:[0,0] op_sel_hi:[0,1]\n"
      "v_pk_fma_f32 v[126:127], v[124:125], v[108:109], v[116:117] op_sel:[1,1,0] op_sel_hi:[1,0,1] neg_lo:[1,0,0]\n"
      "v_pk_mul_f32 v[116:117], v[132:133], v[108:109] op_sel:[0,0] op_sel_hi:[0,1]\n"
      "v_pk_fma_f32 v[134:135], v[132:133], v[108:109], v[116:117] op_sel:[1,1,0] op_sel_hi:[1,0,1] neg_lo:[1,0,0]\n"
      "v_pk_mul_f32 v[116:117], v[126:127], v[108:109] op_sel:[0,0] op_sel_hi:[0,1]\n"
      "v_pk_fma_f32 v[128:129], v[126:127], v[108:109], v[116:117] op_sel:[1,1,0] op_sel_hi:[1,0,1] neg_lo:[1,0,0]\n"
      "v_pk_mul_f32 v[116:117], v[134:135], v[108:109] op_sel:[0,0] op_sel_hi:[0,1]\n"
      "v_pk_fma_f32 v[136:137], v[134:135], v[108:109], v[116:117] op_sel:[1,1,0] op_sel_hi:[1,0,1] neg_lo:[1,0,0]\n"
      "v_pk_mul_f32 v[116:117], v[108:109], v[108:109] op_sel:[0,0] op_sel_hi:[0,1]\n"
      "v_pk_fma_f32 v[110:111], v[108:109], v[108:109], v[116:117] op_sel:[1,1,0] op_sel_hi:[1,0,1] neg_lo:[1,0,0]\n"
      "v_cvt_pk_bf16_f32 v72, v122, v124\n"
      "v_cvt_pk_bf16_f32 v73, v126, v128\n"
      "v_cvt_pk_bf16_f32 v74, v130, v132\n"
      "v_cvt_pk_bf16_f32 v75, v134, v136\n"
      "v_cvt_pk_bf16_f32 v76, v123, v125\n"
      "v_cvt_pk_bf16_f32 v77, v127, v129\n"
      "v_cvt_pk_bf16_f32 v78, v131, v133\n"
      "v_cvt_pk_bf16_f32 v79, v135, v137\n"
      "v_pk_mul_f32 v[116:117], v[138:139], v[114:115] op_sel:[0,0] op_sel_hi:[0,1]\n"
      "v_pk_fma_f32 v[142:143], v[138:139], v[114:115], v[116:117] op_sel:[1,1,0] op_sel_hi:[1,0,1] neg_lo:[1,0,0]\n"
      "v_pk_mul_f32 v[116:117], v[142:143], v[114:115] op_sel:[0,0] op_sel_hi:[0,1]\n"
      "v_pk_fma_f32 v[146:147], v[142:143], v[114:115], v[116:117] op_sel:[1,1,0] op_sel_hi:[1,0,1] neg_lo:[1,0,0]\n"
      "v_pk_mul_f32 v[116:117], v[146:147], v[114:115] op_sel:[0,0] op_sel_hi:[0,1]\n"
      "v_pk_fma_f32 v[150:151], v[146:147], v[114:115], v[116:117] op_sel:[1,1,0] op_sel_hi:[1,0,1] neg_lo:[1,0,0]\n"
      "v_pk_mul_f32 v[116:117], v[150:151], v[114:115] op_sel:[0,0] op_sel_hi:[0,1]\n"
      "v_pk_fma_f32 v[122:123], v[150:151], v[114:115], v[116:117] op_sel:[1,1,0] op_sel_hi:[1,0,1] neg_lo:[1,0,0]\n"
      "v_pk_mul_f32 v[116:117], v[138:139], v[110:111] op_sel:[0,0] op_sel_hi:[0,1]\n"
      "v_pk_fma_f32 v[140:141], v[138:139], v[110:111], v[116:117] op_sel:[1,1,0] op_sel_hi:[1,0,1] neg_lo:[1,0,0]\n"
      "v_pk_mul_f32 v[116:117], v[142:143], v[110:111] op_sel:[0,0] op_sel_hi:[0,1]\n"
      "v_pk_fma_f32 v[144:145], v[142:143], v[110:111], v[116:117] op_sel:[1,1,0] op_sel_hi:[1,0,1] neg_lo:[1,0,0]\n"
      "v_pk_mul_f32 v[116:117], v[146:147], v[110:111] op_sel:[0,0] op_sel_hi:[0,1]\n"
      "v_pk_fma_f32 v[148:149], v[146:147], v[110:111], v[116:117] op_sel:[1,1,0] op_sel_hi:[1,0,1] neg_lo:[1,0,0]\n"
      "v_pk_mul_f32 v[116:117], v[150:151], v[110:111] op_sel:[0,0] op_sel_hi:[0,1]\n"
      "v_pk_fma_f32 v[152:153], v[150:151], v[110:111], v[116:117] op_sel:[1,1,0] op_sel_hi:[1,0,1] neg_lo:[1,0,0]\n"
      "v_cvt_pk_bf16_f32 v80, v138, v140\n"
      "v_cvt_pk_bf16_f32 v81, v142, v144\n"
      "v_cvt_pk_bf16_f32 v82, v146, v148\n"
      "v_cvt_pk_bf16_f32 v83, v150, v152\n"
      "v_cvt_pk_bf16_f32 v84, v139, v141\n"
      "v_cvt_pk_bf16_f32 v85, v143, v145\n"
      "v_cvt_pk_bf16_f32 v86, v147, v149\n"
      "v_cvt_pk_bf16_f32 v87, v151, v153\n"
      "v_pk_mul_f32 v[116:117], v[122:123], v[114:115] op_sel:[0,0] op_sel_hi:[0,1]\n"
      "v_pk_fma_f32 v[124:125], v[122:123], v[114:115], v[116:117] op_sel:[1,1,0] op_sel_hi:[1,0,1] neg_lo:[1,0,0]\n"
      "v_pk_mul_f32 v[116:117], v[124:125], v[114:115] op_sel:[0,0] op_sel_hi:[0,1]\n"
      "v_pk_fma_f32 v[126:127], v[124:125], v[114:115], v[116:117] op_sel:[1,1,0] op_sel_hi:[1,0,1] neg_lo:[1,0,0]\n"
      "v_pk_mul_f32 v[116:117], v[126:127], v[114:115] op_sel:[0,0] op_sel_hi:[0,1]\n"
      "v_pk_fma_f32 v[128:129], v[126:127], v[114:115], v[116:117] op_sel:[1,1,0] op_sel_hi:[1,0,1] neg_lo:[1,0,0]\n"
      "v_pk_mul_f32 v[116:117], v[128:129], v[114:115] op_sel:[0,0] op_sel_hi:[0,1]\n"
      "v_pk_fma_f32 v[130:131], v[128:129], v[114:115], v[116:117] op_sel:[1,1,0] op_sel_hi:[1,0,1] neg_lo:[1,0,0]\n"
      "v_pk_mul_f32 v[116:117], v[130:131], v[114:115] op_sel:[0,0] op_sel_hi:[0,1]\n"
      "v_pk_fma_f32 v[132:133], v[130:131], v[114:115], v[116:117] op_sel:[1,1,0] op_sel_hi:[1,0,1] neg_lo:[1,0,0]\n"
      "v_pk_mul_f32 v[116:117], v[132:133], v[114:115] op_sel:[0,0] op_sel_hi:[0,1]\n"
      "v_pk_fma_f32 v[134:135], v[132:133], v[114:115], v[116:117] op_sel:[1,1,0] op_sel_hi:[1,0,1] neg_lo:[1,0,0]\n"
      "v_pk_mul_f32 v[116:117], v[134:135], v[114:115] op_sel:[0,0] op_sel_hi:[0,1]\n"
      "v_pk_fma_f32 v[136:137], v[134:135], v[114:115], v[116:117] op_sel:[1,1,0] op_sel_hi:[1,0,1] neg_lo:[1,0,0]\n"
      "v_pk_mul_f32 v[116:117], v[136:137], v[114:115] op_sel:[0,0] op_sel_hi:[0,1]\n"
      "v_pk_fma_f32 v[138:139], v[136:137], v[114:115], v[116:117] op_sel:[1,1,0] op_sel_hi:[1,0,1] neg_lo:[1,0,0]\n"
      "v_cvt_pk_bf16_f32 v88, v122, v124\n"
      "v_cvt_pk_bf16_f32 v89, v126, v128\n"
      "v_cvt_pk_bf16_f32 v90, v130, v132\n"
      "v_cvt_pk_bf16_f32 v91, v134, v136\n"
      "v_cvt_pk_bf16_f32 v92, v123, v125\n"
      "v_cvt_pk_bf16_f32 v93, v127, v129\n"
      "v_cvt_pk_bf16_f32 v94, v131, v133\n"
      "v_cvt_pk_bf16_f32 v95, v135, v137\n"
      "v_cvt_pk_bf16_f32 v96, v138, v139\n"
      "v_mov_b32 v181, v97\n"
      "ds_write_b128 v22, v[40:43] offset:96\n"
      "s_waitcnt vmcnt(16)\n"
      "v_lshlrev_b32 v98, 16, v244\n"
      "v_mul_f32 v99, 0x3fb8aa00, v98\n"
      "v_mul_f32 v100, 0x36eca570, v98\n"
      "v_rndne_f32 v101, v99\n"
      "v_sub_f32 v99, v99, v101\n"
      "v_add_f32 v99, v99, v100\n"
      "ds_write_b128 v22, v[44:47] offset:224\n"
      "v_exp_f32 v99, v99\n"
      "v_cvt_i32_f32 v101, v101\n"
      "v_ldexp_f32 v99, v99, v101\n"
      "v_cvt_pk_bf16_f32 v99, v99, v99\n"
      "v_lshlrev_b32 v102, 16, v99\n"
      "v_lshlrev_b32 v98, 16, v240\n"
      "ds_write_b128 v22, v[48:51] offset:112\n"
      "v_mul_f32 v99, 0x3fb8aa00, v98\n"
      "v_mul_f32 v100, 0x36eca570, v98\n"
      "v_rndne_f32 v101, v99\n"
      "v_sub_f32 v99, v99, v101\n"
      "v_add_f32 v99, v99, v100\n"
      "v_exp_f32 v99, v99\n"
      "ds_write_b128 v22, v[52:55] offset:240\n"
      "v_cvt_i32_f32 v101, v101\n"
      "v_ldexp_f32 v99, v99, v101\n"
      "v_cvt_pk_bf16_f32 v99, v99, v99\n"
      "v_lshlrev_b32 v103, 16, v99\n"
      "v_mul_f32 v104, v103, v102\n"
      "v_mul_f32 v104, 0xbfb8aa3b, v104\n"
      "ds_write_b128 v22, v[56:59] offset:0\n"
      "v_lshlrev_b32 v105, 16, v241\n"
      "v_mul_f32 v105, v105, v102\n"
      "v_mul_f32 v105, 0x3e22f983, v105\n"
      "v_mul_f32 v98, 0x41000000, v104\n"
      "v_mul_f32 v99, 0x41000000, v105\n"
      "v_fract_f32 v100, v99\n"
      "ds_write_b128 v22, v[60:63] offset:128\n"
      "v_exp_f32 v98, v98\n"
      "v_cos_f32 v124, v100\n"
      "v_sin_f32 v125, v100\n"
      "v_mul_f32 v124, v98, v124\n"
      "v_mul_f32 v125, v98, v125\n"
      "v_mov_b32 v122, 1.0\n"
      "ds_read_b64_tr_b16 v[182:183], v23 offset:96\n"
      "ds_read_b64_tr_b16 v[184:185], v23 offset:368\n"
      "ds_read_b64_tr_b16 v[186:187], v23 offset:4448\n"
      "ds_read_b64_tr_b16 v[188:189], v23 offset:4720\n"
      "ds_read_b64_tr_b16 v[190:191], v23 offset:8800\n"
      "ds_read_b64_tr_b16 v[192:193], v23 offset:9072\n"
      "ds_read_b64_tr_b16 v[194:195], v23 offset:13152\n"
      "ds_read_b64_tr_b16 v[196:197], v23 offset:13424\n"
      "v_mov_b32 v123, 0\n"
      "v_mov_b64 v[112:113], v[124:125]\n"
      "v_pk_mul_f32 v[116:117], v[124:125], v[112:113] op_sel:[0,0] op_sel_hi:[0,1]\n"
      "v_pk_fma_f32 v[126:127], v[124:125], v[112:113], v[116:117] op_sel:[1,1,0] op_sel_hi:[1,0,1] neg_lo:[1,0,0]\n"
      "v_pk_mul_f32 v[116:117], v[126:127], v[112:113] op_sel:[0,0] op_sel_hi:[0,1]\n"
      "v_pk_fma_f32 v[128:129], v[126:127], v[112:113], v[116:117] op_sel:[1,1,0] op_sel_hi:[1,0,1] neg_lo:[1,0,0]\n"
      "ds_write_b128 v22, v[64:67] offset:16\n"
      "v_pk_mul_f32 v[116:117], v[128:129], v[112:113] op_sel:[0,0] op_sel_hi:[0,1]\n"
      "v_pk_fma_f32 v[130:131], v[128:129], v[112:113], v[116:117] op_sel:[1,1,0] op_sel_hi:[1,0,1] neg_lo:[1,0,0]\n"
      "v_pk_mul_f32 v[116:117], v[130:131], v[112:113] op_sel:[0,0] op_sel_hi:[0,1]\n"
      "v_pk_fma_f32 v[132:133], v[130:131], v[112:113], v[116:117] op_sel:[1,1,0] op_sel_hi:[1,0,1] neg_lo:[1,0,0]\n"
      "v_pk_mul_f32 v[116:117], v[132:133], v[112:113] op_sel:[0,0] op_sel_hi:[0,1]\n"
      "v_pk_fma_f32 v[134:135], v[132:133], v[112:113], v[116:117] op_sel:[1,1,0] op_sel_hi:[1,0,1] neg_lo:[1,0,0]\n"
      "ds_write_b128 v22, v[68:71] offset:144\n"
      "v_pk_mul_f32 v[116:117], v[134:135], v[112:113] op_sel:[0,0] op_sel_hi:[0,1]\n"
      "v_pk_fma_f32 v[136:137], v[134:135], v[112:113], v[116:117] op_sel:[1,1,0] op_sel_hi:[1,0,1] neg_lo:[1,0,0]\n"
      "v_pk_mul_f32 v[116:117], v[136:137], v[112:113] op_sel:[0,0] op_sel_hi:[0,1]\n"
      "v_pk_fma_f32 v[138:139], v[136:137], v[112:113], v[116:117] op_sel:[1,1,0] op_sel_hi:[1,0,1] neg_lo:[1,0,0]\n"
      "v_cvt_pk_bf16_f32 v40, v122, v124\n"
      "v_cvt_pk_bf16_f32 v41, v126, v128\n"
      "ds_write_b128 v22, v[72:75] offset:32\n"
      "v_cvt_pk_bf16_f32 v42, v130, v132\n"
      "v_cvt_pk_bf16_f32 v43, v134, v136\n"
      "v_cvt_pk_bf16_f32 v44, -v123, -v125\n"
      "v_cvt_pk_bf16_f32 v45, -v127, -v129\n"
      "v_cvt_pk_bf16_f32 v46, -v131, -v133\n"
      "v_cvt_pk_bf16_f32 v47, -v135, -v137\n"
      "ds_write_b128 v22, v[76:79] offset:160\n"
      "v_pk_mul_f32 v[116:117], v[138:139], v[112:113] op_sel:[0,0] op_sel_hi:[0,1]\n"
      "v_pk_fma_f32 v[140:141], v[138:139], v[112:113], v[116:117] op_sel:[1,1,0] op_sel_hi:[1,0,1] neg_lo:[1,0,0]\n"
      "v_pk_mul_f32 v[116:117], v[140:141], v[112:113] op_sel:[0,0] op_sel_hi:[0,1]\n"
      "v_pk_fma_f32 v[142:143], v[140:141], v[112:113], v[116:117] op_sel:[1,1,0] op_sel_hi:[1,0,1] neg_lo:[1,0,0]\n"
      "v_pk_mul_f32 v[116:117], v[142:143], v[112:113] op_sel:[0,0] op_sel_hi:[0,1]\n"
      "v_pk_fma_f32 v[144:145], v[142:143], v[112:113], v[116:117] op_sel:[1,1,0] op_sel_hi:[1,0,1] neg_lo:[1,0,0]\n"
      "ds_read_b64_tr_b16 v[198:199], v23\n"
      "ds_read_b64_tr_b16 v[200:201], v23 offset:272\n"
      "ds_read_b64_tr_b16 v[202:203], v23 offset:4352\n"
      "ds_read_b64_tr_b16 v[204:205], v23 offset:4624\n"
      "ds_read_b64_tr_b16 v[206:207], v23 offset:8704\n"
      "ds_read_b64_tr_b16 v[208:209], v23 offset:8976\n"
      "ds_read_b64_tr_b16 v[210:211], v23 offset:13056\n"
      "ds_read_b64_tr_b16 v[212:213], v23 offset:13328\n"
      "v_pk_mul_f32 v[116:117], v[144:145], v[112:113] op_sel:[0,0] op_sel_hi:[0,1]\n"
      "v_pk_fma_f32 v[146:147], v[144:145], v[112:113], v[116:117] op_sel:[1,1,0] op_sel_hi:[1,0,1] neg_lo:[1,0,0]\n"
      "v_pk_mul_f32 v[116:117], v[146:147], v[112:113] op_sel:[0,0] op_sel_hi:[0,1]\n"
      "v_pk_fma_f32 v[148:149], v[146:147], v[112:113], v[116:117] op_sel:[1,1,0] op_sel_hi:[1,0,1] neg_lo:[1,0,0]\n"
      "v_pk_mul_f32 v[116:117], v[148:149], v[112:113] op_sel:[0,0] op_sel_hi:[0,1]\n"
      "v_pk_fma_f32 v[150:151], v[148:149], v[112:113], v[116:117] op_sel:[1,1,0] op_sel_hi:[1,0,1] neg_lo:[1,0,0]\n"
      "ds_write_b128 v22, v[80:83] offset:48\n"
      "v_pk_mul_f32 v[116:117], v[150:151], v[112:113] op_sel:[0,0] op_sel_hi:[0,1]\n"
      "v_pk_fma_f32 v[152:153], v[150:151], v[112:113], v[116:117] op_sel:[1,1,0] op_sel_hi:[1,0,1] neg_lo:[1,0,0]\n"
      "v_fract_f32 v100, v105\n"
      "v_exp_f32 v98, v104\n"
      "v_cos_f32 v106, v100\n"
      "v_sin_f32 v107, v100\n"
      "ds_write_b128 v22, v[84:87] offset:176\n"
      "v_mul_f32 v106, v98, v106\n"
      "v_mul_f32 v107, v98, v107\n"
      "v_cvt_pk_bf16_f32 v48, v138, v140\n"
      "v_cvt_pk_bf16_f32 v49, v142, v144\n"
      "v_cvt_pk_bf16_f32 v50, v146, v148\n"
      "v_cvt_pk_bf16_f32 v51, v150, v152\n"
      "ds_write_b128 v22, v[88:91] offset:64\n"
      "v_cvt_pk_bf16_f32 v52, -v139, -v141\n"
      "v_cvt_pk_bf16_f32 v53, -v143, -v145\n"
      "v_cvt_pk_bf16_f32 v54, -v147, -v149\n"
      "v_cvt_pk_bf16_f32 v55, -v151, -v153\n"
      "v_mul_f32 v98, 0x43000000, v104\n"
      "v_mul_f32 v99, 0x43000000, v105\n"
      "ds_write_b128 v22, v[92:95] offset:192\n"
      "v_fract_f32 v100, v99\n"
      "v_exp_f32 v98, v98\n"
      "v_cos_f32 v114, v100\n"
      "v_sin_f32 v115, v100\n"
      "v_mul_f32 v114, v98, v114\n"
      "v_mul_f32 v115, v98, v115\n"
      "ds_write_b16 v22, v96 offset:80\n"
      "ds_write_b16_d16_hi v22, v96 offset:208\n"
      "buffer_load_dwordx4 a[80:83], v36, s[4:7], s55 offen\n"
      "s_add_u32 s55, s55, 10240\n"
      "s_add_u32 s98, s49, 11\n"
      "s_sub_u32 s99, s98, 10\n"
      "s_min_u32 s98, s98, s99\n"
      "s_sub_u32 s99, s98, 10\n"
      "s_min_u32 s98, s98, s99\n"
      "s_mul_i32 s98, s98, 221184\n"
      "s_add_u32 s99, s49, 2\n"
      "s_lshl_b32 s99, s99, 8\n"
      "s_add_u32 s98, s98, s99\n"
      "s_add_u32 s98, s98, s56\n"
      "buffer_load_dwordx4 a[20:23], v38, s[8:11], s98 offen\n"
      "buffer_load_dwordx4 a[84:87], v36, s[4:7], s55 offen\n"
      "s_add_u32 s55, s55, 10240\n"
      "s_add_u32 s98, s49, 11\n"
      "s_sub_u32 s99, s98, 10\n"
      "s_min_u32 s98, s98, s99\n"
      "s_sub_u32 s99, s98, 10\n"
      "s_min_u32 s98, s98, s99\n"
      "s_mul_i32 s98, s98, 221184\n"
      "s_add_u32 s99, s49, 3\n"
      "s_lshl_b32 s99, s99, 8\n"
      "s_add_u32 s98, s98, s99\n"
      "s_add_u32 s98, s98, s56\n"
      "buffer_load_dwordx4 a[24:27], v38, s[8:11], s98 offen\n"
      "buffer_load_dwordx4 a[88:91], v36, s[4:7], s55 offen\n"
      "s_add_u32 s55, s55, 10240\n"
      "s_add_u32 s98, s49, 12\n"
      "s_sub_u32 s99, s98, 10\n"
      "s_min_u32 s98, s98, s99\n"
      "s_sub_u32 s99, s98, 10\n"
      "s_min_u32 s98, s98, s99\n"
      "s_mul_i32 s98, s98, 221184\n"
      "s_add_u32 s99, s49, 3\n"
      "s_lshl_b32 s99, s99, 8\n"
      "s_add_u32 s98, s98, s99\n"
      "s_add_u32 s98, s98, s56\n"
      "buffer_load_dwordx4 a[28:31], v38, s[8:11], s98 offen\n"
      "buffer_load_dwordx4 a[92:95], v36, s[4:7], s55 offen\n"
      "s_add_u32 s55, s55, 10240\n"
      "s_add_u32 s98, s49, 12\n"
      "s_sub_u32 s99, s98, 10\n"
      "s_min_u32 s98, s98, s99\n"
      "s_sub_u32 s99, s98, 10\n"
      "s_min_u32 s98, s98, s99\n"
      "s_mul_i32 s98, s98, 221184\n"
      "s_add_u32 s99, s49, 4\n"
      "s_lshl_b32 s99, s99, 8\n"
      "s_add_u32 s98, s98, s99\n"
      "s_add_u32 s98, s98, s56\n"
      "buffer_load_dwordx4 a[32:35], v38, s[8:11], s98 offen\n"
      "s_add_u32 s98, s49, 13\n"
      "s_sub_u32 s99, s98, 10\n"
      "s_min_u32 s98, s98, s99\n"
      "s_sub_u32 s99, s98, 10\n"
      "s_min_u32 s98, s98, s99\n"
      "s_mul_i32 s98, s98, 221184\n"
      "s_add_u32 s99, s49, 4\n"
      "s_lshl_b32 s99, s99, 8\n"
      "s_add_u32 s98, s98, s99\n"
      "s_add_u32 s98, s98, s56\n"
      "buffer_load_dwordx4 a[36:39], v38, s[8:11], s98 offen\n"
      "s_add_u32 s98, s49, 13\n"
      "s_sub_u32 s99, s98, 10\n"
      "s_min_u32 s98, s98, s99\n"
      "s_sub_u32 s99, s98, 10\n"
      "s_min_u32 s98, s98, s99\n"
      "s_mul_i32 s98, s98, 221184\n"
      "s_add_u32 s99, s49, 5\n"
      "s_lshl_b32 s99, s99, 8\n"
      "s_add_u32 s98, s98, s99\n"
      "s_add_u32 s98, s98, s56\n"
      "buffer_load_dwordx4 a[40:43], v38, s[8:11], s98 offen\n"
      "s_add_u32 s98, s49, 14\n"
      "s_sub_u32 s99, s98, 10\n"
      "s_min_u32 s98, s98, s99\n"
      "s_sub_u32 s99, s98, 10\n"
      "s_min_u32 s98, s98, s99\n"
      "s_mul_i32 s98, s98, 221184\n"
      "s_add_u32 s99, s49, 5\n"
      "s_lshl_b32 s99, s99, 8\n"
      "s_add_u32 s98, s98, s99\n"
      "s_add_u32 s98, s98, s56\n"
      "buffer_load_dwordx4 a[44:47], v38, s[8:11], s98 offen\n"
      "s_add_u32 s98, s49, 14\n"
      "s_sub_u32 s99, s98, 10\n"
      "s_min_u32 s98, s98, s99\n"
      "s_sub_u32 s99, s98, 10\n"
      "s_min_u32 s98, s98, s99\n"
      "s_mul_i32 s98, s98, 221184\n"
      "s_add_u32 s99, s49, 6\n"
      "s_lshl_b32 s99, s99, 8\n"
      "s_add_u32 s98, s98, s99\n"
      "s_add_u32 s98, s98, s56\n"
      "buffer_load_dwordx4 a[48:51], v38, s[8:11], s98 offen\n"
      "s_add_u32 s98, s49, 15\n"
      "s_sub_u32 s99, s98, 10\n"
      "s_min_u32 s98, s98, s99\n"
      "s_sub_u32 s99, s98, 10\n"
      "s_min_u32 s98, s98, s99\n"
      "s_mul_i32 s98, s98, 221184\n"
      "s_add_u32 s99, s49, 6\n"
      "s_lshl_b32 s99, s99, 8\n"
      "s_add_u32 s98, s98, s99\n"
      "s_add_u32 s98, s98, s56\n"
      "buffer_load_dwordx4 a[52:55], v38, s[8:11], s98 offen\n"
      "s_waitcnt lgkmcnt(12)\n"
      "v_mfma_f32_16x16x32_bf16 v[158:161], v[198:201], v[182:185], 0\n"
      "s_waitcnt lgkmcnt(10)\n"
      "v_mfma_f32_16x16x32_bf16 v[158:161], v[202:205], v[186:189], v[158:161]\n"
      "s_waitcnt lgkmcnt(8)\n"
      "v_mfma_f32_16x16x32_bf16 v[158:161], v[206:209], v[190:193], v[158:161]\n"
      "s_waitcnt lgkmcnt(6)\n"
      "v_mfma_f32_16x16x32_bf16 v[158:161], v[210:213], v[194:197], v[158:161]\n"
      "ds_read_b64_tr_b16 v[214:215], v23 offset:32\n"
      "ds_read_b64_tr_b16 v[216:217], v23 offset:304\n"
      "ds_read_b64_tr_b16 v[218:219], v23 offset:4384\n"
      "ds_read_b64_tr_b16 v[220:221], v23 offset:4656\n"
      "ds_read_b64_tr_b16 v[222:223], v23 offset:8736\n"
      "ds_read_b64_tr_b16 v[224:225], v23 offset:9008\n"
      "ds_read_b64_tr_b16 v[226:227], v23 offset:13088\n"
      "ds_read_b64_tr_b16 v[228:229], v23 offset:13360\n"
      "v_add_f32 v102, v102, v102\n"
      "v_lshlrev_b32 v122, 16, v242\n"
      "v_lshlrev_b32 v123, 16, v243\n"
      "v_mul_f32 v122, v122, v102\n"
      "v_mul_f32 v123, v123, v102\n"
      "s_waitcnt vmcnt(28)\n"
      "v_lshlrev_b32 v97, 16, v245\n"
      "ds_read_b64_tr_b16 v[198:199], v23 offset:64\n"
      "ds_read_b64_tr_b16 v[200:201], v23 offset:336\n"
      "ds_read_b64_tr_b16 v[202:203], v23 offset:4416\n"
      "ds_read_b64_tr_b16 v[204:205], v23 offset:4688\n"
      "ds_read_b64_tr_b16 v[206:207], v23 offset:8768\n"
      "ds_read_b64_tr_b16 v[208:209], v23 offset:9040\n"
      "ds_read_b64_tr_b16 v[210:211], v23 offset:13120\n"
      "ds_read_b64_tr_b16 v[212:213], v23 offset:13392\n"
      "v_pk_mul_f32 v[116:117], v[122:123], v[114:115] op_sel:[0,0] op_sel_hi:[0,1]\n"
      "v_pk_fma_f32 v[138:139], v[122:123], v[114:115], v[116:117] op_sel:[1,1,0] op_sel_hi:[1,0,1] neg_lo:[1,0,0]\n"
      "v_pk_mul_f32 v[116:117], v[122:123], v[106:107] op_sel:[0,0] op_sel_hi:[0,1]\n"
      "v_pk_fma_f32 v[124:125], v[122:123], v[106:107], v[116:117] op_sel:[1,1,0] op_sel_hi:[1,0,1] neg_lo:[1,0,0]\n"
      "v_pk_mul_f32 v[116:117], v[124:125], v[106:107] op_sel:[0,0] op_sel_hi:[0,1]\n"
      "v_pk_fma_f32 v[126:127], v[124:125], v[106:107], v[116:117] op_sel:[1,1,0] op_sel_hi:[1,0,1] neg_lo:[1,0,0]\n"
      "v_add_u32 v154, s65, v24\n"
      "v_add_u32 v155, s65, v25\n"
      "v_add_u32 v157, s65, v2\n"
      "s_mul_i32 s68, s50, -34816\n"
      "s_add_u32 s68, s68, 17408\n"
      "v_add_u32 v156, s68, v28\n"
      "v_pk_mul_f32 v[116:117], v[126:127], v[106:107] op_sel:[0,0] op_sel_hi:[0,1]\n"
      "v_pk_fma_f32 v[128:129], v[126:127], v[106:107], v[116:117] op_sel:[1,1,0] op_sel_hi:[1,0,1] neg_lo:[1,0,0]\n"
      "v_pk_mul_f32 v[116:117], v[128:129], v[106:107] op_sel:[0,0] op_sel_hi:[0,1]\n"
      "v_pk_fma_f32 v[130:131], v[128:129], v[106:107], v[116:117] op_sel:[1,1,0] op_sel_hi:[1,0,1] neg_lo:[1,0,0]\n"
      "v_pk_mul_f32 v[116:117], v[130:131], v[106:107] op_sel:[0,0] op_sel_hi:[0,1]\n"
      "v_pk_fma_f32 v[132:133], v[130:131], v[106:107], v[116:117] op_sel:[1,1,0] op_sel_hi:[1,0,1] neg_lo:[1,0,0]\n"
      "v_pk_mul_f32 v[116:117], v[132:133], v[106:107] op_sel:[0,0] op_sel_hi:[0,1]\n"
      "v_pk_fma_f32 v[134:135], v[132:133], v[106:107], v[116:117] op_sel:[1,1,0] op_sel_hi:[1,0,1] neg_lo:[1,0,0]\n"
      "v_pk_mul_f32 v[116:117], v[134:135], v[106:107] op_sel:[0,0] op_sel_hi:[0,1]\n"
      "v_pk_fma_f32 v[136:137], v[134:135], v[106:107], v[116:117] op_sel:[1,1,0] op_sel_hi:[1,0,1] neg_lo:[1,0,0]\n"
      "v_cvt_pk_bf16_f32 v56, v122, v124\n"
      "v_cvt_pk_bf16_f32 v57, v126, v128\n"
      "v_cvt_pk_bf16_f32 v58, v130, v132\n"
      "v_cvt_pk_bf16_f32 v59, v134, v136\n"
      "v_cvt_pk_bf16_f32 v60, v123, v125\n"
      "v_cvt_pk_bf16_f32 v61, v127, v129\n"
      "v_cvt_pk_bf16_f32 v62, v131, v133\n"
      "v_cvt_pk_bf16_f32 v63, v135, v137\n"
      "s_waitcnt lgkmcnt(14)\n"
      "v_mfma_f32_16x16x32_bf16 v[162:165], v[214:217], v[182:185], 0\n"
      "s_waitcnt lgkmcnt(12)\n"
      "v_mfma_f32_16x16x32_bf16 v[162:165], v[218:221], v[186:189], v[162:165]\n"
      "s_waitcnt lgkmcnt(10)\n"
      "v_mfma_f32_16x16x32_bf16 v[162:165], v[222:225], v[190:193], v[162:165]\n"
      "s_waitcnt lgkmcnt(8)\n"
      "v_mfma_f32_16x16x32_bf16 v[162:165], v[226:229], v[194:197], v[162:165]\n"
      "s_waitcnt lgkmcnt(6)\n"
      "v_mfma_f32_16x16x32_bf16 v[166:169], v[198:201], v[182:185], 0\n"
      "s_waitcnt lgkmcnt(4)\n"
      "v_mfma_f32_16x16x32_bf16 v[166:169], v[202:205], v[186:189], v[166:169]\n"
      "s_waitcnt lgkmcnt(2)\n"
      "v_mfma_f32_16x16x32_bf16 v[166:169], v[206:209], v[190:193], v[166:169]\n"
      "s_waitcnt lgkmcnt(0)\n"
      "v_mfma_f32_16x16x32_bf16 v[166:169], v[210:213], v[194:197], v[166:169]\n"
      "v_pk_mul_f32 v[116:117], v[138:139], v[114:115] op_sel:[0,0] op_sel_hi:[0,1]\n"
      "v_pk_fma_f32 v[122:123], v[138:139], v[114:115], v[116:117] op_sel:[1,1,0] op_sel_hi:[1,0,1] neg_lo:[1,0,0]\n"
      "v_pk_mul_f32 v[116:117], v[138:139], v[106:107] op_sel:[0,0] op_sel_hi:[0,1]\n"
      "v_pk_fma_f32 v[140:141], v[138:139], v[106:107], v[116:117] op_sel:[1,1,0] op_sel_hi:[1,0,1] neg_lo:[1,0,0]\n"
      "v_pk_mul_f32 v[116:117], v[140:141], v[106:107] op_sel:[0,0] op_sel_hi:[0,1]\n"
      "v_pk_fma_f32 v[142:143], v[140:141], v[106:107], v[116:117] op_sel:[1,1,0] op_sel_hi:[1,0,1] neg_lo:[1,0,0]\n"
      "v_pk_mul_f32 v[116:117], v[142:143], v[106:107] op_sel:[0,0] op_sel_hi:[0,1]\n"
      "v_pk_fma_f32 v[144:145], v[142:143], v[106:107], v[116:117] op_sel:[1,1,0] op_sel_hi:[1,0,1] neg_lo:[1,0,0]\n"
      "v_pk_mul_f32 v[116:117], v[144:145], v[106:107] op_sel:[0,0] op_sel_hi:[0,1]\n"
      "v_pk_fma_f32 v[146:147], v[144:145], v[106:107], v[116:117] op_sel:[1,1,0] op_sel_hi:[1,0,1] neg_lo:[1,0,0]\n"
      "v_pk_mul_f32 v[116:117], v[146:147], v[106:107] op_sel:[0,0] op_sel_hi:[0,1]\n"
      "v_pk_fma_f32 v[148:149], v[146:147], v[106:107], v[116:117] op_sel:[1,1,0] op_sel_hi:[1,0,1] neg_lo:[1,0,0]\n"
      "v_pk_mul_f32 v[116:117], v[148:149], v[106:107] op_sel:[0,0] op_sel_hi:[0,1]\n"
      "v_pk_fma_f32 v[150:151], v[148:149], v[106:107], v[116:117] op_sel:[1,1,0] op_sel_hi:[1,0,1] neg_lo:[1,0,0]\n"
      "v_pk_mul_f32 v[116:117], v[150:151], v[106:107] op_sel:[0,0] op_sel_hi:[0,1]\n"
      "v_pk_fma_f32 v[152:153], v[150:151], v[106:107], v[116:117] op_sel:[1,1,0] op_sel_hi:[1,0,1] neg_lo:[1,0,0]\n"
      "v_pk_mul_f32 v[116:117], v[106:107], v[106:107] op_sel:[0,0] op_sel_hi:[0,1]\n"
      "v_pk_fma_f32 v[108:109], v[106:107], v[106:107], v[116:117] op_sel:[1,1,0] op_sel_hi:[1,0,1] neg_lo:[1,0,0]\n"
      "s_cmp_lg_u32 s50, 0\n"
      "s_cbranch_scc1 .Lkx_22\n"
      "ds_write_b128 v28, v[158:161]\n"
      "ds_write_b128 v28, v[162:165] offset:1024\n"
      "v_mov_b32 v170, 0\n"
      "v_mov_b32 v171, 0\n"
      "v_mov_b32 v172, 0\n"
      "v_mov_b32 v173, 0\n"
      "s_mov_b64 exec, 0x3fffffff\n"
      "ds_write_b128 v157, v[170:173] offset:4096\n"
      "s_mov_b64 exec, -1\n"
      "v_cvt_pk_bf16_f32 v64, v138, v140\n"
      "v_cvt_pk_bf16_f32 v65, v142, v144\n"
      "v_cvt_pk_bf16_f32 v66, v146, v148\n"
      "v_cvt_pk_bf16_f32 v67, v150, v152\n"
      "v_cvt_pk_bf16_f32 v68, v139, v141\n"
      "v_cvt_pk_bf16_f32 v69, v143, v145\n"
      "v_cvt_pk_bf16_f32 v70, v147, v149\n"
      "v_cvt_pk_bf16_f32 v71, v151, v153\n"
      "v_pk_mul_f32 v[116:117], v[122:123], v[114:115] op_sel:[0,0] op_sel_hi:[0,1]\n"
      "v_pk_fma_f32 v[130:131], v[122:123], v[114:115], v[116:117] op_sel:[1,1,0] op_sel_hi:[1,0,1] neg_lo:[1,0,0]\n"
      "v_pk_mul_f32 v[116:117], v[130:131], v[114:115] op_sel:[0,0] op_sel_hi:[0,1]\n"
      "v_pk_fma_f32 v[138:139], v[130:131], v[114:115], v[116:117] op_sel:[1,1,0] op_sel_hi:[1,0,1] neg_lo:[1,0,0]\n"
      "v_pk_mul_f32 v[116:117], v[122:123], v[108:109] op_sel:[0,0] op_sel_hi:[0,1]\n"
      "v_pk_fma_f32 v[124:125], v[122:123], v[108:109], v[116:117] op_sel:[1,1,0] op_sel_hi:[1,0,1] neg_lo:[1,0,0]\n"
      "v_pk_mul_f32 v[116:117], v[130:131], v[108:109] op_sel:[0,0] op_sel_hi:[0,1]\n"
      "v_pk_fma_f32 v[132:133], v[130:131], v[108:109], v[116:117] op_sel:[1,1,0] op_sel_hi:[1,0,1] neg_lo:[1,0,0]\n"
      "v_pk_mul_f32 v[116:117], v[124:125], v[108:109] op_sel:[0,0] op_sel_hi:[0,1]\n"
      "v_pk_fma_f32 v[126:127], v[124:125], v[108:109], v[116:117] op_sel:[1,1,0] op_sel_hi:[1,0,1] neg_lo:[1,0,0]\n"
      "v_pk_mul_f32 v[116:117], v[132:133], v[108:109] op_sel:[0,0] op_sel_hi:[0,1]\n"
      "v_pk_fma_f32 v[134:135], v[132:133], v[108:109], v[116:117] op_sel:[1,1,0] op_sel_hi:[1,0,1] neg_lo:[1,0,0]\n"
      "v_pk_mul_f32 v[116:117], v[126:127], v[108:109] op_sel:[0,0] op_sel_hi:[0,1]\n"
      "v_pk_fma_f32 v[128:129], v[126:127], v[108:109], v[116:117] op_sel:[1,1,0] op_sel_hi:[1,0,1] neg_lo:[1,0,0]\n"
      "v_pk_mul_f32 v[116:117], v[134:135], v[108:109] op_sel:[0,0] op_sel_hi:[0,1]\n"
      "v_pk_fma_f32 v[136:137], v[134:135], v[108:109], v[116:117] op_sel:[1,1,0] op_sel_hi:[1,0,1] neg_lo:[1,0,0]\n"
      "s_waitcnt lgkmcnt(0)\n"
      "s_barrier\n"
      "ds_read_b128 v[170:173], v156\n"
      "v_pk_mul_f32 v[116:117], v[108:109], v[108:109] op_sel:[0,0] op_sel_hi:[0,1]\n"
      "v_pk_fma_f32 v[110:111], v[108:109], v[108:109], v[116:117] op_sel:[1,1,0] op_sel_hi:[1,0,1] neg_lo:[1,0,0]\n"
      "v_cvt_pk_bf16_f32 v72, v122, v124\n"
      "v_cvt_pk_bf16_f32 v73, v126, v128\n"
      "v_cvt_pk_bf16_f32 v74, v130, v132\n"
      "v_cvt_pk_bf16_f32 v75, v134, v136\n"
      "v_cvt_pk_bf16_f32 v76, v123, v125\n"
      "v_cvt_pk_bf16_f32 v77, v127, v129\n"
      "v_cvt_pk_bf16_f32 v78, v131, v133\n"
      "v_cvt_pk_bf16_f32 v79, v135, v137\n"
      "v_pk_mul_f32 v[116:117], v[138:139], v[114:115] op_sel:[0,0] op_sel_hi:[0,1]\n"
      "v_pk_fma_f32 v[142:143], v[138:139], v[114:115], v[116:117] op_sel:[1,1,0] op_sel_hi:[1,0,1] neg_lo:[1,0,0]\n"
      "v_pk_mul_f32 v[116:117], v[142:143], v[114:115] op_sel:[0,0] op_sel_hi:[0,1]\n"
      "v_pk_fma_f32 v[146:147], v[142:143], v[114:115], v[116:117] op_sel:[1,1,0] op_sel_hi:[1,0,1] neg_lo:[1,0,0]\n"
      "v_pk_mul_f32 v[116:117], v[146:147], v[114:115] op_sel:[0,0] op_sel_hi:[0,1]\n"
      "v_pk_fma_f32 v[150:151], v[146:147], v[114:115], v[116:117] op_sel:[1,1,0] op_sel_hi:[1,0,1] neg_lo:[1,0,0]\n"
      "v_pk_mul_f32 v[116:117], v[150:151], v[114:115] op_sel:[0,0] op_sel_hi:[0,1]\n"
      "v_pk_fma_f32 v[122:123], v[150:151], v[114:115], v[116:117] op_sel:[1,1,0] op_sel_hi:[1,0,1] neg_lo:[1,0,0]\n"
      "v_pk_mul_f32 v[116:117], v[138:139], v[110:111] op_sel:[0,0] op_sel_hi:[0,1]\n"
      "v_pk_fma_f32 v[140:141], v[138:139], v[110:111], v[116:117] op_sel:[1,1,0] op_sel_hi:[1,0,1] neg_lo:[1,0,0]\n"
      "v_pk_mul_f32 v[116:117], v[142:143], v[110:111] op_sel:[0,0] op_sel_hi:[0,1]\n"
      "v_pk_fma_f32 v[144:145], v[142:143], v[110:111], v[116:117] op_sel:[1,1,0] op_sel_hi:[1,0,1] neg_lo:[1,0,0]\n"
      "v_pk_mul_f32 v[116:117], v[146:147], v[110:111] op_sel:[0,0] op_sel_hi:[0,1]\n"
      "v_pk_fma_f32 v[148:149], v[146:147], v[110:111], v[116:117] op_sel:[1,1,0] op_sel_hi:[1,0,1] neg_lo:[1,0,0]\n"
      "v_pk_mul_f32 v[116:117], v[150:151], v[110:111] op_sel:[0,0] op_sel_hi:[0,1]\n"
      "v_pk_fma_f32 v[152:153], v[150:151], v[110:111], v[116:117] op_sel:[1,1,0] op_sel_hi:[1,0,1] neg_lo:[1,0,0]\n"
      "v_cvt_pk_bf16_f32 v80, v138, v140\n"
      "v_cvt_pk_bf16_f32 v81, v142, v144\n"
      "v_cvt_pk_bf16_f32 v82, v146, v148\n"
      "v_cvt_pk_bf16_f32 v83, v150, v152\n"
      "v_cvt_pk_bf16_f32 v84, v139, v141\n"
      "v_cvt_pk_bf16_f32 v85, v143, v145\n"
      "v_cvt_pk_bf16_f32 v86, v147, v149\n"
      "v_cvt_pk_bf16_f32 v87, v151, v153\n"
      "v_pk_mul_f32 v[116:117], v[122:123], v[114:115] op_sel:[0,0] op_sel_hi:[0,1]\n"
      "v_pk_fma_f32 v[124:125], v[122:123], v[114:115], v[116:117] op_sel:[1,1,0] op_sel_hi:[1,0,1] neg_lo:[1,0,0]\n"
      "s_waitcnt lgkmcnt(0)\n"
      "v_add_f32 v166, v166, v170\n"
      "v_add_f32 v167, v167, v171\n"
      "v_add_f32 v168, v168, v172\n"
      "v_add_f32 v169, v169, v173\n"
      "v_cvt_pk_bf16_f32 v170, v166, v166\n"
      "v_mov_b32 v171, v170\n"
      "v_cvt_pk_bf16_f32 v172, v167, v167\n"
      "v_mov_b32 v173, v172\n"
      "v_cvt_pk_bf16_f32 v174, v168, v168\n"
      "v_mov_b32 v175, v174\n"
      "v_cvt_pk_bf16_f32 v176, v169, v169\n"
      "v_mov_b32 v177, v176\n"
      "s_mov_b32 exec_hi, 0\n"
      "ds_write_b64 v154, v[170:171] offset:776\n"
      "ds_write_b64 v154, v[172:173] offset:520\n"
      "ds_write_b64 v154, v[174:175] offset:264\n"
      "ds_write_b64 v154, v[176:177] offset:8\n"
      "ds_write_b64 v154, v[172:173] offset:528\n"
      "ds_write_b64 v154, v[174:175] offset:272\n"
      "ds_write_b64 v154, v[176:177] offset:16\n"
      "s_mov_b32 exec_lo, 0xfffffffe\n"
      "s_mov_b32 exec_hi, 1\n"
      "ds_write_b64 v154, v[170:171] offset:784\n"
      "s_mov_b64 exec, 1\n"
      "ds_write_b32 v154, v170 offset:784\n"
      "s_mov_b32 exec_lo, 0x55555555\n"
      "ds_write_b16 v154, v170 offset:774\n"
      "ds_write_b16 v154, v172 offset:518\n"
      "ds_write_b16 v154, v174 offset:262\n"
      "ds_write_b16 v154, v176 offset:6\n"
      "s_mov_b64 exec, -1\n"
      "v_pk_mul_f32 v[116:117], v[124:125], v[114:115] op_sel:[0,0] op_sel_hi:[0,1]\n"
      "v_pk_fma_f32 v[126:127], v[124:125], v[114:115], v[116:117] op_sel:[1,1,0] op_sel_hi:[1,0,1] neg_lo:[1,0,0]\n"
      "v_pk_mul_f32 v[116:117], v[126:127], v[114:115] op_sel:[0,0] op_sel_hi:[0,1]\n"
      "v_pk_fma_f32 v[128:129], v[126:127], v[114:115], v[116:117] op_sel:[1,1,0] op_sel_hi:[1,0,1] neg_lo:[1,0,0]\n"
      "v_pk_mul_f32 v[116:117], v[128:129], v[114:115] op_sel:[0,0] op_sel_hi:[0,1]\n"
      "v_pk_fma_f32 v[130:131], v[128:129], v[114:115], v[116:117] op_sel:[1,1,0] op_sel_hi:[1,0,1] neg_lo:[1,0,0]\n"
      "v_pk_mul_f32 v[116:117], v[130:131], v[114:115] op_sel:[0,0] op_sel_hi:[0,1]\n"
      "v_pk_fma_f32 v[132:133], v[130:131], v[114:115], v[116:117] op_sel:[1,1,0] op_sel_hi:[1,0,1] neg_lo:[1,0,0]\n"
      "v_pk_mul_f32 v[116:117], v[132:133], v[114:115] op_sel:[0,0] op_sel_hi:[0,1]\n"
      "v_pk_fma_f32 v[134:135], v[132:133], v[114:115], v[116:117] op_sel:[1,1,0] op_sel_hi:[1,0,1] neg_lo:[1,0,0]\n"
      "v_pk_mul_f32 v[116:117], v[134:135], v[114:115] op_sel:[0,0] op_sel_hi:[0,1]\n"
      "v_pk_fma_f32 v[136:137], v[134:135], v[114:115], v[116:117] op_sel:[1,1,0] op_sel_hi:[1,0,1] neg_lo:[1,0,0]\n"
      "v_pk_mul_f32 v[116:117], v[136:137], v[114:115] op_sel:[0,0] op_sel_hi:[0,1]\n"
      "v_pk_fma_f32 v[138:139], v[136:137], v[114:115], v[116:117] op_sel:[1,1,0] op_sel_hi:[1,0,1] neg_lo:[1,0,0]\n"
      "v_cvt_pk_bf16_f32 v88, v122, v124\n"
      "v_cvt_pk_bf16_f32 v89, v126, v128\n"
      "v_cvt_pk_bf16_f32 v90, v130, v132\n"
      "v_cvt_pk_bf16_f32 v91, v134, v136\n"
      "v_cvt_pk_bf16_f32 v92, v123, v125\n"
      "v_cvt_pk_bf16_f32 v93, v127, v129\n"
      "v_cvt_pk_bf16_f32 v94, v131, v133\n"
      "v_cvt_pk_bf16_f32 v95, v135, v137\n"
      "v_cvt_pk_bf16_f32 v96, v138, v139\n"
      "s_nop 1\n"
      "s_branch .Lkx_23\n"
      ".Lkx_22:\n"
      "s_mov_b64 exec, 1\n"
      "v_add_f32 v158, v158, v181\n"
      "s_mov_b64 exec, -1\n"
      "ds_write_b128 v28, v[166:169]\n"
      "v_cvt_pk_bf16_f32 v64, v138, v140\n"
      "v_cvt_pk_bf16_f32 v65, v142, v144\n"
      "v_cvt_pk_bf16_f32 v66, v146, v148\n"
      "v_cvt_pk_bf16_f32 v67, v150, v152\n"
      "v_cvt_pk_bf16_f32 v68, v139, v141\n"
      "v_cvt_pk_bf16_f32 v69, v143, v145\n"
      "v_cvt_pk_bf16_f32 v70, v147, v149\n"
      "v_cvt_pk_bf16_f32 v71, v151, v153\n"
      "v_pk_mul_f32 v[116:117], v[122:123], v[114:115] op_sel:[0,0] op_sel_hi:[0,1]\n"
      "v_pk_fma_f32 v[130:131], v[122:123], v[114:115], v[116:117] op_sel:[1,1,0] op_sel_hi:[1,0,1] neg_lo:[1,0,0]\n"
      "v_pk_mul_f32 v[116:117], v[130:131], v[114:115] op_sel:[0,0] op_sel_hi:[0,1]\n"
      "v_pk_fma_f32 v[138:139], v[130:131], v[114:115], v[116:117] op_sel:[1,1,0] op_sel_hi:[1,0,1] neg_lo:[1,0,0]\n"
      "v_pk_mul_f32 v[116:117], v[122:123], v[108:109] op_sel:[0,0] op_sel_hi:[0,1]\n"
      "v_pk_fma_f32 v[124:125], v[122:123], v[108:109], v[116:117] op_sel:[1,1,0] op_sel_hi:[1,0,1] neg_lo:[1,0,0]\n"
      "v_pk_mul_f32 v[116:117], v[130:131], v[108:109] op_sel:[0,0] op_sel_hi:[0,1]\n"
      "v_pk_fma_f32 v[132:133], v[130:131], v[108:109], v[116:117] op_sel:[1,1,0] op_sel_hi:[1,0,1] neg_lo:[1,0,0]\n"
      "v_pk_mul_f32 v[116:117], v[124:125], v[108:109] op_sel:[0,0] op_sel_hi:[0,1]\n"
      "v_pk_fma_f32 v[126:127], v[124:125], v[108:109], v[116:117] op_sel:[1,1,0] op_sel_hi:[1,0,1] neg_lo:[1,0,0]\n"
      "v_pk_mul_f32 v[116:117], v[132:133], v[108:109] op_sel:[0,0] op_sel_hi:[0,1]\n"
      "v_pk_fma_f32 v[134:135], v[132:133], v[108:109], v[116:117] op_sel:[1,1,0] op_sel_hi:[1,0,1] neg_lo:[1,0,0]\n"
      "v_pk_mul_f32 v[116:117], v[126:127], v[108:109] op_sel:[0,0] op_sel_hi:[0,1]\n"
      "v_pk_fma_f32 v[128:129], v[126:127], v[108:109], v[116:117] op_sel:[1,1,0] op_sel_hi:[1,0,1] neg_lo:[1,0,0]\n"
      "v_pk_mul_f32 v[116:117], v[134:135], v[108:109] op_sel:[0,0] op_sel_hi:[0,1]\n"
      "v_pk_fma_f32 v[136:137], v[134:135], v[108:109], v[116:117] op_sel:[1,1,0] op_sel_hi:[1,0,1] neg_lo:[1,0,0]\n"
      "s_waitcnt lgkmcnt(0)\n"
      "s_barrier\n"
      "ds_read_b128 v[174:177], v156 offset:1024\n"
      "ds_read_b128 v[170:173], v156\n"
      "v_pk_mul_f32 v[116:117], v[108:109], v[108:109] op_sel:[0,0] op_sel_hi:[0,1]\n"
      "v_pk_fma_f32 v[110:111], v[108:109], v[108:109], v[116:117] op_sel:[1,1,0] op_sel_hi:[1,0,1] neg_lo:[1,0,0]\n"
      "v_cvt_pk_bf16_f32 v72, v122, v124\n"
      "v_cvt_pk_bf16_f32 v73, v126, v128\n"
      "v_cvt_pk_bf16_f32 v74, v130, v132\n"
      "v_cvt_pk_bf16_f32 v75, v134, v136\n"
      "v_cvt_pk_bf16_f32 v76, v123, v125\n"
      "v_cvt_pk_bf16_f32 v77, v127, v129\n"
      "v_cvt_pk_bf16_f32 v78, v131, v133\n"
      "v_cvt_pk_bf16_f32 v79, v135, v137\n"
      "v_pk_mul_f32 v[116:117], v[138:139], v[114:115] op_sel:[0,0] op_sel_hi:[0,1]\n"
      "v_pk_fma_f32 v[142:143], v[138:139], v[114:115], v[116:117] op_sel:[1,1,0] op_sel_hi:[1,0,1] neg_lo:[1,0,0]\n"
      "v_pk_mul_f32 v[116:117], v[142:143], v[114:115] op_sel:[0,0] op_sel_hi:[0,1]\n"
      "v_pk_fma_f32 v[146:147], v[142:143], v[114:115], v[116:117] op_sel:[1,1,0] op_sel_hi:[1,0,1] neg_lo:[1,0,0]\n"
      "v_pk_mul_f32 v[116:117], v[146:147], v[114:115] op_sel:[0,0] op_sel_hi:[0,1]\n"
      "v_pk_fma_f32 v[150:151], v[146:147], v[114:115], v[116:117] op_sel:[1,1,0] op_sel_hi:[1,0,1] neg_lo:[1,0,0]\n"
      "v_pk_mul_f32 v[116:117], v[150:151], v[114:115] op_sel:[0,0] op_sel_hi:[0,1]\n"
      "v_pk_fma_f32 v[122:123], v[150:151], v[114:115], v[116:117] op_sel:[1,1,0] op_sel_hi:[1,0,1] neg_lo:[1,0,0]\n"
      "v_pk_mul_f32 v[116:117], v[138:139], v[110:111] op_sel:[0,0] op_sel_hi:[0,1]\n"
      "v_pk_fma_f32 v[140:141], v[138:139], v[110:111], v[116:117] op_sel:[1,1,0] op_sel_hi:[1,0,1] neg_lo:[1,0,0]\n"
      "v_pk_mul_f32 v[116:117], v[142:143], v[110:111] op_sel:[0,0] op_sel_hi:[0,1]\n"
      "v_pk_fma_f32 v[144:145], v[142:143], v[110:111], v[116:117] op_sel:[1,1,0] op_sel_hi:[1,0,1] neg_lo:[1,0,0]\n"
      "v_pk_mul_f32 v[116:117], v[146:147], v[110:111] op_sel:[0,0] op_sel_hi:[0,1]\n"
      "v_pk_fma_f32 v[148:149], v[146:147], v[110:111], v[116:117] op_sel:[1,1,0] op_sel_hi:[1,0,1] neg_lo:[1,0,0]\n"
      "v_pk_mul_f32 v[116:117], v[150:151], v[110:111] op_sel:[0,0] op_sel_hi:[0,1]\n"
      "v_pk_fma_f32 v[152:153], v[150:151], v[110:111], v[116:117] op_sel:[1,1,0] op_sel_hi:[1,0,1] neg_lo:[1,0,0]\n"
      "v_cvt_pk_bf16_f32 v80, v138, v140\n"
      "v_cvt_pk_bf16_f32 v81, v142, v144\n"
      "v_cvt_pk_bf16_f32 v82, v146, v148\n"
      "v_cvt_pk_bf16_f32 v83, v150, v152\n"
      "v_cvt_pk_bf16_f32 v84, v139, v141\n"
      "v_cvt_pk_bf16_f32 v85, v143, v145\n"
      "v_cvt_pk_bf16_f32 v86, v147, v149\n"
      "v_cvt_pk_bf16_f32 v87, v151, v153\n"
      "v_pk_mul_f32 v[116:117], v[122:123], v[114:115] op_sel:[0,0] op_sel_hi:[0,1]\n"
      "v_pk_fma_f32 v[124:125], v[122:123], v[114:115], v[116:117] op_sel:[1,1,0] op_sel_hi:[1,0,1] neg_lo:[1,0,0]\n"
      "s_waitcnt lgkmcnt(1)\n"
      "v_add_f32 v162, v162, v174\n"
      "v_add_f32 v163, v163, v175\n"
      "v_add_f32 v164, v164, v176\n"
      "v_add_f32 v165, v165, v177\n"
      "s_waitcnt lgkmcnt(0)\n"
      "v_add_f32 v158, v158, v170\n"
      "v_add_f32 v159, v159, v171\n"
      "v_add_f32 v160, v160, v172\n"
      "v_add_f32 v161, v161, v173\n"
      "v_cvt_pk_bf16_f32 v170, v163, v162\n"
      "v_cvt_pk_bf16_f32 v171, v162, v162\n"
      "v_cvt_pk_bf16_f32 v172, v163, v163\n"
      "v_cvt_pk_bf16_f32 v174, v165, v164\n"
      "v_cvt_pk_bf16_f32 v175, v164, v164\n"
      "v_cvt_pk_bf16_f32 v176, v165, v165\n"
      "s_mov_b32 exec_lo, 0\n"
      "ds_write_b32 v154, v171 offset:272\n"
      "ds_write_b64 v154, v[170:171] offset:264\n"
      "ds_write_b32 v154, v172 offset:260\n"
      "ds_write_b32 v154, v175 offset:16\n"
      "ds_write_b64 v154, v[174:175] offset:8\n"
      "ds_write_b32 v154, v176 offset:4\n"
      "s_mov_b32 exec_lo, -1\n"
      "v_cvt_pk_bf16_f32 v178, v164, v163\n"
      "v_mov_b32 v179, v171\n"
      "v_cvt_pk_bf16_f32 v180, v161, v160\n"
      "v_cvt_pk_bf16_f32 v181, v159, v158\n"
      "s_mov_b32 exec_hi, 0\n"
      "ds_write_b16 v154, v171 offset:16\n"
      "ds_write_b64 v154, v[178:179] offset:8\n"
      "ds_write_b32 v154, v175 offset:4\n"
      "ds_write_b16 v154, v176 offset:2\n"
      "s_mov_b64 exec, -1\n"
      "ds_write_b64 v155, v[180:181]\n"
      "v_pk_mul_f32 v[116:117], v[124:125], v[114:115] op_sel:[0,0] op_sel_hi:[0,1]\n"
      "v_pk_fma_f32 v[126:127], v[124:125], v[114:115], v[116:117] op_sel:[1,1,0] op_sel_hi:[1,0,1] neg_lo:[1,0,0]\n"
      "v_pk_mul_f32 v[116:117], v[126:127], v[114:115] op_sel:[0,0] op_sel_hi:[0,1]\n"
      "v_pk_fma_f32 v[128:129], v[126:127], v[114:115], v[116:117] op_sel:[1,1,0] op_sel_hi:[1,0,1] neg_lo:[1,0,0]\n"
      "v_pk_mul_f32 v[116:117], v[128:129], v[114:115] op_sel:[0,0] op_sel_hi:[0,1]\n"
      "v_pk_fma_f32 v[130:131], v[128:129], v[114:115], v[116:117] op_sel:[1,1,0] op_sel_hi:[1,0,1] neg_lo:[1,0,0]\n"
      "v_pk_mul_f32 v[116:117], v[130:131], v[114:115] op_sel:[0,0] op_sel_hi:[0,1]\n"
      "v_pk_fma_f32 v[132:133], v[130:131], v[114:115], v[116:117] op_sel:[1,1,0] op_sel_hi:[1,0,1] neg_lo:[1,0,0]\n"
      "v_pk_mul_f32 v[116:117], v[132:133], v[114:115] op_sel:[0,0] op_sel_hi:[0,1]\n"
      "v_pk_fma_f32 v[134:135], v[132:133], v[114:115], v[116:117] op_sel:[1,1,0] op_sel_hi:[1,0,1] neg_lo:[1,0,0]\n"
      "v_pk_mul_f32 v[116:117], v[134:135], v[114:115] op_sel:[0,0] op_sel_hi:[0,1]\n"
      "v_pk_fma_f32 v[136:137], v[134:135], v[114:115], v[116:117] op_sel:[1,1,0] op_sel_hi:[1,0,1] neg_lo:[1,0,0]\n"
      "v_pk_mul_f32 v[116:117], v[136:137], v[114:115] op_sel:[0,0] op_sel_hi:[0,1]\n"
      "v_pk_fma_f32 v[138:139], v[136:137], v[114:115], v[116:117] op_sel:[1,1,0] op_sel_hi:[1,0,1] neg_lo:[1,0,0]\n"
      "v_cvt_pk_bf16_f32 v88, v122, v124\n"
      "v_cvt_pk_bf16_f32 v89, v126, v128\n"
      "v_cvt_pk_bf16_f32 v90, v130, v132\n"
      "v_cvt_pk_bf16_f32 v91, v134, v136\n"
      "v_cvt_pk_bf16_f32 v92, v123, v125\n"
      "v_cvt_pk_bf16_f32 v93, v127, v129\n"
      "v_cvt_pk_bf16_f32 v94, v131, v133\n"
      "v_cvt_pk_bf16_f32 v95, v135, v137\n"
      "v_cvt_pk_bf16_f32 v96, v138, v139\n"
      "s_nop 1\n"
      ".Lkx_23:\n"
      "s_waitcnt lgkmcnt(0)\n"
      "s_barrier\n"
      "s_add_u32 s65, s65, 9216\n"
      "ds_read_b128 v[232:235], v31 offset:0\n"
      "ds_read_b128 v[236:239], v31 offset:4096\n"
      "ds_read_b128 v[32:35], v31 offset:8192\n"
      "s_waitcnt lgkmcnt(2)\n"
      "buffer_store_dwordx4 v[232:235], v2, s[16:19], s53 offen sc1\n"
      "s_waitcnt lgkmcnt(1)\n"
      "buffer_store_dwordx4 v[236:239], v3, s[16:19], s53 offen sc1\n"
      "s_add_u32 s98, s53, 8192\n"
      "s_cmp_lg_u32 s43, 0\n"
      "s_cselect_b32 s98, 2146435072, s98\n"
      "s_waitcnt lgkmcnt(0)\n"
      "buffer_store_dwordx4 v[32:35], v2, s[16:19], s98 offen sc1\n"
      "s_add_u32 s53, s53, 9216\n"
      "v_mov_b32 v181, v97\n"
      "ds_write_b128 v22, v[40:43] offset:96\n"
      "s_waitcnt vmcnt(26)\n"
      "v_lshlrev_b32 v98, 16, v250\n"
      "v_mul_f32 v99, 0x3fb8aa00, v98\n"
      "v_mul_f32 v100, 0x36eca570, v98\n"
      "v_rndne_f32 v101, v99\n"
      "v_sub_f32 v99, v99, v101\n"
      "v_add_f32 v99, v99, v100\n"
      "ds_write_b128 v22, v[44:47] offset:224\n"
      "v_exp_f32 v99, v99\n"
      "v_cvt_i32_f32 v101, v101\n"
      "v_ldexp_f32 v99, v99, v101\n"
      "v_cvt_pk_bf16_f32 v99, v99, v99\n"
      "v_lshlrev_b32 v102, 16, v99\n"
      "v_lshlrev_b32 v98, 16, v246\n"
      "ds_write_b128 v22, v[48:51] offset:112\n"
      "v_mul_f32 v99, 0x3fb8aa00, v98\n"
      "v_mul_f32 v100, 0x36eca570, v98\n"
      "v_rndne_f32 v101, v99\n"
      "v_sub_f32 v99, v99, v101\n"
      "v_add_f32 v99, v99, v100\n"
      "v_exp_f32 v99, v99\n"
      "ds_write_b128 v22, v[52:55] offset:240\n"
      "v_cvt_i32_f32 v101, v101\n"
      "v_ldexp_f32 v99, v99, v101\n"
      "v_cvt_pk_bf16_f32 v99, v99, v99\n"
      "v_lshlrev_b32 v103, 16, v99\n"
      "v_mul_f32 v104, v103, v102\n"
      "v_mul_f32 v104, 0xbfb8aa3b, v104\n"
      "ds_write_b128 v22, v[56:59] offset:0\n"
      "v_lshlrev_b32 v105, 16, v247\n"
      "v_mul_f32 v105, v105, v102\n"
      "v_mul_f32 v105, 0x3e22f983, v105\n"
      "v_mul_f32 v98, 0x41000000, v104\n"
      "v_mul_f32 v99, 0x41000000, v105\n"
      "v_fract_f32 v100, v99\n"
      "ds_write_b128 v22, v[60:63] offset:128\n"
      "v_exp_f32 v98, v98\n"
      "v_cos_f32 v124, v100\n"
      "v_sin_f32 v125, v100\n"
      "v_mul_f32 v124, v98, v124\n"
      "v_mul_f32 v125, v98, v125\n"
      "v_mov_b32 v122, 1.0\n"
      "ds_read_b64_tr_b16 v[182:183], v23 offset:96\n"
      "ds_read_b64_tr_b16 v[184:185], v23 offset:368\n"
      "ds_read_b64_tr_b16 v[186:187], v23 offset:4448\n"
      "ds_read_b64_tr_b16 v[188:189], v23 offset:4720\n"
      "ds_read_b64_tr_b16 v[190:191], v23 offset:8800\n"
      "ds_read_b64_tr_b16 v[192:193], v23 offset:9072\n"
      "ds_read_b64_tr_b16 v[194:195], v23 offset:13152\n"
      "ds_read_b64_tr_b16 v[196:197], v23 offset:13424\n"
      "v_mov_b32 v123, 0\n"
      "v_mov_b64 v[112:113], v[124:125]\n"
      "v_pk_mul_f32 v[116:117], v[124:125], v[112:113] op_sel:[0,0] op_sel_hi:[0,1]\n"
      "v_pk_fma_f32 v[126:127], v[124:125], v[112:113], v[116:117] op_sel:[1,1,0] op_sel_hi:[1,0,1] neg_lo:[1,0,0]\n"
      "v_pk_mul_f32 v[116:117], v[126:127], v[112:113] op_sel:[0,0] op_sel_hi:[0,1]\n"
      "v_pk_fma_f32 v[128:129], v[126:127], v[112:113], v[116:117] op_sel:[1,1,0] op_sel_hi:[1,0,1] neg_lo:[1,0,0]\n"
      "ds_write_b128 v22, v[64:67] offset:16\n"
      "v_pk_mul_f32 v[116:117], v[128:129], v[112:113] op_sel:[0,0] op_sel_hi:[0,1]\n"
      "v_pk_fma_f32 v[130:131], v[128:129], v[112:113], v[116:117] op_sel:[1,1,0] op_sel_hi:[1,0,1] neg_lo:[1,0,0]\n"
      "v_pk_mul_f32 v[116:117], v[130:131], v[112:113] op_sel:[0,0] op_sel_hi:[0,1]\n"
      "v_pk_fma_f32 v[132:133], v[130:131], v[112:113], v[116:117] op_sel:[1,1,0] op_sel_hi:[1,0,1] neg_lo:[1,0,0]\n"
      "v_pk_mul_f32 v[116:117], v[132:133], v[112:113] op_sel:[0,0] op_sel_hi:[0,1]\n"
      "v_pk_fma_f32 v[134:135], v[132:133], v[112:113], v[116:117] op_sel:[1,1,0] op_sel_hi:[1,0,1] neg_lo:[1,0,0]\n"
      "ds_write_b128 v22, v[68:71] offset:144\n"
      "v_pk_mul_f32 v[116:117], v[134:135], v[112:113] op_sel:[0,0] op_sel_hi:[0,1]\n"
      "v_pk_fma_f32 v[136:137], v[134:135], v[112:113], v[116:117] op_sel:[1,1,0] op_sel_hi:[1,0,1] neg_lo:[1,0,0]\n"
      "v_pk_mul_f32 v[116:117], v[136:137], v[112:113] op_sel:[0,0] op_sel_hi:[0,1]\n"
      "v_pk_fma_f32 v[138:139], v[136:137], v[112:113], v[116:117] op_sel:[1,1,0] op_sel_hi:[1,0,1] neg_lo:[1,0,0]\n"
      "v_cvt_pk_bf16_f32 v40, v122, v124\n"
      "v_cvt_pk_bf16_f32 v41, v126, v128\n"
      "ds_write_b128 v22, v[72:75] offset:32\n"
      "v_cvt_pk_bf16_f32 v42, v130, v132\n"
      "v_cvt_pk_bf16_f32 v43, v134, v136\n"
      "v_cvt_pk_bf16_f32 v44, -v123, -v125\n"
      "v_cvt_pk_bf16_f32 v45, -v127, -v129\n"
      "v_cvt_pk_bf16_f32 v46, -v131, -v133\n"
      "v_cvt_pk_bf16_f32 v47, -v135, -v137\n"
      "ds_write_b128 v22, v[76:79] offset:160\n"
      "v_pk_mul_f32 v[116:117], v[138:139], v[112:113] op_sel:[0,0] op_sel_hi:[0,1]\n"
      "v_pk_fma_f32 v[140:141], v[138:139], v[112:113], v[116:117] op_sel:[1,1,0] op_sel_hi:[1,0,1] neg_lo:[1,0,0]\n"
      "v_pk_mul_f32 v[116:117], v[140:141], v[112:113] op_sel:[0,0] op_sel_hi:[0,1]\n"
      "v_pk_fma_f32 v[142:143], v[140:141], v[112:113], v[116:117] op_sel:[1,1,0] op_sel_hi:[1,0,1] neg_lo:[1,0,0]\n"
      "v_pk_mul_f32 v[116:117], v[142:143], v[112:113] op_sel:[0,0] op_sel_hi:[0,1]\n"
      "v_pk_fma_f32 v[144:145], v[142:143], v[112:113], v[116:117] op_sel:[1,1,0] op_sel_hi:[1,0,1] neg_lo:[1,0,0]\n"
      "ds_read_b64_tr_b16 v[198:199], v23\n"
      "ds_read_b64_tr_b16 v[200:201], v23 offset:272\n"
      "ds_read_b64_tr_b16 v[202:203], v23 offset:4352\n"
      "ds_read_b64_tr_b16 v[204:205], v23 offset:4624\n"
      "ds_read_b64_tr_b16 v[206:207], v23 offset:8704\n"
      "ds_read_b64_tr_b16 v[208:209], v23 offset:8976\n"
      "ds_read_b64_tr_b16 v[210:211], v23 offset:13056\n"
      "ds_read_b64_tr_b16 v[212:213], v23 offset:13328\n"
      "v_pk_mul_f32 v[116:117], v[144:145], v[112:113] op_sel:[0,0] op_sel_hi:[0,1]\n"
      "v_pk_fma_f32 v[146:147], v[144:145], v[112:113], v[116:117] op_sel:[1,1,0] op_sel_hi:[1,0,1] neg_lo:[1,0,0]\n"
      "v_pk_mul_f32 v[116:117], v[146:147], v[112:113] op_sel:[0,0] op_sel_hi:[0,1]\n"
      "v_pk_fma_f32 v[148:149], v[146:147], v[112:113], v[116:117] op_sel:[1,1,0] op_sel_hi:[1,0,1] neg_lo:[1,0,0]\n"
      "v_pk_mul_f32 v[116:117], v[148:149], v[112:113] op_sel:[0,0] op_sel_hi:[0,1]\n"
      "v_pk_fma_f32 v[150:151], v[148:149], v[112:113], v[116:117] op_sel:[1,1,0] op_sel_hi:[1,0,1] neg_lo:[1,0,0]\n"
      "ds_write_b128 v22, v[80:83] offset:48\n"
      "v_pk_mul_f32 v[116:117], v[150:151], v[112:113] op_sel:[0,0] op_sel_hi:[0,1]\n"
      "v_pk_fma_f32 v[152:153], v[150:151], v[112:113], v[116:117] op_sel:[1,1,0] op_sel_hi:[1,0,1] neg_lo:[1,0,0]\n"
      "v_fract_f32 v100, v105\n"
      "v_exp_f32 v98, v104\n"
      "v_cos_f32 v106, v100\n"
      "v_sin_f32 v107, v100\n"
      "ds_write_b128 v22, v[84:87] offset:176\n"
      "v_mul_f32 v106, v98, v106\n"
      "v_mul_f32 v107, v98, v107\n"
      "v_cvt_pk_bf16_f32 v48, v138, v140\n"
      "v_cvt_pk_bf16_f32 v49, v142, v144\n"
      "v_cvt_pk_bf16_f32 v50, v146, v148\n"
      "v_cvt_pk_bf16_f32 v51, v150, v152\n"
      "ds_write_b128 v22, v[88:91] offset:64\n"
      "v_cvt_pk_bf16_f32 v52, -v139, -v141\n"
      "v_cvt_pk_bf16_f32 v53, -v143, -v145\n"
      "v_cvt_pk_bf16_f32 v54, -v147, -v149\n"
      "v_cvt_pk_bf16_f32 v55, -v151, -v153\n"
      "v_mul_f32 v98, 0x43000000, v104\n"
      "v_mul_f32 v99, 0x43000000, v105\n"
      "ds_write_b128 v22, v[92:95] offset:192\n"
      "v_fract_f32 v100, v99\n"
      "v_exp_f32 v98, v98\n"
      "v_cos_f32 v114, v100\n"
      "v_sin_f32 v115, v100\n"
      "v_mul_f32 v114, v98, v114\n"
      "v_mul_f32 v115, v98, v115\n"
      "ds_write_b16 v22, v96 offset:80\n"
      "ds_write_b16_d16_hi v22, v96 offset:208\n"
      "s_waitcnt lgkmcnt(12)\n"
      "v_mfma_f32_16x16x32_bf16 v[158:161], v[198:201], v[182:185], 0\n"
      "s_waitcnt lgkmcnt(10)\n"
      "v_mfma_f32_16x16x32_bf16 v[158:161], v[202:205], v[186:189], v[158:161]\n"
      "s_waitcnt lgkmcnt(8)\n"
      "v_mfma_f32_16x16x32_bf16 v[158:161], v[206:209], v[190:193], v[158:161]\n"
      "s_waitcnt lgkmcnt(6)\n"
      "v_mfma_f32_16x16x32_bf16 v[158:161], v[210:213], v[194:197], v[158:161]\n"
      "ds_read_b64_tr_b16 v[214:215], v23 offset:32\n"
      "ds_read_b64_tr_b16 v[216:217], v23 offset:304\n"
      "ds_read_b64_tr_b16 v[218:219], v23 offset:4384\n"
      "ds_read_b64_tr_b16 v[220:221], v23 offset:4656\n"
      "ds_read_b64_tr_b16 v[222:223], v23 offset:8736\n"
      "ds_read_b64_tr_b16 v[224:225], v23 offset:9008\n"
      "ds_read_b64_tr_b16 v[226:227], v23 offset:13088\n"
      "ds_read_b64_tr_b16 v[228:229], v23 offset:13360\n"
      "v_add_f32 v102, v102, v102\n"
      "v_lshlrev_b32 v122, 16, v248\n"
      "v_lshlrev_b32 v123, 16, v249\n"
      "v_mul_f32 v122, v122, v102\n"
      "v_mul_f32 v123, v123, v102\n"
      "s_waitcnt vmcnt(25)\n"
      "v_lshlrev_b32 v97, 16, v251\n"
      "ds_read_b64_tr_b16 v[198:199], v23 offset:64\n"
      "ds_read_b64_tr_b16 v[200:201], v23 offset:336\n"
      "ds_read_b64_tr_b16 v[202:203], v23 offset:4416\n"
      "ds_read_b64_tr_b16 v[204:205], v23 offset:4688\n"
      "ds_read_b64_tr_b16 v[206:207], v23 offset:8768\n"
      "ds_read_b64_tr_b16 v[208:209], v23 offset:9040\n"
      "ds_read_b64_tr_b16 v[210:211], v23 offset:13120\n"
      "ds_read_b64_tr_b16 v[212:213], v23 offset:13392\n"
      "v_pk_mul_f32 v[116:117], v[122:123], v[114:115] op_sel:[0,0] op_sel_hi:[0,1]\n"
      "v_pk_fma_f32 v[138:139], v[122:123], v[114:115], v[116:117] op_sel:[1,1,0] op_sel_hi:[1,0,1] neg_lo:[1,0,0]\n"
      "v_pk_mul_f32 v[116:117], v[122:123], v[106:107] op_sel:[0,0] op_sel_hi:[0,1]\n"
      "v_pk_fma_f32 v[124:125], v[122:123], v[106:107], v[116:117] op_sel:[1,1,0] op_sel_hi:[1,0,1] neg_lo:[1,0,0]\n"
      "v_pk_mul_f32 v[116:117], v[124:125], v[106:107] op_sel:[0,0] op_sel_hi:[0,1]\n"
      "v_pk_fma_f32 v[126:127], v[124:125], v[106:107], v[116:117] op_sel:[1,1,0] op_sel_hi:[1,0,1] neg_lo:[1,0,0]\n"
      "v_add_u32 v154, s65, v24\n"
      "v_add_u32 v155, s65, v25\n"
      "v_add_u32 v157, s65, v2\n"
      "s_mul_i32 s68, s50, -34816\n"
      "s_add_u32 s68, s68, 17408\n"
      "v_add_u32 v156, s68, v28\n"
      "v_pk_mul_f32 v[116:117], v[126:127], v[106:107] op_sel:[0,0] op_sel_hi:[0,1]\n"
      "v_pk_fma_f32 v[128:129], v[126:127], v[106:107], v[116:117] op_sel:[1,1,0] op_sel_hi:[1,0,1] neg_lo:[1,0,0]\n"
      "v_pk_mul_f32 v[116:117], v[128:129], v[106:107] op_sel:[0,0] op_sel_hi:[0,1]\n"
      "v_pk_fma_f32 v[130:131], v[128:129], v[106:107], v[116:117] op_sel:[1,1,0] op_sel_hi:[1,0,1] neg_lo:[1,0,0]\n"
      "v_pk_mul_f32 v[116:117], v[130:131], v[106:107] op_sel:[0,0] op_sel_hi:[0,1]\n"
      "v_pk_fma_f32 v[132:133], v[130:131], v[106:107], v[116:117] op_sel:[1,1,0] op_sel_hi:[1,0,1] neg_lo:[1,0,0]\n"
      "v_pk_mul_f32 v[116:117], v[132:133], v[106:107] op_sel:[0,0] op_sel_hi:[0,1]\n"
      "v_pk_fma_f32 v[134:135], v[132:133], v[106:107], v[116:117] op_sel:[1,1,0] op_sel_hi:[1,0,1] neg_lo:[1,0,0]\n"
      "v_pk_mul_f32 v[116:117], v[134:135], v[106:107] op_sel:[0,0] op_sel_hi:[0,1]\n"
      "v_pk_fma_f32 v[136:137], v[134:135], v[106:107], v[116:117] op_sel:[1,1,0] op_sel_hi:[1,0,1] neg_lo:[1,0,0]\n"
      "v_cvt_pk_bf16_f32 v56, v122, v124\n"
      "v_cvt_pk_bf16_f32 v57, v126, v128\n"
      "v_cvt_pk_bf16_f32 v58, v130, v132\n"
      "v_cvt_pk_bf16_f32 v59, v134, v136\n"
      "v_cvt_pk_bf16_f32 v60, v123, v125\n"
      "v_cvt_pk_bf16_f32 v61, v127, v129\n"
      "v_cvt_pk_bf16_f32 v62, v131, v133\n"
      "v_cvt_pk_bf16_f32 v63, v135, v137\n"
      "s_waitcnt lgkmcnt(14)\n"
      "v_mfma_f32_16x16x32_bf16 v[162:165], v[214:217], v[182:185], 0\n"
      "s_waitcnt lgkmcnt(12)\n"
      "v_mfma_f32_16x16x32_bf16 v[162:165], v[218:221], v[186:189], v[162:165]\n"
      "s_waitcnt lgkmcnt(10)\n"
      "v_mfma_f32_16x16x32_bf16 v[162:165], v[222:225], v[190:193], v[162:165]\n"
      "s_waitcnt lgkmcnt(8)\n"
      "v_mfma_f32_16x16x32_bf16 v[162:165], v[226:229], v[194:197], v[162:165]\n"
      "s_waitcnt lgkmcnt(6)\n"
      "v_mfma_f32_16x16x32_bf16 v[166:169], v[198:201], v[182:185], 0\n"
      "s_waitcnt lgkmcnt(4)\n"
      "v_mfma_f32_16x16x32_bf16 v[166:169], v[202:205], v[186:189], v[166:169]\n"
      "s_waitcnt lgkmcnt(2)\n"
      "v_mfma_f32_16x16x32_bf16 v[166:169], v[206:209], v[190:193], v[166:169]\n"
      "s_waitcnt lgkmcnt(0)\n"
      "v_mfma_f32_16x16x32_bf16 v[166:169], v[210:213], v[194:197], v[166:169]\n"
      "v_pk_mul_f32 v[116:117], v[138:139], v[114:115] op_sel:[0,0] op_sel_hi:[0,1]\n"
      "v_pk_fma_f32 v[122:123], v[138:139], v[114:115], v[116:117] op_sel:[1,1,0] op_sel_hi:[1,0,1] neg_lo:[1,0,0]\n"
      "v_pk_mul_f32 v[116:117], v[138:139], v[106:107] op_sel:[0,0] op_sel_hi:[0,1]\n"
      "v_pk_fma_f32 v[140:141], v[138:139], v[106:107], v[116:117] op_sel:[1,1,0] op_sel_hi:[1,0,1] neg_lo:[1,0,0]\n"
      "v_pk_mul_f32 v[116:117], v[140:141], v[106:107] op_sel:[0,0] op_sel_hi:[0,1]\n"
      "v_pk_fma_f32 v[142:143], v[140:141], v[106:107], v[116:117] op_sel:[1,1,0] op_sel_hi:[1,0,1] neg_lo:[1,0,0]\n"
      "v_pk_mul_f32 v[116:117], v[142:143], v[106:107] op_sel:[0,0] op_sel_hi:[0,1]\n"
      "v_pk_fma_f32 v[144:145], v[142:143], v[106:107], v[116:117] op_sel:[1,1,0] op_sel_hi:[1,0,1] neg_lo:[1,0,0]\n"
      "v_pk_mul_f32 v[116:117], v[144:145], v[106:107] op_sel:[0,0] op_sel_hi:[0,1]\n"
      "v_pk_fma_f32 v[146:147], v[144:145], v[106:107], v[116:117] op_sel:[1,1,0] op_sel_hi:[1,0,1] neg_lo:[1,0,0]\n"
      "v_pk_mul_f32 v[116:117], v[146:147], v[106:107] op_sel:[0,0] op_sel_hi:[0,1]\n"
      "v_pk_fma_f32 v[148:149], v[146:147], v[106:107], v[116:117] op_sel:[1,1,0] op_sel_hi:[1,0,1] neg_lo:[1,0,0]\n"
      "v_pk_mul_f32 v[116:117], v[148:149], v[106:107] op_sel:[0,0] op_sel_hi:[0,1]\n"
      "v_pk_fma_f32 v[150:151], v[148:149], v[106:107], v[116:117] op_sel:[1,1,0] op_sel_hi:[1,0,1] neg_lo:[1,0,0]\n"
      "v_pk_mul_f32 v[116:117], v[150:151], v[106:107] op_sel:[0,0] op_sel_hi:[0,1]\n"
      "v_pk_fma_f32 v[152:153], v[150:151], v[106:107], v[116:117] op_sel:[1,1,0] op_sel_hi:[1,0,1] neg_lo:[1,0,0]\n"
      "v_pk_mul_f32 v[116:117], v[106:107], v[106:107] op_sel:[0,0] op_sel_hi:[0,1]\n"
      "v_pk_fma_f32 v[108:109], v[106:107], v[106:107], v[116:117] op_sel:[1,1,0] op_sel_hi:[1,0,1] neg_lo:[1,0,0]\n"
      "s_cmp_lg_u32 s50, 0\n"
      "s_cbranch_scc1 .Lkx_24\n"
      "ds_write_b128 v28, v[158:161]\n"
      "ds_write_b128 v28, v[162:165] offset:1024\n"
      "v_mov_b32 v170, 0\n"
      "v_mov_b32 v171, 0\n"
      "v_mov_b32 v172, 0\n"
      "v_mov_b32 v173, 0\n"
      "s_mov_b64 exec, 0x3fffffff\n"
      "ds_write_b128 v157, v[170:173] offset:4096\n"
      "s_mov_b64 exec, -1\n"
      "v_cvt_pk_bf16_f32 v64, v138, v140\n"
      "v_cvt_pk_bf16_f32 v65, v142, v144\n"
      "v_cvt_pk_bf16_f32 v66, v146, v148\n"
      "v_cvt_pk_bf16_f32 v67, v150, v152\n"
      "v_cvt_pk_bf16_f32 v68, v139, v141\n"
      "v_cvt_pk_bf16_f32 v69, v143, v145\n"
      "v_cvt_pk_bf16_f32 v70, v147, v149\n"
      "v_cvt_pk_bf16_f32 v71, v151, v153\n"
      "v_pk_mul_f32 v[116:117], v[122:123], v[114:115] op_sel:[0,0] op_sel_hi:[0,1]\n"
      "v_pk_fma_f32 v[130:131], v[122:123], v[114:115], v[116:117] op_sel:[1,1,0] op_sel_hi:[1,0,1] neg_lo:[1,0,0]\n"
      "v_pk_mul_f32 v[116:117], v[130:131], v[114:115] op_sel:[0,0] op_sel_hi:[0,1]\n"
      "v_pk_fma_f32 v[138:139], v[130:131], v[114:115], v[116:117] op_sel:[1,1,0] op_sel_hi:[1,0,1] neg_lo:[1,0,0]\n"
      "v_pk_mul_f32 v[116:117], v[122:123], v[108:109] op_sel:[0,0] op_sel_hi:[0,1]\n"
      "v_pk_fma_f32 v[124:125], v[122:123], v[108:109], v[116:117] op_sel:[1,1,0] op_sel_hi:[1,0,1] neg_lo:[1,0,0]\n"
      "v_pk_mul_f32 v[116:117], v[130:131], v[108:109] op_sel:[0,0] op_sel_hi:[0,1]\n"
      "v_pk_fma_f32 v[132:133], v[130:131], v[108:109], v[116:117] op_sel:[1,1,0] op_sel_hi:[1,0,1] neg_lo:[1,0,0]\n"
      "v_pk_mul_f32 v[116:117], v[124:125], v[108:109] op_sel:[0,0] op_sel_hi:[0,1]\n"
      "v_pk_fma_f32 v[126:127], v[124:125], v[108:109], v[116:117] op_sel:[1,1,0] op_sel_hi:[1,0,1] neg_lo:[1,0,0]\n"
      "v_pk_mul_f32 v[116:117], v[132:133], v[108:109] op_sel:[0,0] op_sel_hi:[0,1]\n"
      "v_pk_fma_f32 v[134:135], v[132:133], v[108:109], v[116:117] op_sel:[1,1,0] op_sel_hi:[1,0,1] neg_lo:[1,0,0]\n"
      "v_pk_mul_f32 v[116:117], v[126:127], v[108:109] op_sel:[0,0] op_sel_hi:[0,1]\n"
      "v_pk_fma_f32 v[128:129], v[126:127], v[108:109], v[116:117] op_sel:[1,1,0] op_sel_hi:[1,0,1] neg_lo:[1,0,0]\n"
      "v_pk_mul_f32 v[116:117], v[134:135], v[108:109] op_sel:[0,0] op_sel_hi:[0,1]\n"
      "v_pk_fma_f32 v[136:137], v[134:135], v[108:109], v[116:117] op_sel:[1,1,0] op_sel_hi:[1,0,1] neg_lo:[1,0,0]\n"
      "s_waitcnt lgkmcnt(0)\n"
      "s_barrier\n"
      "ds_read_b128 v[170:173], v156\n"
      "v_pk_mul_f32 v[116:117], v[108:109], v[108:109] op_sel:[0,0] op_sel_hi:[0,1]\n"
      "v_pk_fma_f32 v[110:111], v[108:109], v[108:109], v[116:117] op_sel:[1,1,0] op_sel_hi:[1,0,1] neg_lo:[1,0,0]\n"
      "v_cvt_pk_bf16_f32 v72, v122, v124\n"
      "v_cvt_pk_bf16_f32 v73, v126, v128\n"
      "v_cvt_pk_bf16_f32 v74, v130, v132\n"
      "v_cvt_pk_bf16_f32 v75, v134, v136\n"
      "v_cvt_pk_bf16_f32 v76, v123, v125\n"
      "v_cvt_pk_bf16_f32 v77, v127, v129\n"
      "v_cvt_pk_bf16_f32 v78, v131, v133\n"
      "v_cvt_pk_bf16_f32 v79, v135, v137\n"
      "v_pk_mul_f32 v[116:117], v[138:139], v[114:115] op_sel:[0,0] op_sel_hi:[0,1]\n"
      "v_pk_fma_f32 v[142:143], v[138:139], v[114:115], v[116:117] op_sel:[1,1,0] op_sel_hi:[1,0,1] neg_lo:[1,0,0]\n"
      "v_pk_mul_f32 v[116:117], v[142:143], v[114:115] op_sel:[0,0] op_sel_hi:[0,1]\n"
      "v_pk_fma_f32 v[146:147], v[142:143], v[114:115], v[116:117] op_sel:[1,1,0] op_sel_hi:[1,0,1] neg_lo:[1,0,0]\n"
      "v_pk_mul_f32 v[116:117], v[146:147], v[114:115] op_sel:[0,0] op_sel_hi:[0,1]\n"
      "v_pk_fma_f32 v[150:151], v[146:147], v[114:115], v[116:117] op_sel:[1,1,0] op_sel_hi:[1,0,1] neg_lo:[1,0,0]\n"
      "v_pk_mul_f32 v[116:117], v[150:151], v[114:115] op_sel:[0,0] op_sel_hi:[0,1]\n"
      "v_pk_fma_f32 v[122:123], v[150:151], v[114:115], v[116:117] op_sel:[1,1,0] op_sel_hi:[1,0,1] neg_lo:[1,0,0]\n"
      "v_pk_mul_f32 v[116:117], v[138:139], v[110:111] op_sel:[0,0] op_sel_hi:[0,1]\n"
      "v_pk_fma_f32 v[140:141], v[138:139], v[110:111], v[116:117] op_sel:[1,1,0] op_sel_hi:[1,0,1] neg_lo:[1,0,0]\n"
      "v_pk_mul_f32 v[116:117], v[142:143], v[110:111] op_sel:[0,0] op_sel_hi:[0,1]\n"
      "v_pk_fma_f32 v[144:145], v[142:143], v[110:111], v[116:117] op_sel:[1,1,0] op_sel_hi:[1,0,1] neg_lo:[1,0,0]\n"
      "v_pk_mul_f32 v[116:117], v[146:147], v[110:111] op_sel:[0,0] op_sel_hi:[0,1]\n"
      "v_pk_fma_f32 v[148:149], v[146:147], v[110:111], v[116:117] op_sel:[1,1,0] op_sel_hi:[1,0,1] neg_lo:[1,0,0]\n"
      "v_pk_mul_f32 v[116:117], v[150:151], v[110:111] op_sel:[0,0] op_sel_hi:[0,1]\n"
      "v_pk_fma_f32 v[152:153], v[150:151], v[110:111], v[116:117] op_sel:[1,1,0] op_sel_hi:[1,0,1] neg_lo:[1,0,0]\n"
      "v_cvt_pk_bf16_f32 v80, v138, v140\n"
      "v_cvt_pk_bf16_f32 v81, v142, v144\n"
      "v_cvt_pk_bf16_f32 v82, v146, v148\n"
      "v_cvt_pk_bf16_f32 v83, v150, v152\n"
      "v_cvt_pk_bf16_f32 v84, v139, v141\n"
      "v_cvt_pk_bf16_f32 v85, v143, v145\n"
      "v_cvt_pk_bf16_f32 v86, v147, v149\n"
      "v_cvt_pk_bf16_f32 v87, v151, v153\n"
      "v_pk_mul_f32 v[116:117], v[122:123], v[114:115] op_sel:[0,0] op_sel_hi:[0,1]\n"
      "v_pk_fma_f32 v[124:125], v[122:123], v[114:115], v[116:117] op_sel:[1,1,0] op_sel_hi:[1,0,1] neg_lo:[1,0,0]\n"
      "s_waitcnt lgkmcnt(0)\n"
      "v_add_f32 v166, v166, v170\n"
      "v_add_f32 v167, v167, v171\n"
      "v_add_f32 v168, v168, v172\n"
      "v_add_f32 v169, v169, v173\n"
      "v_cvt_pk_bf16_f32 v170, v166, v166\n"
      "v_mov_b32 v171, v170\n"
      "v_cvt_pk_bf16_f32 v172, v167, v167\n"
      "v_mov_b32 v173, v172\n"
      "v_cvt_pk_bf16_f32 v174, v168, v168\n"
      "v_mov_b32 v175, v174\n"
      "v_cvt_pk_bf16_f32 v176, v169, v169\n"
      "v_mov_b32 v177, v176\n"
      "s_mov_b32 exec_hi, 0\n"
      "ds_write_b64 v154, v[170:171] offset:776\n"
      "ds_write_b64 v154, v[172:173] offset:520\n"
      "ds_write_b64 v154, v[174:175] offset:264\n"
      "ds_write_b64 v154, v[176:177] offset:8\n"
      "ds_write_b64 v154, v[172:173] offset:528\n"
      "ds_write_b64 v154, v[174:175] offset:272\n"
      "ds_write_b64 v154, v[176:177] offset:16\n"
      "s_mov_b32 exec_lo, 0xfffffffe\n"
      "s_mov_b32 exec_hi, 1\n"
      "ds_write_b64 v154, v[170:171] offset:784\n"
      "s_mov_b64 exec, 1\n"
      "ds_write_b32 v154, v170 offset:784\n"
      "s_mov_b32 exec_lo, 0x55555555\n"
      "ds_write_b16 v154, v170 offset:774\n"
      "ds_write_b16 v154, v172 offset:518\n"
      "ds_write_b16 v154, v174 offset:262\n"
      "ds_write_b16 v154, v176 offset:6\n"
      "s_mov_b64 exec, -1\n"
      "v_pk_mul_f32 v[116:117], v[124:125], v[114:115] op_sel:[0,0] op_sel_hi:[0,1]\n"
      "v_pk_fma_f32 v[126:127], v[124:125], v[114:115], v[116:117] op_sel:[1,1,0] op_sel_hi:[1,0,1] neg_lo:[1,0,0]\n"
      "v_pk_mul_f32 v[116:117], v[126:127], v[114:115] op_sel:[0,0] op_sel_hi:[0,1]\n"
      "v_pk_fma_f32 v[128:129], v[126:127], v[114:115], v[116:117] op_sel:[1,1,0] op_sel_hi:[1,0,1] neg_lo:[1,0,0]\n"
      "v_pk_mul_f32 v[116:117], v[128:129], v[114:115] op_sel:[0,0] op_sel_hi:[0,1]\n"
      "v_pk_fma_f32 v[130:131], v[128:129], v[114:115], v[116:117] op_sel:[1,1,0] op_sel_hi:[1,0,1] neg_lo:[1,0,0]\n"
      "v_pk_mul_f32 v[116:117], v[130:131], v[114:115] op_sel:[0,0] op_sel_hi:[0,1]\n"
      "v_pk_fma_f32 v[132:133], v[130:131], v[114:115], v[116:117] op_sel:[1,1,0] op_sel_hi:[1,0,1] neg_lo:[1,0,0]\n"
      "v_pk_mul_f32 v[116:117], v[132:133], v[114:115] op_sel:[0,0] op_sel_hi:[0,1]\n"
      "v_pk_fma_f32 v[134:135], v[132:133], v[114:115], v[116:117] op_sel:[1,1,0] op_sel_hi:[1,0,1] neg_lo:[1,0,0]\n"
      "v_pk_mul_f32 v[116:117], v[134:135], v[114:115] op_sel:[0,0] op_sel_hi:[0,1]\n"
      "v_pk_fma_f32 v[136:137], v[134:135], v[114:115], v[116:117] op_sel:[1,1,0] op_sel_hi:[1,0,1] neg_lo:[1,0,0]\n"
      "v_pk_mul_f32 v[116:117], v[136:137], v[114:115] op_sel:[0,0] op_sel_hi:[0,1]\n"
      "v_pk_fma_f32 v[138:139], v[136:137], v[114:115], v[116:117] op_sel:[1,1,0] op_sel_hi:[1,0,1] neg_lo:[1,0,0]\n"
      "v_cvt_pk_bf16_f32 v88, v122, v124\n"
      "v_cvt_pk_bf16_f32 v89, v126, v128\n"
      "v_cvt_pk_bf16_f32 v90, v130, v132\n"
      "v_cvt_pk_bf16_f32 v91, v134, v136\n"
      "v_cvt_pk_bf16_f32 v92, v123, v125\n"
      "v_cvt_pk_bf16_f32 v93, v127, v129\n"
      "v_cvt_pk_bf16_f32 v94, v131, v133\n"
      "v_cvt_pk_bf16_f32 v95, v135, v137\n"
      "v_cvt_pk_bf16_f32 v96, v138, v139\n"
      "s_nop 1\n"
      "s_branch .Lkx_25\n"
      ".Lkx_24:\n"
      "s_mov_b64 exec, 1\n"
      "v_add_f32 v158, v158, v181\n"
      "s_mov_b64 exec, -1\n"
      "ds_write_b128 v28, v[166:169]\n"
      "v_cvt_pk_bf16_f32 v64, v138, v140\n"
      "v_cvt_pk_bf16_f32 v65, v142, v144\n"
      "v_cvt_pk_bf16_f32 v66, v146, v148\n"
      "v_cvt_pk_bf16_f32 v67, v150, v152\n"
      "v_cvt_pk_bf16_f32 v68, v139, v141\n"
      "v_cvt_pk_bf16_f32 v69, v143, v145\n"
      "v_cvt_pk_bf16_f32 v70, v147, v149\n"
      "v_cvt_pk_bf16_f32 v71, v151, v153\n"
      "v_pk_mul_f32 v[116:117], v[122:123], v[114:115] op_sel:[0,0] op_sel_hi:[0,1]\n"
      "v_pk_fma_f32 v[130:131], v[122:123], v[114:115], v[116:117] op_sel:[1,1,0] op_sel_hi:[1,0,1] neg_lo:[1,0,0]\n"
      "v_pk_mul_f32 v[116:117], v[130:131], v[114:115] op_sel:[0,0] op_sel_hi:[0,1]\n"
      "v_pk_fma_f32 v[138:139], v[130:131], v[114:115], v[116:117] op_sel:[1,1,0] op_sel_hi:[1,0,1] neg_lo:[1,0,0]\n"
      "v_pk_mul_f32 v[116:117], v[122:123], v[108:109] op_sel:[0,0] op_sel_hi:[0,1]\n"
      "v_pk_fma_f32 v[124:125], v[122:123], v[108:109], v[116:117] op_sel:[1,1,0] op_sel_hi:[1,0,1] neg_lo:[1,0,0]\n"
      "v_pk_mul_f32 v[116:117], v[130:131], v[108:109] op_sel:[0,0] op_sel_hi:[0,1]\n"
      "v_pk_fma_f32 v[132:133], v[130:131], v[108:109], v[116:117] op_sel:[1,1,0] op_sel_hi:[1,0,1] neg_lo:[1,0,0]\n"
      "v_pk_mul_f32 v[116:117], v[124:125], v[108:109] op_sel:[0,0] op_sel_hi:[0,1]\n"
      "v_pk_fma_f32 v[126:127], v[124:125], v[108:109], v[116:117] op_sel:[1,1,0] op_sel_hi:[1,0,1] neg_lo:[1,0,0]\n"
      "v_pk_mul_f32 v[116:117], v[132:133], v[108:109] op_sel:[0,0] op_sel_hi:[0,1]\n"
      "v_pk_fma_f32 v[134:135], v[132:133], v[108:109], v[116:117] op_sel:[1,1,0] op_sel_hi:[1,0,1] neg_lo:[1,0,0]\n"
      "v_pk_mul_f32 v[116:117], v[126:127], v[108:109] op_sel:[0,0] op_sel_hi:[0,1]\n"
      "v_pk_fma_f32 v[128:129], v[126:127], v[108:109], v[116:117] op_sel:[1,1,0] op_sel_hi:[1,0,1] neg_lo:[1,0,0]\n"
      "v_pk_mul_f32 v[116:117], v[134:135], v[108:109] op_sel:[0,0] op_sel_hi:[0,1]\n"
      "v_pk_fma_f32 v[136:137], v[134:135], v[108:109], v[116:117] op_sel:[1,1,0] op_sel_hi:[1,0,1] neg_lo:[1,0,0]\n"
      "s_waitcnt lgkmcnt(0)\n"
      "s_barrier\n"
      "ds_read_b128 v[174:177], v156 offset:1024\n"
      "ds_read_b128 v[170:173], v156\n"
      "v_pk_mul_f32 v[116:117], v[108:109], v[108:109] op_sel:[0,0] op_sel_hi:[0,1]\n"
      "v_pk_fma_f32 v[110:111], v[108:109], v[108:109], v[116:117] op_sel:[1,1,0] op_sel_hi:[1,0,1] neg_lo:[1,0,0]\n"
      "v_cvt_pk_bf16_f32 v72, v122, v124\n"
      "v_cvt_pk_bf16_f32 v73, v126, v128\n"
      "v_cvt_pk_bf16_f32 v74, v130, v132\n"
      "v_cvt_pk_bf16_f32 v75, v134, v136\n"
      "v_cvt_pk_bf16_f32 v76, v123, v125\n"
      "v_cvt_pk_bf16_f32 v77, v127, v129\n"
      "v_cvt_pk_bf16_f32 v78, v131, v133\n"
      "v_cvt_pk_bf16_f32 v79, v135, v137\n"
      "v_pk_mul_f32 v[116:117], v[138:139], v[114:115] op_sel:[0,0] op_sel_hi:[0,1]\n"
      "v_pk_fma_f32 v[142:143], v[138:139], v[114:115], v[116:117] op_sel:[1,1,0] op_sel_hi:[1,0,1] neg_lo:[1,0,0]\n"
      "v_pk_mul_f32 v[116:117], v[142:143], v[114:115] op_sel:[0,0] op_sel_hi:[0,1]\n"
      "v_pk_fma_f32 v[146:147], v[142:143], v[114:115], v[116:117] op_sel:[1,1,0] op_sel_hi:[1,0,1] neg_lo:[1,0,0]\n"
      "v_pk_mul_f32 v[116:117], v[146:147], v[114:115] op_sel:[0,0] op_sel_hi:[0,1]\n"
      "v_pk_fma_f32 v[150:151], v[146:147], v[114:115], v[116:117] op_sel:[1,1,0] op_sel_hi:[1,0,1] neg_lo:[1,0,0]\n"
      "v_pk_mul_f32 v[116:117], v[150:151], v[114:115] op_sel:[0,0] op_sel_hi:[0,1]\n"
      "v_pk_fma_f32 v[122:123], v[150:151], v[114:115], v[116:117] op_sel:[1,1,0] op_sel_hi:[1,0,1] neg_lo:[1,0,0]\n"
      "v_pk_mul_f32 v[116:117], v[138:139], v[110:111] op_sel:[0,0] op_sel_hi:[0,1]\n"
      "v_pk_fma_f32 v[140:141], v[138:139], v[110:111], v[116:117] op_sel:[1,1,0] op_sel_hi:[1,0,1] neg_lo:[1,0,0]\n"
      "v_pk_mul_f32 v[116:117], v[142:143], v[110:111] op_sel:[0,0] op_sel_hi:[0,1]\n"
      "v_pk_fma_f32 v[144:145], v[142:143], v[110:111], v[116:117] op_sel:[1,1,0] op_sel_hi:[1,0,1] neg_lo:[1,0,0]\n"
      "v_pk_mul_f32 v[116:117], v[146:147], v[110:111] op_sel:[0,0] op_sel_hi:[0,1]\n"
      "v_pk_fma_f32 v[148:149], v[146:147], v[110:111], v[116:117] op_sel:[1,1,0] op_sel_hi:[1,0,1] neg_lo:[1,0,0]\n"
      "v_pk_mul_f32 v[116:117], v[150:151], v[110:111] op_sel:[0,0] op_sel_hi:[0,1]\n"
      "v_pk_fma_f32 v[152:153], v[150:151], v[110:111], v[116:117] op_sel:[1,1,0] op_sel_hi:[1,0,1] neg_lo:[1,0,0]\n"
      "v_cvt_pk_bf16_f32 v80, v138, v140\n"
      "v_cvt_pk_bf16_f32 v81, v142, v144\n"
      "v_cvt_pk_bf16_f32 v82, v146, v148\n"
      "v_cvt_pk_bf16_f32 v83, v150, v152\n"
      "v_cvt_pk_bf16_f32 v84, v139, v141\n"
      "v_cvt_pk_bf16_f32 v85, v143, v145\n"
      "v_cvt_pk_bf16_f32 v86, v147, v149\n"
      "v_cvt_pk_bf16_f32 v87, v151, v153\n"
      "v_pk_mul_f32 v[116:117], v[122:123], v[114:115] op_sel:[0,0] op_sel_hi:[0,1]\n"
      "v_pk_fma_f32 v[124:125], v[122:123], v[114:115], v[116:117] op_sel:[1,1,0] op_sel_hi:[1,0,1] neg_lo:[1,0,0]\n"
      "s_waitcnt lgkmcnt(1)\n"
      "v_add_f32 v162, v162, v174\n"
      "v_add_f32 v163, v163, v175\n"
      "v_add_f32 v164, v164, v176\n"
      "v_add_f32 v165, v165, v177\n"
      "s_waitcnt lgkmcnt(0)\n"
      "v_add_f32 v158, v158, v170\n"
      "v_add_f32 v159, v159, v171\n"
      "v_add_f32 v160, v160, v172\n"
      "v_add_f32 v161, v161, v173\n"
      "v_cvt_pk_bf16_f32 v170, v163, v162\n"
      "v_cvt_pk_bf16_f32 v171, v162, v162\n"
      "v_cvt_pk_bf16_f32 v172, v163, v163\n"
      "v_cvt_pk_bf16_f32 v174, v165, v164\n"
      "v_cvt_pk_bf16_f32 v175, v164, v164\n"
      "v_cvt_pk_bf16_f32 v176, v165, v165\n"
      "s_mov_b32 exec_lo, 0\n"
      "ds_write_b32 v154, v171 offset:272\n"
      "ds_write_b64 v154, v[170:171] offset:264\n"
      "ds_write_b32 v154, v172 offset:260\n"
      "ds_write_b32 v154, v175 offset:16\n"
      "ds_write_b64 v154, v[174:175] offset:8\n"
      "ds_write_b32 v154, v176 offset:4\n"
      "s_mov_b32 exec_lo, -1\n"
      "v_cvt_pk_bf16_f32 v178, v164, v163\n"
      "v_mov_b32 v179, v171\n"
      "v_cvt_pk_bf16_f32 v180, v161, v160\n"
      "v_cvt_pk_bf16_f32 v181, v159, v158\n"
      "s_mov_b32 exec_hi, 0\n"
      "ds_write_b16 v154, v171 offset:16\n"
      "ds_write_b64 v154, v[178:179] offset:8\n"
      "ds_write_b32 v154, v175 offset:4\n"
      "ds_write_b16 v154, v176 offset:2\n"
      "s_mov_b64 exec, -1\n"
      "ds_write_b64 v155, v[180:181]\n"
      "v_pk_mul_f32 v[116:117], v[124:125], v[114:115] op_sel:[0,0] op_sel_hi:[0,1]\n"
      "v_pk_fma_f32 v[126:127], v[124:125], v[114:115], v[116:117] op_sel:[1,1,0] op_sel_hi:[1,0,1] neg_lo:[1,0,0]\n"
      "v_pk_mul_f32 v[116:117], v[126:127], v[114:115] op_sel:[0,0] op_sel_hi:[0,1]\n"
      "v_pk_fma_f32 v[128:129], v[126:127], v[114:115], v[116:117] op_sel:[1,1,0] op_sel_hi:[1,0,1] neg_lo:[1,0,0]\n"
      "v_pk_mul_f32 v[116:117], v[128:129], v[114:115] op_sel:[0,0] op_sel_hi:[0,1]\n"
      "v_pk_fma_f32 v[130:131], v[128:129], v[114:115], v[116:117] op_sel:[1,1,0] op_sel_hi:[1,0,1] neg_lo:[1,0,0]\n"
      "v_pk_mul_f32 v[116:117], v[130:131], v[114:115] op_sel:[0,0] op_sel_hi:[0,1]\n"
      "v_pk_fma_f32 v[132:133], v[130:131], v[114:115], v[116:117] op_sel:[1,1,0] op_sel_hi:[1,0,1] neg_lo:[1,0,0]\n"
      "v_pk_mul_f32 v[116:117], v[132:133], v[114:115] op_sel:[0,0] op_sel_hi:[0,1]\n"
      "v_pk_fma_f32 v[134:135], v[132:133], v[114:115], v[116:117] op_sel:[1,1,0] op_sel_hi:[1,0,1] neg_lo:[1,0,0]\n"
      "v_pk_mul_f32 v[116:117], v[134:135], v[114:115] op_sel:[0,0] op_sel_hi:[0,1]\n"
      "v_pk_fma_f32 v[136:137], v[134:135], v[114:115], v[116:117] op_sel:[1,1,0] op_sel_hi:[1,0,1] neg_lo:[1,0,0]\n"
      "v_pk_mul_f32 v[116:117], v[136:137], v[114:115] op_sel:[0,0] op_sel_hi:[0,1]\n"
      "v_pk_fma_f32 v[138:139], v[136:137], v[114:115], v[116:117] op_sel:[1,1,0] op_sel_hi:[1,0,1] neg_lo:[1,0,0]\n"
      "v_cvt_pk_bf16_f32 v88, v122, v124\n"
      "v_cvt_pk_bf16_f32 v89, v126, v128\n"
      "v_cvt_pk_bf16_f32 v90, v130, v132\n"
      "v_cvt_pk_bf16_f32 v91, v134, v136\n"
      "v_cvt_pk_bf16_f32 v92, v123, v125\n"
      "v_cvt_pk_bf16_f32 v93, v127, v129\n"
      "v_cvt_pk_bf16_f32 v94, v131, v133\n"
      "v_cvt_pk_bf16_f32 v95, v135, v137\n"
      "v_cvt_pk_bf16_f32 v96, v138, v139\n"
      "s_nop 1\n"
      ".Lkx_25:\n"
      "s_waitcnt lgkmcnt(0)\n"
      "s_barrier\n"
      "s_add_u32 s65, s65, 9216\n"
      "ds_read_b128 v[232:235], v31 offset:9216\n"
      "ds_read_b128 v[236:239], v31 offset:13312\n"
      "ds_read_b128 v[32:35], v31 offset:17408\n"
      "s_waitcnt lgkmcnt(2)\n"
      "buffer_store_dwordx4 v[232:235], v2, s[16:19], s53 offen sc1\n"
      "s_waitcnt lgkmcnt(1)\n"
      "buffer_store_dwordx4 v[236:239], v3, s[16:19], s53 offen sc1\n"
      "s_add_u32 s98, s53, 8192\n"
      "s_cmp_lg_u32 s43, 0\n"
      "s_cselect_b32 s98, 2146435072, s98\n"
      "s_waitcnt lgkmcnt(0)\n"
      "buffer_store_dwordx4 v[32:35], v2, s[16:19], s98 offen sc1\n"
      "s_add_u32 s53, s53, 9216\n"
      "v_mov_b32 v181, v97\n"
      "ds_write_b128 v22, v[40:43] offset:96\n"
      "ds_write_b128 v22, v[44:47] offset:224\n"
      "ds_write_b128 v22, v[48:51] offset:112\n"
      "ds_write_b128 v22, v[52:55] offset:240\n"
      "ds_write_b128 v22, v[56:59] offset:0\n"
      "ds_write_b128 v22, v[60:63] offset:128\n"
      "ds_read_b64_tr_b16 v[182:183], v23 offset:96\n"
      "ds_read_b64_tr_b16 v[184:185], v23 offset:368\n"
      "ds_read_b64_tr_b16 v[186:187], v23 offset:4448\n"
      "ds_read_b64_tr_b16 v[188:189], v23 offset:4720\n"
      "ds_read_b64_tr_b16 v[190:191], v23 offset:8800\n"
      "ds_read_b64_tr_b16 v[192:193], v23 offset:9072\n"
      "ds_read_b64_tr_b16 v[194:195], v23 offset:13152\n"
      "ds_read_b64_tr_b16 v[196:197], v23 offset:13424\n"
      "ds_write_b128 v22, v[64:67] offset:16\n"
      "ds_write_b128 v22, v[68:71] offset:144\n"
      "ds_write_b128 v22, v[72:75] offset:32\n"
      "ds_write_b128 v22, v[76:79] offset:160\n"
      "ds_read_b64_tr_b16 v[198:199], v23\n"
      "ds_read_b64_tr_b16 v[200:201], v23 offset:272\n"
      "ds_read_b64_tr_b16 v[202:203], v23 offset:4352\n"
      "ds_read_b64_tr_b16 v[204:205], v23 offset:4624\n"
      "ds_read_b64_tr_b16 v[206:207], v23 offset:8704\n"
      "ds_read_b64_tr_b16 v[208:209], v23 offset:8976\n"
      "ds_read_b64_tr_b16 v[210:211], v23 offset:13056\n"
      "ds_read_b64_tr_b16 v[212:213], v23 offset:13328\n"
      "ds_write_b128 v22, v[80:83] offset:48\n"
      "ds_write_b128 v22, v[84:87] offset:176\n"
      "ds_write_b128 v22, v[88:91] offset:64\n"
      "ds_write_b128 v22, v[92:95] offset:192\n"
      "ds_write_b16 v22, v96 offset:80\n"
      "ds_write_b16_d16_hi v22, v96 offset:208\n"
      "s_mul_i32 s68, s47, 5\n"
      "s_lshr_b32 s68, s68, 2\n"
      "s_mul_i32 s68, s68, 27\n"
      "v_min_u32 v40, 53, v1\n"
      "v_add_u32 v40, s68, v40\n"
      "v_bfe_u32 v41, v40, 3, 2\n"
      "v_bfe_u32 v42, v40, 1, 2\n"
      "v_mad_u32_u24 v41, v41, 7, v42\n"
      "v_lshrrev_b32 v42, 5, v40\n"
      "v_lshlrev_b32 v41, 3, v41\n"
      "v_lshl_add_u32 v41, v42, 1, v41\n"
      "v_add_u32 v41, 25, v41\n"
      "v_add_u32 v43, -128, v40\n"
      "v_mul_u32_u24 v42, 205, v43\n"
      "v_lshrrev_b32 v42, 11, v42\n"
      "v_mul_u32_u24 v47, 10, v42\n"
      "v_sub_u32 v43, v43, v47\n"
      "v_and_b32 v47, 3, v42\n"
      "v_mad_u32_u24 v47, v47, 7, v43\n"
      "v_lshlrev_b32 v47, 3, v47\n"
      "v_lshrrev_b32 v42, 2, v42\n"
      "v_lshl_add_u32 v42, v42, 1, v47\n"
      "v_add_u32 v47, -55, v42\n"
      "v_cmp_lt_u32 vcc, 6, v43\n"
      "s_nop 1\n"
      "v_cndmask_b32 v42, v42, v47, vcc\n"
      "v_cmp_gt_u32 vcc, 128, v40\n"
      "s_nop 1\n"
      "v_cndmask_b32 v40, v42, v41, vcc\n"
      "v_lshlrev_b32 v18, 7, v40\n"
      "s_sub_u32 s68, s63, s51\n"
      "s_mul_i32 s69, s44, 80\n"
      "s_min_u32 s70, s45, 23\n"
      "s_add_u32 s69, s69, s70\n"
      "s_add_u32 s69, s69, 56\n"
      "s_lshr_b32 s68, s68, 6\n"
      "s_lshr_b32 s69, s69, 6\n"
      "v_mov_b32 v40, s68\n"
      "v_mov_b32 v41, s69\n"
      "v_and_b32 v42, 1, v5\n"
      "v_cmp_eq_u32 vcc, 0, v42\n"
      "s_nop 1\n"
      "v_cndmask_b32 v40, v41, v40, vcc\n"
      "v_and_b32 v41, 3, v1\n"
      "v_mul_u32_u24 v42, 2, v41\n"
      "v_sub_u32 v40, v40, v42\n"
      "v_add_u32 v42, 10, v40\n"
      "v_cmp_gt_i32 vcc, 0, v40\n"
      "s_nop 1\n"
      "v_cndmask_b32 v40, v40, v42, vcc\n"
      "v_mad_u32_u24 v41, v41, 7, v40\n"
      "v_lshlrev_b32 v41, 3, v41\n"
      "v_add_u32 v42, -55, v41\n"
      "v_cmp_lt_u32 vcc, 6, v40\n"
      "s_nop 1\n"
      "v_cndmask_b32 v41, v41, v42, vcc\n"
      "v_bfe_u32 v42, v1, 2, 2\n"
      "v_lshl_add_u32 v41, v42, 1, v41\n"
      "v_lshlrev_b32 v41, 7, v41\n"
      "v_add_u32 v19, 0, v41\n"
      "v_min_u32 v40, 13, v1\n"
      "v_lshlrev_b32 v41, 3, v40\n"
      "v_add_u32 v42, -55, v41\n"
      "v_cmp_lt_u32 vcc, 6, v40\n"
      "s_nop 1\n"
      "v_cndmask_b32 v41, v41, v42, vcc\n"
      "s_bfe_u32 s69, s46, 0x20001\n"
      "s_mul_i32 s69, s69, 56\n"
      "s_and_b32 s70, s44, 6\n"
      "s_add_u32 s69, s69, s70\n"
      "s_lshl_b32 s69, s69, 7\n"
      "s_add_u32 s69, s69, 32768\n"
      "v_lshl_add_u32 v20, v41, 7, s69\n"
      "v_and_b32 v40, 31, v1\n"
      "v_lshl_add_u32 v40, v40, 3, s47\n"
      "v_lshlrev_b32 v40, 7, v40\n"
      "v_add_u32 v21, 65536, v40\n"
      "v_add_u32 v41, 1, v4\n"
      "v_and_b32 v42, 3, v41\n"
      "v_mul_u32_u24 v42, 5184, v42\n"
      "v_lshrrev_b32 v41, 2, v41\n"
      "v_lshl_add_u32 v42, v41, 3, v42\n"
      "v_lshl_add_u32 v42, v5, 4, v42\n"
      "s_mul_i32 s69, s51, 20736\n"
      "s_lshl_b32 s70, s50, 8\n"
      "s_add_u32 s68, s69, s70\n"
      "v_add_u32 v26, s68, v42\n"
      "v_lshlrev_b32 v41, 5, v4\n"
      "v_lshlrev_b32 v42, 4, v5\n"
      "v_sub_u32 v42, v42, v41\n"
      "s_add_u32 s70, s70, 480\n"
      "v_add_u32 v27, s70, v42\n"
      "s_lshl_b32 s68, s43, 12\n"
      "s_add_u32 s68, s68, 98304\n"
      "v_add_u32 v29, s68, v2\n"
      "v_and_b32 v42, 1, v5\n"
      "v_lshl_add_u32 v41, v42, 9, v41\n"
      "v_lshrrev_b32 v42, 1, v5\n"
      "v_lshl_add_u32 v41, v42, 4, v41\n"
      "s_lshl_b32 s68, s50, 11\n"
      "v_add_u32 v30, s68, v41\n"
      "s_mul_i32 s68, s43, 14\n"
      "s_min_u32 s68, s68, 41\n"
      "s_cmp_ge_u32 s43, 2\n"
      "s_cselect_b32 s61, 13, 99\n"
      "s_and_b32 s69, s46, 7\n"
      "s_lshl_b32 s60, s69, 1\n"
      "s_sub_u32 s69, s60, 14\n"
      "s_min_u32 s60, s60, s69\n"
      "s_mov_b32 s60, 0\n"
      "s_mul_i32 s69, s68, 40\n"
      "s_mul_i32 s70, s47, 5\n"
      "s_add_u32 s69, s69, s70\n"
      "s_lshl_b32 s69, s69, 10\n"
      "s_add_u32 s64, s69, 3145728\n"
      "s_add_u32 s69, s43, 1\n"
      "s_and_b32 s69, s69, 3\n"
      "s_lshl_b32 s70, s46, 2\n"
      "s_add_u32 s69, s69, s70\n"
      "s_mul_i32 s69, s69, 54\n"
      "s_add_u32 s69, s69, s68\n"
      "s_lshl_b32 s69, s69, 10\n"
      "s_add_u32 s84, s69, 9437184\n"
      "s_add_u32 s69, s43, 2\n"
      "s_and_b32 s69, s69, 3\n"
      "s_lshl_b32 s70, s46, 2\n"
      "s_add_u32 s69, s69, s70\n"
      "s_mul_i32 s69, s69, 54\n"
      "s_add_u32 s69, s69, s68\n"
      "s_lshl_b32 s69, s69, 10\n"
      "s_add_u32 s85, s69, 9437184\n"
      "s_add_u32 s69, s43, 3\n"
      "s_and_b32 s69, s69, 3\n"
      "s_lshl_b32 s70, s46, 2\n"
      "s_add_u32 s69, s69, s70\n"
      "s_mul_i32 s69, s69, 54\n"
      "s_add_u32 s69, s69, s68\n"
      "s_lshl_b32 s69, s69, 10\n"
      "s_add_u32 s86, s69, 9437184\n"
      "s_add_u32 s69, s43, 4\n"
      "s_and_b32 s69, s69, 3\n"
      "s_lshl_b32 s70, s46, 2\n"
      "s_add_u32 s69, s69, s70\n"
      "s_mul_i32 s69, s69, 54\n"
      "s_add_u32 s69, s69, s68\n"
      "s_lshl_b32 s69, s69, 10\n"
      "s_add_u32 s87, s69, 9437184\n"
      "global_load_dwordx2 v[14:15], v18, s[16:17] sc1\n"
      "s_waitcnt lgkmcnt(12)\n"
      "v_mfma_f32_16x16x32_bf16 v[158:161], v[198:201], v[182:185], 0\n"
      "s_waitcnt lgkmcnt(10)\n"
      "v_mfma_f32_16x16x32_bf16 v[158:161], v[202:205], v[186:189], v[158:161]\n"
      "s_waitcnt lgkmcnt(8)\n"
      "v_mfma_f32_16x16x32_bf16 v[158:161], v[206:209], v[190:193], v[158:161]\n"
      "s_waitcnt lgkmcnt(6)\n"
      "v_mfma_f32_16x16x32_bf16 v[158:161], v[210:213], v[194:197], v[158:161]\n"
      "ds_read_b64_tr_b16 v[214:215], v23 offset:32\n"
      "ds_read_b64_tr_b16 v[216:217], v23 offset:304\n"
      "ds_read_b64_tr_b16 v[218:219], v23 offset:4384\n"
      "ds_read_b64_tr_b16 v[220:221], v23 offset:4656\n"
      "ds_read_b64_tr_b16 v[222:223], v23 offset:8736\n"
      "ds_read_b64_tr_b16 v[224:225], v23 offset:9008\n"
      "ds_read_b64_tr_b16 v[226:227], v23 offset:13088\n"
      "ds_read_b64_tr_b16 v[228:229], v23 offset:13360\n"
      "ds_read_b64_tr_b16 v[198:199], v23 offset:64\n"
      "ds_read_b64_tr_b16 v[200:201], v23 offset:336\n"
      "ds_read_b64_tr_b16 v[202:203], v23 offset:4416\n"
      "ds_read_b64_tr_b16 v[204:205], v23 offset:4688\n"
      "ds_read_b64_tr_b16 v[206:207], v23 offset:8768\n"
      "ds_read_b64_tr_b16 v[208:209], v23 offset:9040\n"
      "ds_read_b64_tr_b16 v[210:211], v23 offset:13120\n"
      "ds_read_b64_tr_b16 v[212:213], v23 offset:13392\n"
      "v_add_u32 v154, s65, v24\n"
      "v_add_u32 v155, s65, v25\n"
      "v_add_u32 v157, s65, v2\n"
      "s_mul_i32 s68, s50, -34816\n"
      "s_add_u32 s68, s68, 17408\n"
      "v_add_u32 v156, s68, v28\n"
      "s_waitcnt lgkmcnt(14)\n"
      "v_mfma_f32_16x16x32_bf16 v[162:165], v[214:217], v[182:185], 0\n"
      "s_waitcnt lgkmcnt(12)\n"
      "v_mfma_f32_16x16x32_bf16 v[162:165], v[218:221], v[186:189], v[162:165]\n"
      "s_waitcnt lgkmcnt(10)\n"
      "v_mfma_f32_16x16x32_bf16 v[162:165], v[222:225], v[190:193], v[162:165]\n"
      "s_waitcnt lgkmcnt(8)\n"
      "v_mfma_f32_16x16x32_bf16 v[162:165], v[226:229], v[194:197], v[162:165]\n"
      "s_waitcnt lgkmcnt(6)\n"
      "v_mfma_f32_16x16x32_bf16 v[166:169], v[198:201], v[182:185], 0\n"
      "s_waitcnt lgkmcnt(4)\n"
      "v_mfma_f32_16x16x32_bf16 v[166:169], v[202:205], v[186:189], v[166:169]\n"
      "s_waitcnt lgkmcnt(2)\n"
      "v_mfma_f32_16x16x32_bf16 v[166:169], v[206:209], v[190:193], v[166:169]\n"
      "s_waitcnt lgkmcnt(0)\n"
      "v_mfma_f32_16x16x32_bf16 v[166:169], v[210:213], v[194:197], v[166:169]\n"
      "s_cmp_lg_u32 s50, 0\n"
      "s_cbranch_scc1 .Lkx_26\n"
      "ds_write_b128 v28, v[158:161]\n"
      "ds_write_b128 v28, v[162:165] offset:1024\n"
      "v_mov_b32 v170, 0\n"
      "v_mov_b32 v171, 0\n"
      "v_mov_b32 v172, 0\n"
      "v_mov_b32 v173, 0\n"
      "s_mov_b64 exec, 0x3fffffff\n"
      "ds_write_b128 v157, v[170:173] offset:4096\n"
      "s_mov_b64 exec, -1\n"
      "s_waitcnt lgkmcnt(0)\n"
      "s_barrier\n"
      "ds_read_b128 v[170:173], v156\n"
      "s_waitcnt lgkmcnt(0)\n"
      "v_add_f32 v166, v166, v170\n"
      "v_add_f32 v167, v167, v171\n"
      "v_add_f32 v168, v168, v172\n"
      "v_add_f32 v169, v169, v173\n"
      "v_cvt_pk_bf16_f32 v170, v166, v166\n"
      "v_mov_b32 v171, v170\n"
      "v_cvt_pk_bf16_f32 v172, v167, v167\n"
      "v_mov_b32 v173, v172\n"
      "v_cvt_pk_bf16_f32 v174, v168, v168\n"
      "v_mov_b32 v175, v174\n"
      "v_cvt_pk_bf16_f32 v176, v169, v169\n"
      "v_mov_b32 v177, v176\n"
      "s_mov_b32 exec_hi, 0\n"
      "ds_write_b64 v154, v[170:171] offset:776\n"
      "ds_write_b64 v154, v[172:173] offset:520\n"
      "ds_write_b64 v154, v[174:175] offset:264\n"
      "ds_write_b64 v154, v[176:177] offset:8\n"
      "ds_write_b64 v154, v[172:173] offset:528\n"
      "ds_write_b64 v154, v[174:175] offset:272\n"
      "ds_write_b64 v154, v[176:177] offset:16\n"
      "s_mov_b32 exec_lo, 0xfffffffe\n"
      "s_mov_b32 exec_hi, 1\n"
      "ds_write_b64 v154, v[170:171] offset:784\n"
      "s_mov_b64 exec, 1\n"
      "ds_write_b32 v154, v170 offset:784\n"
      "s_mov_b32 exec_lo, 0x55555555\n"
      "ds_write_b16 v154, v170 offset:774\n"
      "ds_write_b16 v154, v172 offset:518\n"
      "ds_write_b16 v154, v174 offset:262\n"
      "ds_write_b16 v154, v176 offset:6\n"
      "s_mov_b64 exec, -1\n"
      "s_branch .Lkx_27\n"
      ".Lkx_26:\n"
      "s_mov_b64 exec, 1\n"
      "v_add_f32 v158, v158, v181\n"
      "s_mov_b64 exec, -1\n"
      "s_nop 2\n"
      "ds_write_b128 v28, v[166:169]\n"
      "s_waitcnt lgkmcnt(0)\n"
      "s_barrier\n"
      "ds_read_b128 v[174:177], v156 offset:1024\n"
      "ds_read_b128 v[170:173], v156\n"
      "s_waitcnt lgkmcnt(1)\n"
      "v_add_f32 v162, v162, v174\n"
      "v_add_f32 v163, v163, v175\n"
      "v_add_f32 v164, v164, v176\n"
      "v_add_f32 v165, v165, v177\n"
      "s_waitcnt lgkmcnt(0)\n"
      "v_add_f32 v158, v158, v170\n"
      "v_add_f32 v159, v159, v171\n"
      "v_add_f32 v160, v160, v172\n"
      "v_add_f32 v161, v161, v173\n"
      "v_cvt_pk_bf16_f32 v170, v163, v162\n"
      "v_cvt_pk_bf16_f32 v171, v162, v162\n"
      "v_cvt_pk_bf16_f32 v172, v163, v163\n"
      "v_cvt_pk_bf16_f32 v174, v165, v164\n"
      "v_cvt_pk_bf16_f32 v175, v164, v164\n"
      "v_cvt_pk_bf16_f32 v176, v165, v165\n"
      "s_mov_b32 exec_lo, 0\n"
      "ds_write_b32 v154, v171 offset:272\n"
      "ds_write_b64 v154, v[170:171] offset:264\n"
      "ds_write_b32 v154, v172 offset:260\n"
      "ds_write_b32 v154, v175 offset:16\n"
      "ds_write_b64 v154, v[174:175] offset:8\n"
      "ds_write_b32 v154, v176 offset:4\n"
      "s_mov_b32 exec_lo, -1\n"
      "v_cvt_pk_bf16_f32 v178, v164, v163\n"
      "v_mov_b32 v179, v171\n"
      "v_cvt_pk_bf16_f32 v180, v161, v160\n"
      "v_cvt_pk_bf16_f32 v181, v159, v158\n"
      "s_mov_b32 exec_hi, 0\n"
      "ds_write_b16 v154, v171 offset:16\n"
      "ds_write_b64 v154, v[178:179] offset:8\n"
      "ds_write_b32 v154, v175 offset:4\n"
      "ds_write_b16 v154, v176 offset:2\n"
      "s_mov_b64 exec, -1\n"
      "ds_write_b64 v155, v[180:181]\n"
      ".Lkx_27:\n"
      "s_waitcnt lgkmcnt(0)\n"
      "s_barrier\n"
      "ds_read_b128 v[232:235], v31 offset:18432\n"
      "ds_read_b128 v[236:239], v31 offset:22528\n"
      "ds_read_b128 v[32:35], v31 offset:26624\n"
      "s_waitcnt lgkmcnt(2)\n"
      "buffer_store_dwordx4 v[232:235], v2, s[16:19], s53 offen sc1\n"
      "s_waitcnt lgkmcnt(1)\n"
      "buffer_store_dwordx4 v[236:239], v3, s[16:19], s53 offen sc1\n"
      "s_add_u32 s98, s53, 8192\n"
      "s_cmp_lg_u32 s43, 0\n"
      "s_cselect_b32 s98, 2146435072, s98\n"
      "s_waitcnt lgkmcnt(0)\n"
      "buffer_store_dwordx4 v[32:35], v2, s[16:19], s98 offen sc1\n"
      "s_add_u32 s53, s53, 9216\n"
      "v_add_u32 v6, s84, v2\n"
      "v_add_u32 v7, s85, v2\n"
      "v_add_u32 v8, s86, v2\n"
      "v_add_u32 v9, s87, v2\n"
      "s_waitcnt vmcnt(3)\n"
      "v_cmp_eq_u64 vcc, s[40:41], v[14:15]\n"
      "s_nop 0\n"
      "s_cmp_eq_u64 vcc, -1\n"
      "s_cbranch_scc1 .Lok_28\n"
      "s_mov_b32 s82, 0x20000\n"
      ".Lsp_29:\n"
      "s_sleep 4\n"
      "global_load_dwordx2 v[14:15], v18, s[16:17] sc1\n"
      "s_waitcnt vmcnt(0)\n"
      "v_cmp_eq_u64 vcc, s[40:41], v[14:15]\n"
      "s_nop 0\n"
      "s_cmp_eq_u64 vcc, -1\n"
      "s_cbranch_scc1 .Lok_28\n"
      "s_sub_u32 s82, s82, 1\n"
      "s_cmp_lg_u32 s82, 0\n"
      "s_cbranch_scc1 .Lsp_29\n"
      ".Lok_28:\n"
      "s_add_u32 s62, s60, 0\n"
      "s_sub_u32 s73, s62, 14\n"
      "s_min_u32 s62, s62, s73\n"
      "s_mul_i32 s73, s62, 40960\n"
      "s_add_u32 s73, s73, s64\n"
      "s_cmp_eq_u32 s62, s61\n"
      "s_cselect_b32 s73, 2146435072, s73\n"
      "buffer_load_dwordx4 a[0:3], v2, s[16:19], s73 offen sc1\n"
      "buffer_load_dwordx4 a[4:7], v2, s[16:19], s73 offen offset:1024 sc1\n"
      "buffer_load_dwordx4 a[8:11], v2, s[16:19], s73 offen offset:2048 sc1\n"
      "buffer_load_dwordx4 a[12:15], v2, s[16:19], s73 offen offset:3072 sc1\n"
      "buffer_load_dwordx4 a[16:19], v3, s[16:19], s73 offen sc1\n"
      "s_add_u32 s62, s60, 1\n"
      "s_sub_u32 s73, s62, 14\n"
      "s_min_u32 s62, s62, s73\n"
      "s_mul_i32 s73, s62, 40960\n"
      "s_add_u32 s73, s73, s64\n"
      "s_cmp_eq_u32 s62, s61\n"
      "s_cselect_b32 s73, 2146435072, s73\n"
      "buffer_load_dwordx4 a[20:23], v2, s[16:19], s73 offen sc1\n"
      "buffer_load_dwordx4 a[24:27], v2, s[16:19], s73 offen offset:1024 sc1\n"
      "buffer_load_dwordx4 a[28:31], v2, s[16:19], s73 offen offset:2048 sc1\n"
      "buffer_load_dwordx4 a[32:35], v2, s[16:19], s73 offen offset:3072 sc1\n"
      "buffer_load_dwordx4 a[36:39], v3, s[16:19], s73 offen sc1\n"
      "s_waitcnt vmcnt(10)\n"
      "s_barrier\n"
      "s_cmp_lg_u32 s43, 0\n"
      "s_cbranch_scc1 .Lw0_30\n"
      "s_lshl_b32 s98, s42, 7\n"
      "s_add_u32 s98, s98, 229376\n"
      "v_mov_b32 v14, s98\n"
      "v_mov_b32 v16, s40\n"
      "v_mov_b32 v17, s41\n"
      "global_store_dwordx2 v14, v[16:17], s[16:17] sc1\n"
      "s_nop 0\n"
      ".Lw0_30:\n"
      "s_add_u32 s62, s60, 2\n"
      "s_sub_u32 s73, s62, 14\n"
      "s_min_u32 s62, s62, s73\n"
      "s_mul_i32 s73, s62, 40960\n"
      "s_add_u32 s73, s73, s64\n"
      "s_cmp_eq_u32 s62, s61\n"
      "s_cselect_b32 s73, 2146435072, s73\n"
      "buffer_load_dwordx4 a[40:43], v2, s[16:19], s73 offen sc1\n"
      "buffer_load_dwordx4 a[44:47], v2, s[16:19], s73 offen offset:1024 sc1\n"
      "buffer_load_dwordx4 a[48:51], v2, s[16:19], s73 offen offset:2048 sc1\n"
      "buffer_load_dwordx4 a[52:55], v2, s[16:19], s73 offen offset:3072 sc1\n"
      "buffer_load_dwordx4 a[56:59], v3, s[16:19], s73 offen sc1\n"
      "s_add_u32 s62, s60, 3\n"
      "s_sub_u32 s73, s62, 14\n"
      "s_min_u32 s62, s62, s73\n"
      "s_mul_i32 s73, s62, 40960\n"
      "s_add_u32 s73, s73, s64\n"
      "s_cmp_eq_u32 s62, s61\n"
      "s_cselect_b32 s73, 2146435072, s73\n"
      "buffer_load_dwordx4 a[60:63], v2, s[16:19], s73 offen sc1\n"
      "buffer_load_dwordx4 a[64:67], v2, s[16:19], s73 offen offset:1024 sc1\n"
      "buffer_load_dwordx4 a[68:71], v2, s[16:19], s73 offen offset:2048 sc1\n"
      "buffer_load_dwordx4 a[72:75], v2, s[16:19], s73 offen offset:3072 sc1\n"
      "buffer_load_dwordx4 a[76:79], v3, s[16:19], s73 offen sc1\n"
      "s_add_u32 s62, s60, 4\n"
      "s_sub_u32 s73, s62, 14\n"
      "s_min_u32 s62, s62, s73\n"
      "s_mul_i32 s73, s62, 40960\n"
      "s_add_u32 s73, s73, s64\n"
      "s_cmp_eq_u32 s62, s61\n"
      "s_cselect_b32 s73, 2146435072, s73\n"
      "buffer_load_dwordx4 a[80:83], v2, s[16:19], s73 offen sc1\n"
      "buffer_load_dwordx4 a[84:87], v2, s[16:19], s73 offen offset:1024 sc1\n"
      "buffer_load_dwordx4 a[88:91], v2, s[16:19], s73 offen offset:2048 sc1\n"
      "buffer_load_dwordx4 a[92:95], v2, s[16:19], s73 offen offset:3072 sc1\n"
      "buffer_load_dwordx4 a[96:99], v3, s[16:19], s73 offen sc1\n"
      "s_add_u32 s62, s60, 5\n"
      "s_sub_u32 s73, s62, 14\n"
      "s_min_u32 s62, s62, s73\n"
      "s_mul_i32 s73, s62, 40960\n"
      "s_add_u32 s73, s73, s64\n"
      "s_cmp_eq_u32 s62, s61\n"
      "s_cselect_b32 s73, 2146435072, s73\n"
      "buffer_load_dwordx4 a[100:103], v2, s[16:19], s73 offen sc1\n"
      "buffer_load_dwordx4 a[104:107], v2, s[16:19], s73 offen offset:1024 sc1\n"
      "buffer_load_dwordx4 a[108:111], v2, s[16:19], s73 offen offset:2048 sc1\n"
      "buffer_load_dwordx4 a[112:115], v2, s[16:19], s73 offen offset:3072 sc1\n"
      "buffer_load_dwordx4 a[116:119], v3, s[16:19], s73 offen sc1\n"
      "s_add_u32 s62, s60, 6\n"
      "s_sub_u32 s73, s62, 14\n"
      "s_min_u32 s62, s62, s73\n"
      "s_mul_i32 s73, s62, 40960\n"
      "s_add_u32 s73, s73, s64\n"
      "s_cmp_eq_u32 s62, s61\n"
      "s_cselect_b32 s73, 2146435072, s73\n"
      "buffer_load_dwordx4 a[120:123], v2, s[16:19], s73 offen sc1\n"
      "buffer_load_dwordx4 a[124:127], v2, s[16:19], s73 offen offset:1024 sc1\n"
      "buffer_load_dwordx4 a[128:131], v2, s[16:19], s73 offen offset:2048 sc1\n"
      "buffer_load_dwordx4 a[132:135], v2, s[16:19], s73 offen offset:3072 sc1\n"
      "buffer_load_dwordx4 a[136:139], v3, s[16:19], s73 offen sc1\n"
      "s_add_u32 s62, s60, 7\n"
      "s_sub_u32 s73, s62, 14\n"
      "s_min_u32 s62, s62, s73\n"
      "s_mul_i32 s73, s62, 40960\n"
      "s_add_u32 s73, s73, s64\n"
      "s_cmp_eq_u32 s62, s61\n"
      "s_cselect_b32 s73, 2146435072, s73\n"
      "buffer_load_dwordx4 a[140:143], v2, s[16:19], s73 offen sc1\n"
      "buffer_load_dwordx4 a[144:147], v2, s[16:19], s73 offen offset:1024 sc1\n"
      "buffer_load_dwordx4 a[148:151], v2, s[16:19], s73 offen offset:2048 sc1\n"
      "buffer_load_dwordx4 a[152:155], v2, s[16:19], s73 offen offset:3072 sc1\n"
      "buffer_load_dwordx4 a[156:159], v3, s[16:19], s73 offen sc1\n"
      "s_add_u32 s62, s60, 8\n"
      "s_sub_u32 s73, s62, 14\n"
      "s_min_u32 s62, s62, s73\n"
      "s_mul_i32 s73, s62, 40960\n"
      "s_add_u32 s73, s73, s64\n"
      "s_cmp_eq_u32 s62, s61\n"
      "s_cselect_b32 s73, 2146435072, s73\n"
      "buffer_load_dwordx4 a[160:163], v2, s[16:19], s73 offen sc1\n"
      "buffer_load_dwordx4 a[164:167], v2, s[16:19], s73 offen offset:1024 sc1\n"
      "buffer_load_dwordx4 a[168:171], v2, s[16:19], s73 offen offset:2048 sc1\n"
      "buffer_load_dwordx4 a[172:175], v2, s[16:19], s73 offen offset:3072 sc1\n"
      "buffer_load_dwordx4 a[176:179], v3, s[16:19], s73 offen sc1\n"
      "s_add_u32 s62, s60, 9\n"
      "s_sub_u32 s73, s62, 14\n"
      "s_min_u32 s62, s62, s73\n"
      "s_mul_i32 s73, s62, 40960\n"
      "s_add_u32 s73, s73, s64\n"
      "s_cmp_eq_u32 s62, s61\n"
      "s_cselect_b32 s73, 2146435072, s73\n"
      "buffer_load_dwordx4 a[180:183], v2, s[16:19], s73 offen sc1\n"
      "buffer_load_dwordx4 a[184:187], v2, s[16:19], s73 offen offset:1024 sc1\n"
      "buffer_load_dwordx4 a[188:191], v2, s[16:19], s73 offen offset:2048 sc1\n"
      "buffer_load_dwordx4 a[192:195], v2, s[16:19], s73 offen offset:3072 sc1\n"
      "buffer_load_dwordx4 a[196:199], v3, s[16:19], s73 offen sc1\n"
      "s_add_u32 s62, s60, 10\n"
      "s_sub_u32 s73, s62, 14\n"
      "s_min_u32 s62, s62, s73\n"
      "s_mul_i32 s73, s62, 40960\n"
      "s_add_u32 s73, s73, s64\n"
      "s_cmp_eq_u32 s62, s61\n"
      "s_cselect_b32 s73, 2146435072, s73\n"
      "buffer_load_dwordx4 a[200:203], v2, s[16:19], s73 offen sc1\n"
      "buffer_load_dwordx4 a[204:207], v2, s[16:19], s73 offen offset:1024 sc1\n"
      "buffer_load_dwordx4 a[208:211], v2, s[16:19], s73 offen offset:2048 sc1\n"
      "buffer_load_dwordx4 a[212:215], v2, s[16:19], s73 offen offset:3072 sc1\n"
      "buffer_load_dwordx4 a[216:219], v3, s[16:19], s73 offen sc1\n"
      "s_add_u32 s62, s60, 11\n"
      "s_sub_u32 s73, s62, 14\n"
      "s_min_u32 s62, s62, s73\n"
      "s_mul_i32 s73, s62, 40960\n"
      "s_add_u32 s73, s73, s64\n"
      "s_cmp_eq_u32 s62, s61\n"
      "s_cselect_b32 s73, 2146435072, s73\n"
      "buffer_load_dwordx4 a[220:223], v2, s[16:19], s73 offen sc1\n"
      "buffer_load_dwordx4 a[224:227], v2, s[16:19], s73 offen offset:1024 sc1\n"
      "buffer_load_dwordx4 a[228:231], v2, s[16:19], s73 offen offset:2048 sc1\n"
      "buffer_load_dwordx4 a[232:235], v2, s[16:19], s73 offen offset:3072 sc1\n"
      "buffer_load_dwordx4 a[236:239], v3, s[16:19], s73 offen sc1\n"
      "s_add_u32 s62, s60, 12\n"
      "s_sub_u32 s73, s62, 14\n"
      "s_min_u32 s62, s62, s73\n"
      "s_mul_i32 s73, s62, 40960\n"
      "s_add_u32 s73, s73, s64\n"
      "s_cmp_eq_u32 s62, s61\n"
      "s_cselect_b32 s73, 2146435072, s73\n"
      "buffer_load_dwordx4 a[240:243], v2, s[16:19], s73 offen sc1\n"
      "buffer_load_dwordx4 a[244:247], v2, s[16:19], s73 offen offset:1024 sc1\n"
      "buffer_load_dwordx4 a[248:251], v2, s[16:19], s73 offen offset:2048 sc1\n"
      "buffer_load_dwordx4 a[252:255], v2, s[16:19], s73 offen offset:3072 sc1\n"
      "buffer_load_dwordx4 v[232:235], v3, s[16:19], s73 offen sc1\n"
      "s_add_u32 s62, s60, 13\n"
      "s_sub_u32 s73, s62, 14\n"
      "s_min_u32 s62, s62, s73\n"
      "s_mul_i32 s73, s62, 40960\n"
      "s_add_u32 s73, s73, s64\n"
      "s_cmp_eq_u32 s62, s61\n"
      "s_cselect_b32 s73, 2146435072, s73\n"
      "buffer_load_dwordx4 v[236:239], v2, s[16:19], s73 offen sc1\n"
      "buffer_load_dwordx4 v[240:243], v2, s[16:19], s73 offen offset:1024 sc1\n"
      "buffer_load_dwordx4 v[244:247], v2, s[16:19], s73 offen offset:2048 sc1\n"
      "buffer_load_dwordx4 v[248:251], v2, s[16:19], s73 offen offset:3072 sc1\n"
      "buffer_load_dwordx4 v[252:255], v3, s[16:19], s73 offen sc1\n"
      "global_load_dwordx2 v[12:13], v20, s[16:17] sc1\n"
      "s_waitcnt vmcnt(0)\n"
      "v_cmp_eq_u64 vcc, s[40:41], v[12:13]\n"
      "s_nop 0\n"
      "s_cmp_eq_u64 vcc, -1\n"
      "s_cbranch_scc1 .Lok_31\n"
      "s_mov_b32 s82, 0x20000\n"
      ".Lsp_32:\n"
      "s_sleep 8\n"
      "global_load_dwordx2 v[12:13], v20, s[16:17] sc1\n"
      "s_waitcnt vmcnt(0)\n"
      "v_cmp_eq_u64 vcc, s[40:41], v[12:13]\n"
      "s_nop 0\n"
      "s_cmp_eq_u64 vcc, -1\n"
      "s_cbranch_scc1 .Lok_31\n"
      "s_sub_u32 s82, s82, 1\n"
      "s_cmp_lg_u32 s82, 0\n"
      "s_cbranch_scc1 .Lsp_32\n"
      ".Lok_31:\n"
      "s_mul_i32 s69, s47, 160\n"
      "v_add_u32 v43, s69, v2\n"
      "v_min_u32 v43, 1264, v43\n"
      "s_lshl_b32 s71, s43, 10\n"
      "s_add_u32 m0, s71, 61440\n"
      "s_nop 0\n"
      "global_load_lds_dwordx4 v43, s[34:35]\n"
      "s_add_u32 s71, s71, 61440\n"
      "v_lshl_add_u32 v34, v5, 3, s71\n"
      "s_lshr_b32 s69, s46, 1\n"
      "s_mul_i32 s69, s69, 5120\n"
      "s_and_b32 s71, s46, 1\n"
      "s_lshl_b32 s71, s71, 8\n"
      "s_add_u32 s69, s69, s71\n"
      "s_lshl_b32 s71, s43, 6\n"
      "s_add_u32 s69, s69, s71\n"
      "s_add_u32 s70, s69, 131072\n"
      "v_lshlrev_b32 v43, 2, v4\n"
      "v_lshl_add_u32 v43, v5, 9, v43\n"
      "v_and_b32 v35, 2, v5\n"
      "v_lshl_add_u32 v35, v35, 29, v43\n"
      "buffer_load_dword v22, v43, s[16:19], s70 offen sc1\n"
      "buffer_load_dword v23, v43, s[16:19], s70 offen offset:2048 sc1\n"
      "s_add_u32 s70, s70, 4096\n"
      "buffer_load_dword v35, v35, s[16:19], s70 offen sc1\n"
      "s_add_u32 s92, s60, 0\n"
      "s_sub_u32 s93, s92, 14\n"
      "s_min_u32 s92, s92, s93\n"
      "s_lshl_b32 s93, s92, 10\n"
      "s_cmp_eq_u32 s92, s61\n"
      "s_cselect_b32 s93, 2146435072, s93\n"
      "buffer_load_dwordx4 v[144:147], v6, s[16:19], s93 offen sc1\n"
      "buffer_load_dwordx4 v[148:151], v7, s[16:19], s93 offen sc1\n"
      "buffer_load_dwordx4 v[152:155], v8, s[16:19], s93 offen sc1\n"
      "buffer_load_dwordx4 v[156:159], v9, s[16:19], s93 offen sc1\n"
      "s_add_u32 s92, s60, 1\n"
      "s_sub_u32 s93, s92, 14\n"
      "s_min_u32 s92, s92, s93\n"
      "s_lshl_b32 s93, s92, 10\n"
      "s_cmp_eq_u32 s92, s61\n"
      "s_cselect_b32 s93, 2146435072, s93\n"
      "buffer_load_dwordx4 v[160:163], v6, s[16:19], s93 offen sc1\n"
      "buffer_load_dwordx4 v[164:167], v7, s[16:19], s93 offen sc1\n"
      "buffer_load_dwordx4 v[168:171], v8, s[16:19], s93 offen sc1\n"
      "buffer_load_dwordx4 v[172:175], v9, s[16:19], s93 offen sc1\n"
      "s_add_u32 s92, s60, 2\n"
      "s_sub_u32 s93, s92, 14\n"
      "s_min_u32 s92, s92, s93\n"
      "s_lshl_b32 s93, s92, 10\n"
      "s_cmp_eq_u32 s92, s61\n"
      "s_cselect_b32 s93, 2146435072, s93\n"
      "buffer_load_dwordx4 v[176:179], v6, s[16:19], s93 offen sc1\n"
      "buffer_load_dwordx4 v[180:183], v7, s[16:19], s93 offen sc1\n"
      "buffer_load_dwordx4 v[184:187], v8, s[16:19], s93 offen sc1\n"
      "buffer_load_dwordx4 v[188:191], v9, s[16:19], s93 offen sc1\n"
      "s_mov_b32 s83, 0\n"
      "s_branch .Lgemm2\n"
      ".Lcode_end:\n"
      "s_endpgm\n"
      :
      : "s"(__builtin_amdgcn_kernarg_segment_ptr()), "s"(blockIdx.x), "v"(threadIdx.x)
      : "v1", "v2", "v3", "v4", "v5", "v6", "v7", "v8", "v9", "v10", "v11", "v12", "v13", "v14", "v15", "v16", "v17", "v18", "v19", "v20", "v21", "v22", "v23", "v24", "v25", "v26", "v27", "v28", "v29", "v30", "v31", "v32", "v33", "v34", "v35", "v36", "v37", "v38", "v39", "v40", "v41", "v42", "v43", "v44", "v45", "v46", "v47", "v48", "v49", "v50", "v51", "v52", "v53", "v54", "v55", "v56", "v57", "v58", "v59", "v60", "v61", "v62", "v63", "v64", "v65", "v66", "v67", "v68", "v69", "v70", "v71", "v72", "v73", "v74", "v75", "v76", "v77", "v78", "v79", "v80", "v81", "v82", "v83", "v84", "v85", "v86", "v87", "v88", "v89", "v90", "v91", "v92", "v93", "v94", "v95", "v96", "v97", "v98", "v99", "v100", "v101", "v102", "v103", "v104", "v105", "v106", "v107", "v108", "v109", "v110", "v111", "v112", "v113", "v114", "v115", "v116", "v117", "v118", "v119", "v120", "v121", "v122", "v123", "v124", "v125", "v126", "v127", "v128", "v129", "v130", "v131", "v132", "v133", "v134", "v135", "v136", "v137", "v138", "v139", "v140", "v141", "v142", "v143", "v144", "v145", "v146", "v147", "v148", "v149", "v150", "v151", "v152", "v153", "v154", "v155", "v156", "v157", "v158", "v159", "v160", "v161", "v162", "v163", "v164", "v165", "v166", "v167", "v168", "v169", "v170", "v171", "v172", "v173", "v174", "v175", "v176", "v177", "v178", "v179", "v180", "v181", "v182", "v183", "v184", "v185", "v186", "v187", "v188", "v189", "v190", "v191", "v192", "v193", "v194", "v195", "v196", "v197", "v198", "v199", "v200", "v201", "v202", "v203", "v204", "v205", "v206", "v207", "v208", "v209", "v210", "v211", "v212", "v213", "v214", "v215", "v216", "v217", "v218", "v219", "v220", "v221", "v222", "v223", "v224", "v225", "v226", "v227", "v228", "v229", "v230", "v231", "v232", "v233", "v234", "v235", "v236", "v237", "v238", "v239", "v240", "v241", "v242", "v243", "v244", "v245", "v246", "v247", "v248", "v249", "v250", "v251", "v252", "v253", "v254", "v255",
        "a0", "a1", "a2", "a3", "a4", "a5", "a6", "a7", "a8", "a9", "a10", "a11", "a12", "a13", "a14", "a15", "a16", "a17", "a18", "a19", "a20", "a21", "a22", "a23", "a24", "a25", "a26", "a27", "a28", "a29", "a30", "a31", "a32", "a33", "a34", "a35", "a36", "a37", "a38", "a39", "a40", "a41", "a42", "a43", "a44", "a45", "a46", "a47", "a48", "a49", "a50", "a51", "a52", "a53", "a54", "a55", "a56", "a57", "a58", "a59", "a60", "a61", "a62", "a63", "a64", "a65", "a66", "a67", "a68", "a69", "a70", "a71", "a72", "a73", "a74", "a75", "a76", "a77", "a78", "a79", "a80", "a81", "a82", "a83", "a84", "a85", "a86", "a87", "a88", "a89", "a90", "a91", "a92", "a93", "a94", "a95", "a96", "a97", "a98", "a99", "a100", "a101", "a102", "a103", "a104", "a105", "a106", "a107", "a108", "a109", "a110", "a111", "a112", "a113", "a114", "a115", "a116", "a117", "a118", "a119", "a120", "a121", "a122", "a123", "a124", "a125", "a126", "a127", "a128", "a129", "a130", "a131", "a132", "a133", "a134", "a135", "a136", "a137", "a138", "a139", "a140", "a141", "a142", "a143", "a144", "a145", "a146", "a147", "a148", "a149", "a150", "a151", "a152", "a153", "a154", "a155", "a156", "a157", "a158", "a159", "a160", "a161", "a162", "a163", "a164", "a165", "a166", "a167", "a168", "a169", "a170", "a171", "a172", "a173", "a174", "a175", "a176", "a177", "a178", "a179", "a180", "a181", "a182", "a183", "a184", "a185", "a186", "a187", "a188", "a189", "a190", "a191", "a192", "a193", "a194", "a195", "a196", "a197", "a198", "a199", "a200", "a201", "a202", "a203", "a204", "a205", "a206", "a207", "a208", "a209", "a210", "a211", "a212", "a213", "a214", "a215", "a216", "a217", "a218", "a219", "a220", "a221", "a222", "a223", "a224", "a225", "a226", "a227", "a228", "a229", "a230", "a231", "a232", "a233", "a234", "a235", "a236", "a237", "a238", "a239", "a240", "a241", "a242", "a243", "a244", "a245", "a246", "a247", "a248", "a249", "a250", "a251", "a252", "a253", "a254", "a255",
        "s4", "s5", "s6", "s7", "s8", "s9", "s10", "s11", "s12", "s13", "s14", "s15", "s16", "s17", "s18", "s19", "s20", "s21", "s22", "s23", "s24", "s25", "s26", "s27", "s28", "s29", "s30", "s31", "s32", "s33", "s34", "s35", "s36", "s37", "s38", "s39", "s40", "s41", "s42", "s43", "s44", "s45", "s46", "s47", "s48", "s49", "s50", "s51", "s52", "s53", "s54", "s55", "s56", "s57", "s58", "s59", "s60", "s61", "s62", "s63", "s64", "s65", "s66", "s67", "s68", "s69", "s70", "s71", "s72", "s73", "s74", "s75", "s76", "s77", "s78", "s79", "s80", "s81", "s82", "s83", "s84", "s85", "s86", "s87", "s88", "s89", "s90", "s91", "s92", "s93", "s94", "s95", "s96", "s97", "s98", "s99", "s100", "s101", "vcc", "scc", "memory");
}

extern "C" void kernel_launch(void* const* d_in, const int* in_sizes, int n_in, void* d_out, int out_size, void* d_ws, size_t ws_size,
                              hipStream_t stream) {
  Params p;
  p.x = Srd{d_in[0], 2048u * 640u * 2u, 0x00020000u};
  p.w1 = Srd{d_in[8], 640u * 1728u * 2u, 0x00020000u};
  p.w2 = Srd{d_in[10], 1728u * 640u * 2u, 0x00020000u};
  p.ws = Srd{d_ws, 17825792u, 0x00020000u};
  p.lar = d_in[1]; p.aim = d_in[2]; p.cr = d_in[3]; p.ci = d_in[4]; p.ldt = d_in[5]; p.dsk = d_in[6];
  p.b1 = d_in[9]; p.b2 = d_in[11]; p.gw = d_in[12]; p.out = d_out;
  hipFuncSetAttribute((const void*)fused, hipFuncAttributeMaxDynamicSharedMemorySize, 162880);
  fused<<<256, 256, 162880, stream>>>(p);
}
```
